```python
import torch

def _softmax(x, axis=-1):
    return torch.softmax(x, dim=axis)
def _gelu(x):
    return torch.nn.functional.gelu(x, approximate="tanh")
def _silu(x):
    return torch.nn.functional.silu(x)
def _sigmoid(x):
    return torch.sigmoid(x)
def _layernorm(x, axis=-1, eps=1e-5):
    m = x.mean(dim=axis, keepdim=True)
    v = x.var(dim=axis, keepdim=True, unbiased=False)
    return (x - m) / torch.sqrt(v + eps)
def _scatter_add(tgt, idx, src, axis=0):
    return tgt.clone().index_add_(axis, idx, src)
def _segment_sum(src, seg, n):
    out = torch.zeros((n,) + src.shape[1:], dtype=src.dtype)
    return out.index_add_(0, seg, src)
def _pad(x, pw):
    flat = tuple(v for pair in reversed(pw) for v in pair)
    return torch.nn.functional.pad(x, flat)

kernel_name = 'concat_heads__mha_block_cached__gqa_block_cached__near_ridge'

def setup_inputs():

    _w = (lambda a: (lambda t: t.to(torch.bfloat16) if t.is_floating_point() else t)(torch.as_tensor(a)))
    def _A_setup():
        B, Sq, D = 8, 224, 448
        H, Sk, Dh = 16, 512, 256
        return {
            "x": _w((lambda *s: torch.randn(*s, dtype=torch.bfloat16) * 0.8)(B, Sq, D)),
            "k_cache": _w((lambda *s: torch.randn(*s, dtype=torch.bfloat16) * 0.8)(B, H, Sk, Dh)),
            "v_cache": _w((lambda *s: torch.randn(*s, dtype=torch.bfloat16) * 0.05)(B, H, Sk, Dh)),
            "wq": _w((lambda *s: torch.randn(*s, dtype=torch.bfloat16) * 0.8)(D, H * Dh) * (D ** -0.5)),
            "wo": _w((lambda *s: torch.randn(*s, dtype=torch.bfloat16) * 0.05)(H * Dh, D) * ((H * Dh) ** -0.5)),
        }
    _A_in = _A_setup()

    def _B_setup():
        B, Sq, D = 8, 224, 448
        Hq, Hkv, Sk, Dh = 16, 16, 512, 256
        assert Hq % Hkv == 0, (Hq, Hkv)
        return {
            "x": _w((lambda *s: torch.randn(*s, dtype=torch.bfloat16) * 0.8)(B, Sq, D)),
            "k_cache": _w((lambda *s: torch.randn(*s, dtype=torch.bfloat16) * 0.8)(B, Hkv, Sk, Dh)),
            "v_cache": _w((lambda *s: torch.randn(*s, dtype=torch.bfloat16) * 0.05)(B, Hkv, Sk, Dh)),
            "wq": _w((lambda *s: torch.randn(*s, dtype=torch.bfloat16) * 0.8)(D, Hq * Dh) * (D ** -0.5)),
            "wo": _w((lambda *s: torch.randn(*s, dtype=torch.bfloat16) * 0.05)(Hq * Dh, D) * ((Hq * Dh) ** -0.5)),
        }
    _B_in = _B_setup()

    return {
        "A_x": _A_in["x"],
        "A_k_cache": _A_in["k_cache"],
        "A_v_cache": _A_in["v_cache"],
        "A_wq": _A_in["wq"],
        "A_wo": _A_in["wo"],
        "B_x": _B_in["x"],
        "B_k_cache": _B_in["k_cache"],
        "B_v_cache": _B_in["v_cache"],
        "B_wq": _B_in["wq"],
        "B_wo": _B_in["wo"],
    }

def reference(A_x, A_k_cache, A_v_cache, A_wq, A_wo, B_x, B_k_cache, B_v_cache, B_wq, B_wo):
    def _A_ref(x, k_cache, v_cache, wq, wo):
        B, Sq, D = x.shape
        H, Dh = 16, 256
        q = (lambda x, *d: x.permute(*d))((x @ wq).reshape(B, Sq, H, Dh), 0, 2, 1, 3)
        scores = torch.einsum("bhqd,bhkd->bhqk", q, k_cache) * (Dh ** -0.5)
        p = _softmax(scores, axis=-1)
        o = torch.einsum("bhqk,bhkd->bhqd", p, v_cache)
        return (lambda x, *d: x.permute(*d))(o, 0, 2, 1, 3).reshape(B, Sq, H * Dh) @ wo

    def _B_ref(x, k_cache, v_cache, wq, wo):
        B, Sq, D = x.shape
        Hq, Dh = 16, 256
        Hkv = k_cache.shape[1]
        rep = Hq // Hkv
        q = (lambda x, *d: x.permute(*d))((x @ wq).reshape(B, Sq, Hq, Dh), 0, 2, 1, 3)
        k = (lambda x, n, axis: torch.repeat_interleave(x, n, dim=axis))(k_cache, rep, axis=1)
        v = (lambda x, n, axis: torch.repeat_interleave(x, n, dim=axis))(v_cache, rep, axis=1)
        scores = torch.einsum("bhqd,bhkd->bhqk", q, k) * (Dh ** -0.5)
        p = _softmax(scores, axis=-1)
        o = torch.einsum("bhqk,bhkd->bhqd", p, v)
        return (lambda x, *d: x.permute(*d))(o, 0, 2, 1, 3).reshape(B, Sq, Hq * Dh) @ wo

    a = _A_ref(A_x, A_k_cache, A_v_cache, A_wq, A_wo)
    b = _B_ref(B_x, B_k_cache, B_v_cache, B_wq, B_wo)
    return torch.concatenate([a, b], axis=-1)
```

```cpp
#include <hip/hip_runtime.h>
#include <hip/hip_bf16.h>
typedef unsigned short u16;
__device__ __forceinline__ float bf2f(u16 v){ return __uint_as_float(((unsigned)v)<<16); }
__device__ __forceinline__ u16 f2bf(float f){ unsigned u=__float_as_uint(f); u += 0x7fff + ((u>>16)&1); return (u16)(u>>16); }
constexpr int B=8,SQ=224,D=448,H=16,SK=512,DH=256;
__global__ void gemm_naive(const u16* A,const u16* W,u16* C,int M,int N,int K,int ldc){
  __shared__ float As[16][17], Ws[16][17];
  int tx=threadIdx.x&15, ty=threadIdx.x>>4;
  int row=blockIdx.y*16+ty, col=blockIdx.x*16+tx;
  float acc=0;
  for(int k0=0;k0<K;k0+=16){
    As[ty][tx]=bf2f(A[(size_t)row*K+k0+tx]);
    Ws[ty][tx]=bf2f(W[(size_t)(k0+ty)*N+col]);
    __syncthreads();
    for(int k=0;k<16;k++) acc+=As[ty][k]*Ws[k][tx];
    __syncthreads();
  }
  C[(size_t)row*ldc+col]=f2bf(acc);
}
__global__ void attn_naive(const u16* Q,const u16* Kc,const u16* Vc,u16* O){
  __shared__ float q[DH], p[SK], red[256];
  int qi=blockIdx.x, h=blockIdx.y, b=blockIdx.z, t=threadIdx.x;
  q[t]=bf2f(Q[(size_t)(b*SQ+qi)*(H*DH)+h*DH+t]);
  __syncthreads();
  const u16* Kb=Kc+(size_t)(b*H+h)*SK*DH; const u16* Vb=Vc+(size_t)(b*H+h)*SK*DH;
  float s[2];
  for(int j=0;j<2;j++){ int k=t+256*j; float a=0; for(int d=0;d<DH;d++) a+=q[d]*bf2f(Kb[(size_t)k*DH+d]); s[j]=a*0.0625f; }
  red[t]=fmaxf(s[0],s[1]); __syncthreads();
  for(int o=128;o>0;o>>=1){ if(t<o) red[t]=fmaxf(red[t],red[t+o]); __syncthreads(); }
  float m=red[0]; __syncthreads();
  float e0=__expf(s[0]-m), e1=__expf(s[1]-m);
  red[t]=e0+e1; __syncthreads();
  for(int o=128;o>0;o>>=1){ if(t<o) red[t]+=red[t+o]; __syncthreads(); }
  float l=red[0];
  p[t]=e0/l; p[t+256]=e1/l; __syncthreads();
  float a=0; for(int k=0;k<SK;k++) a+=p[k]*bf2f(Vb[(size_t)k*DH+t]);
  O[(size_t)(b*SQ+qi)*(H*DH)+h*DH+t]=f2bf(a);
}
extern "C" void kernel_launch(void* const* d_in, const int* in_sizes, int n_in, void* d_out, int out_size, void* d_ws, size_t ws_size, hipStream_t stream){
  u16* ws=(u16*)d_ws;
  size_t n=(size_t)B*SQ*H*DH;
  for(int br=0;br<2;br++){
    const u16* x=(const u16*)d_in[br*5+0]; const u16* k=(const u16*)d_in[br*5+1]; const u16* v=(const u16*)d_in[br*5+2];
    const u16* wq=(const u16*)d_in[br*5+3]; const u16* wo=(const u16*)d_in[br*5+4];
    u16* q=ws+(size_t)br*2*n; u16* o=q+n;
    gemm_naive<<<dim3(H*DH/16,B*SQ/16),256,0,stream>>>(x,wq,q,B*SQ,H*DH,D,H*DH);
    attn_naive<<<dim3(SQ,H,B),256,0,stream>>>(q,k,v,o);
    gemm_naive<<<dim3(D/16,B*SQ/16),256,0,stream>>>(o,wo,(u16*)d_out+br*D,B*SQ,D,H*DH,2*D);
  }
}
```

```cpp
#include <hip/hip_runtime.h>
struct Ptrs { const void* p[2][8]; };
__global__ __launch_bounds__(256, 1) __attribute__((aligned(4096))) void fused_attn(Ptrs p) {
  __shared__ __attribute__((aligned(1024))) char lds[163840];
  asm volatile(
"s_memrealtime s[98:99]\n"
"s_memtime vcc\n"
"s_bfe_u32 s23, %1, 0x10002\n"
"s_lshl_b32 s24, s23, 6\n"
"s_load_dwordx16 s[4:19], %0, s24\n"
"v_lshrrev_b32 v30, 6, v0\n"
"v_and_b32 v1, 63, v0\n"
"v_readfirstlane_b32 s60, v30\n"
"s_getpc_b64 s[56:57]\n"
"CODE0%=:\n"
"s_and_b32 s57, s57, 0xffff\n"
"s_mov_b32 s58, CODEEND%=-CODE0%=\n"
"s_mov_b32 s59, 0x20000\n"
"s_bfe_u32 s90, %1, 0x30003\n"
"s_lshl_b32 s90, s90, 13\n"
"v_lshrrev_b32 v2, 2, v0\n"
"v_lshlrev_b32 v2, 7, v2\n"
"buffer_load_dword v255, v2, s[56:59], s90 offen\n"
"s_mov_b32 s34, %3\n"
"s_mov_b32 s86, 0x2d000000\n"
"s_mov_b32 s87, 0x4f800000\n"
"s_mov_b32 s92, 0\n"
"s_lshr_b32 s21, %1, 3\n"
"s_and_b32 s20, %1, 3\n"
"s_lshl_b32 s20, s20, 2\n"
"s_and_b32 s24, s21, 3\n"
"s_or_b32 s20, s20, s24\n"
"s_lshr_b32 s22, s21, 2\n"
"s_lshl_b32 s73, s20, 9\n"
"s_mul_i32 s74, s22, 200704\n"
"s_lshl_b32 s75, s22, 4\n"
"s_or_b32 s75, s75, s20\n"
"s_lshl_b32 s75, s75, 18\n"
"s_and_b32 s79, s20, 3\n"
"s_mul_i32 s79, s79, 224\n"
"s_lshr_b32 s76, s20, 2\n"
"s_mul_i32 s76, s76, 917504\n"
"s_add_u32 s76, s76, s79\n"
"s_mul_i32 s77, s22, 401408\n"
"s_add_u32 s77, s77, s79\n"
"s_mul_i32 s79, s23, 896\n"
"s_add_u32 s77, s77, s79\n"
"s_lshl_b32 s78, s23, 3\n"
"s_or_b32 s78, s78, s22\n"
"s_mul_i32 s78, s78, 3080192\n"
"RESTART%=:\n"
"v_lshrrev_b32 v30, 3, v1\n"
"v_and_b32 v10, 7, v1\n"
"v_lshlrev_b32 v7, 1, v30\n"
"v_and_b32 v7, 4, v7\n"
"v_xor_b32 v7, v7, v10\n"
"v_lshlrev_b32 v7, 4, v7\n"
"v_lshl_add_u32 v6, v30, 13, v7\n"
"v_add_u32 v7, 64504, v6\n"
"v_add_u32 v8, 129008, v6\n"
"v_add_u32 v9, 193512, v6\n"
"s_and_b32 s61, s60, 1\n"
"s_lshr_b32 s35, s60, 1\n"
"s_mul_i32 s85, s35, 74304\n"
"s_add_u32 s85, s85, s34\n"
"s_mul_i32 s62, s61, 4128\n"
"s_add_u32 s62, s62, s85\n"
"s_lshl_b32 s63, s61, 12\n"
"s_add_u32 s63, s63, s85\n"
"s_lshl_b32 s85, s61, 18\n"
"s_and_b32 s24, s20, 3\n"
"s_add_u32 s24, s24, s22\n"
"s_lshl_b32 s24, s24, 1\n"
"s_lshr_b32 s24, 1198372, s24\n"
"s_and_b32 s24, s24, 3\n"
"s_mul_i32 s24, s24, 5\n"
"s_lshr_b32 s24, s24, 1\n"
"s_lshl_b32 s24, s24, 7\n"
"s_lshl_b32 s82, s35, 7\n"
"s_add_u32 s82, s82, s85\n"
"s_lshl_b32 s66, s24, 12\n"
"s_add_u32 s66, s66, s82\n"
"s_cmp_eq_u32 s86, 0\n"
"s_cbranch_scc1 NOADD%=\n"
"s_waitcnt lgkmcnt(0)\n"
"s_add_u32 s10, s10, s73\n"
"s_addc_u32 s11, s11, 0\n"
"s_add_u32 s4, s4, s74\n"
"s_addc_u32 s5, s5, 0\n"
"s_add_u32 s6, s6, s75\n"
"s_addc_u32 s7, s7, 0\n"
"s_add_u32 s8, s8, s75\n"
"s_addc_u32 s9, s9, 0\n"
"s_add_u32 s12, s12, s76\n"
"s_addc_u32 s13, s13, 0\n"
"s_add_u32 s14, s14, s77\n"
"s_addc_u32 s15, s15, 0\n"
"s_add_u32 s16, s16, s78\n"
"s_addc_u32 s17, s17, 0\n"
"NOADD%=:\n"
"s_mov_b64 s[40:41], s[10:11]\n"
"s_and_b32 s41, s41, 0xffff\n"
"s_mov_b32 s42, 3662336\n"
"s_mov_b32 s43, 0x20000\n"
"s_add_u32 m0, s62, 0\n"
"s_add_u32 s93, s66, 0\n"
"buffer_load_dwordx4 v6, s[40:43], s93 offen offset:0 lds\n"
"s_mov_b64 s[36:37], s[4:5]\n"
"s_and_b32 s37, s37, 0xffff\n"
"s_mov_b32 s38, 200704\n"
"s_mov_b32 s39, 0x20000\n"
"v_lshrrev_b32 v26, 4, v1\n"
"buffer_load_dwordx4 v7, s[40:43], s93 offen offset:1032 lds\n"
"v_xor_b32 v26, v26, v10\n"
"v_lshlrev_b32 v26, 4, v26\n"
"v_mul_u32_u24 v30, 896, v30\n"
"v_add_u32 v25, v30, v26\n"
"v_xor_b32 v26, 64, v25\n"
"buffer_load_dwordx4 v8, s[40:43], s93 offen offset:2064 lds\n"
"v_add_u32 v26, 6144, v26\n"
"s_mul_i32 s69, s60, 43008\n"
"s_add_u32 s70, s69, 14336\n"
"s_add_u32 s71, s69, 28672\n"
"s_mul_i32 s72, s35, 14336\n"
"buffer_load_dwordx4 v9, s[40:43], s93 offen offset:3096 lds\n"
"s_add_u32 s72, s72, 172032\n"
"s_add_u32 s25, s24, 128\n"
"s_cmp_eq_u32 s25, 896\n"
"s_cselect_b32 s25, 0, s25\n"
"s_lshl_b32 s67, s25, 12\n"
"s_add_u32 s67, s67, s82\n"
"s_add_u32 m0, s62, 24768\n"
"s_add_u32 s93, s69, s24\n"
"buffer_load_dwordx4 v25, s[36:39], s93 offen offset:0 lds\n"
"s_add_u32 s26, s25, 128\n"
"s_cmp_eq_u32 s26, 896\n"
"s_cselect_b32 s26, 0, s26\n"
"s_lshl_b32 s68, s26, 12\n"
"s_add_u32 s68, s68, s82\n"
"s_add_u32 s27, s26, 128\n"
"s_cmp_eq_u32 s27, 896\n"
"s_cselect_b32 s27, 0, s27\n"
"s_lshl_b32 s88, s27, 12\n"
"s_add_u32 s88, s88, s82\n"
"s_add_u32 s28, s27, 128\n"
"s_cmp_eq_u32 s28, 896\n"
"s_cselect_b32 s28, 0, s28\n"
"s_add_u32 s29, s28, 128\n"
"s_cmp_eq_u32 s29, 896\n"
"s_cselect_b32 s29, 0, s29\n"
"s_add_u32 s30, s29, 128\n"
"s_cmp_eq_u32 s30, 896\n"
"s_cselect_b32 s30, 0, s30\n"
"buffer_load_dwordx4 v26, s[36:39], s93 offen offset:1024 lds\n"
"s_cmp_eq_u32 s35, 0\n"
"s_cselect_b32 s89, s28, s29\n"
"s_lshl_b32 s89, s89, 12\n"
"s_add_u32 s89, s89, s85\n"
"s_cmp_eq_u32 s35, 0\n"
"s_cselect_b32 s91, s30, s28\n"
"s_lshl_b32 s91, s91, 12\n"
"s_add_u32 s91, s91, s82\n"
"s_cmp_eq_u32 s35, 0\n"
"s_cselect_b32 s94, s29, s30\n"
"s_lshl_b32 s94, s94, 12\n"
"s_add_u32 s94, s94, s85\n"
"v_lshrrev_b32 v0, 2, v1\n"
"v_lshlrev_b32 v0, 7, v0\n"
"s_mov_b64 s[44:45], s[6:7]\n"
"s_and_b32 s45, s45, 0xffff\n"
"s_mov_b32 s46, 262144\n"
"s_add_u32 m0, s62, 26816\n"
"s_add_u32 s93, s70, s24\n"
"buffer_load_dwordx4 v25, s[36:39], s93 offen offset:0 lds\n"
"s_mov_b32 s47, 0x20000\n"
"s_mov_b64 s[48:49], s[8:9]\n"
"s_and_b32 s49, s49, 0xffff\n"
"s_mov_b32 s50, 262144\n"
"s_mov_b32 s51, 0x20000\n"
"s_mov_b64 s[52:53], s[16:17]\n"
"s_and_b32 s53, s53, 0xffff\n"
"s_mov_b32 s54, 2894080\n"
"s_mov_b32 s55, 0x20000\n"
"v_and_b32 v30, 15, v1\n"
"v_lshrrev_b32 v10, 4, v1\n"
"v_lshrrev_b32 v27, 1, v30\n"
"v_xor_b32 v27, v27, v10\n"
"v_lshlrev_b32 v27, 4, v27\n"
"buffer_load_dwordx4 v26, s[36:39], s93 offen offset:1024 lds\n"
"v_lshl_add_u32 v27, v30, 7, v27\n"
"v_xor_b32 v28, 64, v27\n"
"s_add_u32 s85, s62, 24768\n"
"v_add_u32 v27, s85, v27\n"
"v_add_u32 v28, s85, v28\n"
"s_mul_i32 s85, s61, 74304\n"
"s_add_u32 s83, s85, s34\n"
"s_sub_u32 s84, 74304, s85\n"
"s_add_u32 s84, s84, s34\n"
"s_lshl_b32 s85, s61, 2\n"
"v_or_b32 v15, s85, v10\n"
"v_xor_b32 v15, v15, v30\n"
"v_lshlrev_b32 v15, 4, v15\n"
"v_lshl_add_u32 v15, v30, 9, v15\n"
"s_add_u32 m0, s62, 33024\n"
"s_add_u32 s93, s71, s24\n"
"buffer_load_dwordx4 v25, s[36:39], s93 offen offset:0 lds\n"
"v_xor_b32 v16, 64, v15\n"
"v_xor_b32 v17, 128, v15\n"
"v_xor_b32 v18, 192, v15\n"
"v_add_u32 v19, s84, v15\n"
"v_add_u32 v20, s84, v16\n"
"v_add_u32 v21, s84, v17\n"
"v_add_u32 v22, s84, v18\n"
"v_add_u32 v15, s83, v15\n"
"v_add_u32 v16, s83, v16\n"
"v_add_u32 v17, s83, v17\n"
"v_add_u32 v18, s83, v18\n"
"v_bfe_u32 v4, v1, 2, 2\n"
"v_and_b32 v5, 3, v1\n"
"v_lshrrev_b32 v2, 1, v4\n"
"buffer_load_dwordx4 v26, s[36:39], s93 offen offset:1024 lds\n"
"v_xor_b32 v2, s61, v2\n"
"v_lshl_add_u32 v2, v2, 2, v5\n"
"v_lshlrev_b32 v2, 4, v2\n"
"v_lshl_add_u32 v2, v4, 7, v2\n"
"v_xor_b32 v3, 64, v2\n"
"v_lshl_add_u32 v29, v10, 2, v4\n"
"v_bfe_u32 v23, v29, 1, 2\n"
"s_lshl_b32 s85, s61, 1\n"
"v_xor_b32 v23, s85, v23\n"
"v_lshlrev_b32 v23, 5, v23\n"
"v_lshl_add_u32 v29, v29, 7, v23\n"
"v_lshl_add_u32 v29, v5, 3, v29\n"
"v_mul_u32_u24 v10, 1032, v10\n"
"v_add_u32 v10, s34, v10\n"
"s_add_u32 m0, s62, 35072\n"
"s_add_u32 s93, s72, s24\n"
"buffer_load_dwordx4 v25, s[36:39], s93 offen offset:0 lds\n"
"v_add_u32 v2, v10, v2\n"
"v_add_u32 v3, v10, v3\n"
"v_add_u32 v4, 74304, v2\n"
"v_add_u32 v5, 74304, v3\n"
"v_lshrrev_b32 v23, 5, v1\n"
"v_and_b32 v10, 31, v1\n"
"s_lshl_b32 s85, s61, 3\n"
"v_or_b32 v11, s85, v23\n"
"v_xor_b32 v11, v11, v10\n"
"v_lshlrev_b32 v11, 4, v11\n"
"v_lshl_add_u32 v11, v23, 9, v11\n"
"v_xor_b32 v12, 32, v11\n"
"v_xor_b32 v13, 64, v11\n"
"v_xor_b32 v14, 96, v11\n"
"buffer_load_dwordx4 v26, s[36:39], s93 offen offset:1024 lds\n"
"s_lshl_b32 s64, s60, 12\n"
"s_lshl_b32 s65, s61, 15\n"
"s_lshl_b32 s85, s35, 13\n"
"s_add_u32 s65, s65, s85\n"
"s_lshl_b32 s85, s60, 11\n"
"v_lshl_add_u32 v23, v1, 4, s85\n"
"v_xor_b32 v24, 2048, v23\n"
"s_add_u32 s85, s34, 153984\n"
"v_add_u32 v23, s85, v23\n"
"v_add_u32 v24, s85, v24\n"
"s_mul_i32 s74, s60, 86016\n"
"s_add_u32 s76, s74, 28672\n"
"s_add_u32 s78, s74, 57344\n"
"s_mul_i32 s80, s35, 28672\n"
"s_add_u32 m0, s62, 8256\n"
"s_add_u32 s93, s66, 256\n"
"buffer_load_dwordx4 v6, s[40:43], s93 offen offset:0 lds\n"
"s_add_u32 s80, s80, 344064\n"
"s_mov_b32 s82, 0x3db8aa3b\n"
"s_lshl_b32 s85, s60, 10\n"
"s_add_u32 s85, s85, 148608\n"
"s_add_u32 s85, s85, s34\n"
"v_lshl_add_u32 v10, v30, 4, s85\n"
"v_mov_b32 v36, 0\n"
"v_mov_b32 v32, 0xc0a00000\n"
"v_mov_b32 v33, v32\n"
"v_mov_b32 v34, v32\n"
"v_mov_b32 v35, v32\n"
"s_add_u32 s85, s34, 153728\n"
"v_mov_b32 v30, s85\n"
"ds_write_b32 v30, v36\n"
"buffer_load_dwordx4 v7, s[40:43], s93 offen offset:1032 lds\n"
"s_cmp_eq_u32 s86, 0\n"
"s_cbranch_scc1 NOZERO%=\n"
"ds_write_b128 v10, v[32:35] offset:0\n"
"ds_write_b128 v10, v[32:35] offset:256\n"
"ds_write_b128 v10, v[32:35] offset:512\n"
"ds_write_b128 v10, v[32:35] offset:768\n"
"NOZERO%=:\n"
"s_waitcnt lgkmcnt(0)\n"
"s_mov_b32 s77, s98\n"
"s_mov_b32 s73, vcc_lo\n"
"buffer_load_dwordx4 v8, s[40:43], s93 offen offset:2064 lds\n"
"buffer_load_dwordx4 v9, s[40:43], s93 offen offset:3096 lds\n"
"s_mov_b64 exec, 0\n"
"s_cmp_eq_u32 s60, 0\n"
"s_cbranch_scc1 PG1_%=\n"
"s_cmp_eq_u32 s60, 1\n"
"s_cbranch_scc1 PG2_%=\n"
"s_cmp_eq_u32 s60, 2\n"
"s_cbranch_scc1 PG3_%=\n"
"s_branch PG4_%=\n"
"PGX%=:\n"
"s_mov_b64 exec, -1\n"
"s_waitcnt vmcnt(12)\n"
"s_barrier\n"
"s_waitcnt vmcnt(8)\n"
"ds_read_b128 a[32:35], v27 offset:0\n"
"ds_read_b128 a[48:51], v28 offset:0\n"
"ds_read_b128 a[36:39], v27 offset:2048\n"
"ds_read_b128 a[52:55], v28 offset:2048\n"
"ds_read_b64_tr_b16 a[0:1], v2 offset:0\n"
"ds_read_b64_tr_b16 a[2:3], v2 offset:512\n"
"ds_read_b64_tr_b16 a[4:5], v2 offset:4128\n"
"ds_read_b64_tr_b16 a[6:7], v2 offset:4640\n"
"s_memrealtime s[2:3]\n"
"s_memtime vcc\n"
"s_waitcnt vmcnt(4)\n"
"ds_read_b128 a[40:43], v27 offset:8256\n"
"ds_read_b128 a[56:59], v28 offset:8256\n"
"ds_read_b128 a[44:47], v27 offset:10304\n"
"ds_read_b128 a[60:63], v28 offset:10304\n"
"ds_read_b64_tr_b16 a[8:9], v2 offset:8\n"
"ds_read_b64_tr_b16 a[10:11], v2 offset:520\n"
"ds_read_b64_tr_b16 a[12:13], v2 offset:4136\n"
"ds_read_b64_tr_b16 a[14:15], v2 offset:4648\n"
"s_add_u32 m0, s62, 16512\n"
"s_add_u32 s93, s67, 0\n"
"buffer_load_dwordx4 v6, s[40:43], s93 offen offset:0 lds\n"
"buffer_load_dwordx4 v7, s[40:43], s93 offen offset:1032 lds\n"
"ds_read_b64_tr_b16 a[16:17], v3 offset:0\n"
"ds_read_b64_tr_b16 a[18:19], v3 offset:512\n"
"ds_read_b64_tr_b16 a[20:21], v3 offset:4128\n"
"ds_read_b64_tr_b16 a[22:23], v3 offset:4640\n"
"s_waitcnt lgkmcnt(15)\n"
"s_waitcnt lgkmcnt(12)\n"
"v_mfma_f32_16x16x32_bf16 v[32:35], a[0:3], a[32:35], 0\n"
"s_mov_b64 s[56:57], s[12:13]\n"
"ds_read_b64_tr_b16 a[24:25], v3 offset:8\n"
"s_and_b32 s57, s57, 0xffff\n"
"v_mfma_f32_16x16x32_bf16 v[32:35], a[4:7], a[48:51], v[32:35]\n"
"s_mov_b32 s58, 917504\n"
"ds_read_b64_tr_b16 a[26:27], v3 offset:520\n"
"s_mov_b32 s59, 0x20000\n"
"s_nop 7\n"
"v_mfma_f32_16x16x32_bf16 v[36:39], a[4:7], a[52:55], 0\n"
"s_and_b32 s90, s20, 3\n"
"ds_read_b64_tr_b16 a[28:29], v3 offset:4136\n"
"s_mul_i32 s90, s90, 224\n"
"v_mfma_f32_16x16x32_bf16 v[36:39], a[0:3], a[36:39], v[36:39]\n"
"s_sub_u32 s56, s56, s90\n"
"s_subb_u32 s57, s57, 0\n"
"ds_read_b64_tr_b16 a[30:31], v3 offset:4648\n"
"s_lshl_b32 s90, s21, 2\n"
"s_waitcnt lgkmcnt(12)\n"
"v_mfma_f32_16x16x32_bf16 v[40:43], a[0:3], a[40:43], 0\n"
"s_add_u32 s90, s90, s60\n"
"s_lshl_b32 s90, s90, 13\n"
"s_lshl_b32 s85, s60, 8\n"
"v_mfma_f32_16x16x32_bf16 v[40:43], a[4:7], a[56:59], v[40:43]\n"
"s_add_u32 s85, s85, s34\n"
"s_add_u32 s85, s85, 152704\n"
"v_mfma_f32_16x16x32_bf16 v[44:47], a[4:7], a[60:63], 0\n"
"s_waitcnt lgkmcnt(8)\n"
"ds_read_b64_tr_b16 a[4:5], v4 offset:4128\n"
"v_mfma_f32_16x16x32_bf16 v[44:47], a[0:3], a[44:47], v[44:47]\n"
"ds_read_b64_tr_b16 a[0:1], v4 offset:0\n"
"v_mfma_f32_16x16x32_bf16 v[48:51], a[8:11], a[32:35], 0\n"
"ds_read_b64_tr_b16 a[2:3], v4 offset:512\n"
"v_mfma_f32_16x16x32_bf16 v[48:51], a[12:15], a[48:51], v[48:51]\n"
"buffer_load_dwordx4 v8, s[40:43], s93 offen offset:2064 lds\n"
"s_nop 7\n"
"v_mfma_f32_16x16x32_bf16 v[52:55], a[12:15], a[52:55], 0\n"
"ds_read_b64_tr_b16 a[6:7], v4 offset:4640\n"
"v_mfma_f32_16x16x32_bf16 v[52:55], a[8:11], a[36:39], v[52:55]\n"
"v_mfma_f32_16x16x32_bf16 v[56:59], a[8:11], a[40:43], 0\n"
"v_mfma_f32_16x16x32_bf16 v[56:59], a[12:15], a[56:59], v[56:59]\n"
"v_mfma_f32_16x16x32_bf16 v[60:63], a[12:15], a[60:63], 0\n"
"s_waitcnt lgkmcnt(8)\n"
"buffer_load_dwordx4 v9, s[40:43], s93 offen offset:3096 lds\n"
"v_mfma_f32_16x16x32_bf16 v[60:63], a[8:11], a[44:47], v[60:63]\n"
"s_addk_i32 m0, 24768\n"
"ds_read_b64_tr_b16 a[8:9], v4 offset:8\n"
"s_add_u32 s93, s69, s25\n"
"v_mfma_f32_16x16x32_bf16 v[64:67], a[16:19], a[32:35], 0\n"
"ds_read_b64_tr_b16 a[10:11], v4 offset:520\n"
"v_mfma_f32_16x16x32_bf16 v[64:67], a[20:23], a[48:51], v[64:67]\n"
"ds_read_b64_tr_b16 a[12:13], v4 offset:4136\n"
"s_nop 8\n"
"v_mfma_f32_16x16x32_bf16 v[68:71], a[20:23], a[52:55], 0\n"
"buffer_load_dwordx4 v25, s[36:39], s93 offen offset:0 lds\n"
"v_mfma_f32_16x16x32_bf16 v[68:71], a[16:19], a[36:39], v[68:71]\n"
"ds_read_b64_tr_b16 a[14:15], v4 offset:4648\n"
"v_mfma_f32_16x16x32_bf16 v[72:75], a[16:19], a[40:43], 0\n"
"s_waitcnt lgkmcnt(8)\n"
"ds_read_b64_tr_b16 a[16:17], v5 offset:0\n"
"v_mfma_f32_16x16x32_bf16 v[72:75], a[20:23], a[56:59], v[72:75]\n"
"ds_read_b64_tr_b16 a[18:19], v5 offset:512\n"
"v_mfma_f32_16x16x32_bf16 v[76:79], a[24:27], a[32:35], 0\n"
"ds_read_b64_tr_b16 a[20:21], v5 offset:4128\n"
"v_mfma_f32_16x16x32_bf16 v[76:79], a[28:31], a[48:51], v[76:79]\n"
"buffer_load_dwordx4 v26, s[36:39], s93 offen offset:1024 lds\n"
"v_mfma_f32_16x16x32_bf16 v[80:83], a[28:31], a[52:55], 0\n"
"s_addk_i32 m0, 2048\n"
"ds_read_b64_tr_b16 a[22:23], v5 offset:4640\n"
"s_add_u32 s93, s70, s25\n"
"v_mfma_f32_16x16x32_bf16 v[80:83], a[24:27], a[36:39], v[80:83]\n"
"s_nop 10\n"
"v_mfma_f32_16x16x32_bf16 v[84:87], a[24:27], a[40:43], 0\n"
"s_waitcnt lgkmcnt(8)\n"
"ds_read_b64_tr_b16 a[24:25], v5 offset:8\n"
"v_mfma_f32_16x16x32_bf16 v[84:87], a[28:31], a[56:59], v[84:87]\n"
"buffer_load_dwordx4 v25, s[36:39], s93 offen offset:0 lds\n"
"v_mfma_f32_16x16x32_bf16 v[88:91], a[0:3], a[32:35], 0\n"
"ds_read_b64_tr_b16 a[26:27], v5 offset:520\n"
"v_mfma_f32_16x16x32_bf16 v[88:91], a[4:7], a[48:51], v[88:91]\n"
"ds_read_b64_tr_b16 a[28:29], v5 offset:4136\n"
"v_mfma_f32_16x16x32_bf16 v[92:95], a[4:7], a[52:55], 0\n"
"ds_read_b64_tr_b16 a[30:31], v5 offset:4648\n"
"v_mfma_f32_16x16x32_bf16 v[92:95], a[0:3], a[36:39], v[92:95]\n"
"buffer_load_dwordx4 v26, s[36:39], s93 offen offset:1024 lds\n"
"v_mfma_f32_16x16x32_bf16 v[96:99], a[0:3], a[40:43], 0\n"
"v_mfma_f32_16x16x32_bf16 v[96:99], a[4:7], a[56:59], v[96:99]\n"
"s_addk_i32 m0, 6208\n"
"s_add_u32 s93, s71, s25\n"
"s_nop 8\n"
"v_mfma_f32_16x16x32_bf16 v[100:103], a[4:7], a[60:63], 0\n"
"s_waitcnt lgkmcnt(8)\n"
"v_mfma_f32_16x16x32_bf16 v[100:103], a[0:3], a[44:47], v[100:103]\n"
"v_mfma_f32_16x16x32_bf16 v[104:107], a[8:11], a[32:35], 0\n"
"buffer_load_dwordx4 v25, s[36:39], s93 offen offset:0 lds\n"
"v_mfma_f32_16x16x32_bf16 v[104:107], a[12:15], a[48:51], v[104:107]\n"
"v_mfma_f32_16x16x32_bf16 v[108:111], a[12:15], a[52:55], 0\n"
"v_mfma_f32_16x16x32_bf16 v[108:111], a[8:11], a[36:39], v[108:111]\n"
"v_mfma_f32_16x16x32_bf16 v[112:115], a[8:11], a[40:43], 0\n"
"buffer_load_dwordx4 v26, s[36:39], s93 offen offset:1024 lds\n"
"v_mfma_f32_16x16x32_bf16 v[112:115], a[12:15], a[56:59], v[112:115]\n"
"s_addk_i32 m0, 2048\n"
"s_add_u32 s93, s72, s25\n"
"s_nop 8\n"
"v_mfma_f32_16x16x32_bf16 v[116:119], a[12:15], a[60:63], 0\n"
"s_waitcnt lgkmcnt(4)\n"
"v_mfma_f32_16x16x32_bf16 v[116:119], a[8:11], a[44:47], v[116:119]\n"
"s_waitcnt vmcnt(10) lgkmcnt(0)\n"
"v_mfma_f32_16x16x32_bf16 v[120:123], a[16:19], a[32:35], 0\n"
"s_barrier\n"
"v_mfma_f32_16x16x32_bf16 v[120:123], a[20:23], a[48:51], v[120:123]\n"
"s_sub_u32 s2, s2, s77\n"
"buffer_load_dwordx4 v25, s[36:39], s93 offen offset:0 lds\n"
"v_mfma_f32_16x16x32_bf16 v[124:127], a[20:23], a[52:55], 0\n"
"s_sub_u32 s73, vcc_lo, s73\n"
"ds_read_b64_tr_b16 a[0:1], v2 offset:8256\n"
"s_mulk_i32 s2, 215\n"
"v_mfma_f32_16x16x32_bf16 v[124:127], a[16:19], a[36:39], v[124:127]\n"
"s_mulk_i32 s73, 10\n"
"ds_read_b64_tr_b16 a[2:3], v2 offset:8768\n"
"v_mfma_f32_16x16x32_bf16 v[128:131], a[16:19], a[40:43], 0\n"
"ds_read_b64_tr_b16 a[4:5], v2 offset:12384\n"
"v_mfma_f32_16x16x32_bf16 v[128:131], a[20:23], a[56:59], v[128:131]\n"
"ds_read_b64_tr_b16 a[6:7], v2 offset:12896\n"
"s_nop 8\n"
"v_mfma_f32_16x16x32_bf16 v[132:135], a[24:27], a[32:35], 0\n"
"buffer_load_dwordx4 v26, s[36:39], s93 offen offset:1024 lds\n"
"v_mfma_f32_16x16x32_bf16 v[132:135], a[28:31], a[48:51], v[132:135]\n"
"s_addk_i32 m0, -26816\n"
"ds_read_b64_tr_b16 a[8:9], v2 offset:8264\n"
"s_add_u32 s93, s67, 256\n"
"v_mfma_f32_16x16x32_bf16 v[136:139], a[28:31], a[52:55], 0\n"
"s_cmp_lt_u32 s73, s2\n"
"s_cselect_b32 s23, 5, 0\n"
"v_mfma_f32_16x16x32_bf16 v[136:139], a[24:27], a[36:39], v[136:139]\n"
"buffer_load_dwordx4 v6, s[40:43], s93 offen offset:0 lds\n"
"v_mfma_f32_16x16x32_bf16 v[140:143], a[24:27], a[40:43], 0\n"
"s_waitcnt lgkmcnt(1)\n"
"ds_read_b64_tr_b16 a[10:11], v2 offset:8776\n"
"v_mfma_f32_16x16x32_bf16 v[140:143], a[28:31], a[56:59], v[140:143]\n"
"ds_read_b64_tr_b16 a[12:13], v2 offset:12392\n"
"v_mfma_f32_16x16x32_bf16 v[144:147], a[0:3], a[32:35], 0\n"
"ds_read_b64_tr_b16 a[14:15], v2 offset:12904\n"
"v_mfma_f32_16x16x32_bf16 v[144:147], a[4:7], a[48:51], v[144:147]\n"
"ds_read_b64_tr_b16 a[16:17], v3 offset:8256\n"
"s_nop 8\n"
"v_mfma_f32_16x16x32_bf16 v[148:151], a[4:7], a[52:55], 0\n"
"buffer_load_dwordx4 v7, s[40:43], s93 offen offset:1032 lds\n"
"v_mfma_f32_16x16x32_bf16 v[148:151], a[0:3], a[36:39], v[148:151]\n"
"ds_read_b64_tr_b16 a[18:19], v3 offset:8768\n"
"v_mfma_f32_16x16x32_bf16 v[152:155], a[0:3], a[40:43], 0\n"
"ds_read_b64_tr_b16 a[20:21], v3 offset:12384\n"
"v_mfma_f32_16x16x32_bf16 v[152:155], a[4:7], a[56:59], v[152:155]\n"
"ds_read_b64_tr_b16 a[22:23], v3 offset:12896\n"
"v_mfma_f32_16x16x32_bf16 v[156:159], a[4:7], a[60:63], 0\n"
"s_waitcnt lgkmcnt(4)\n"
"buffer_load_dwordx4 v8, s[40:43], s93 offen offset:2064 lds\n"
"v_mfma_f32_16x16x32_bf16 v[156:159], a[0:3], a[44:47], v[156:159]\n"
"ds_read_b64_tr_b16 a[24:25], v3 offset:8264\n"
"v_mfma_f32_16x16x32_bf16 v[160:163], a[8:11], a[32:35], 0\n"
"ds_read_b64_tr_b16 a[26:27], v3 offset:8776\n"
"v_mfma_f32_16x16x32_bf16 v[160:163], a[12:15], a[48:51], v[160:163]\n"
"ds_read_b64_tr_b16 a[28:29], v3 offset:12392\n"
"s_nop 8\n"
"v_mfma_f32_16x16x32_bf16 v[164:167], a[12:15], a[52:55], 0\n"
"buffer_load_dwordx4 v9, s[40:43], s93 offen offset:3096 lds\n"
"v_mfma_f32_16x16x32_bf16 v[164:167], a[8:11], a[36:39], v[164:167]\n"
"ds_read_b64_tr_b16 a[30:31], v3 offset:12904\n"
"v_mfma_f32_16x16x32_bf16 v[168:171], a[8:11], a[40:43], 0\n"
"s_add_u32 m0, s62, 57792\n"
"ds_read_b64_tr_b16 a[0:1], v4 offset:8256\n"
"s_add_u32 s93, s69, s26\n"
"v_mfma_f32_16x16x32_bf16 v[168:171], a[12:15], a[56:59], v[168:171]\n"
"ds_read_b64_tr_b16 a[2:3], v4 offset:8768\n"
"v_mfma_f32_16x16x32_bf16 v[172:175], a[12:15], a[60:63], 0\n"
"s_waitcnt lgkmcnt(5)\n"
"ds_read_b64_tr_b16 a[4:5], v4 offset:12384\n"
"v_mfma_f32_16x16x32_bf16 v[172:175], a[8:11], a[44:47], v[172:175]\n"
"buffer_load_dwordx4 v25, s[36:39], s93 offen offset:0 lds\n"
"v_mfma_f32_16x16x32_bf16 v[176:179], a[16:19], a[32:35], 0\n"
"ds_read_b64_tr_b16 a[6:7], v4 offset:12896\n"
"v_mfma_f32_16x16x32_bf16 v[176:179], a[20:23], a[48:51], v[176:179]\n"
"ds_read_b64_tr_b16 a[8:9], v4 offset:8264\n"
"s_nop 8\n"
"v_mfma_f32_16x16x32_bf16 v[180:183], a[20:23], a[52:55], 0\n"
"ds_read_b64_tr_b16 a[10:11], v4 offset:8776\n"
"v_mfma_f32_16x16x32_bf16 v[180:183], a[16:19], a[36:39], v[180:183]\n"
"buffer_load_dwordx4 v26, s[36:39], s93 offen offset:1024 lds\n"
"v_mfma_f32_16x16x32_bf16 v[184:187], a[16:19], a[40:43], 0\n"
"s_waitcnt lgkmcnt(4)\n"
"ds_read_b64_tr_b16 a[12:13], v4 offset:12392\n"
"v_mfma_f32_16x16x32_bf16 v[184:187], a[20:23], a[56:59], v[184:187]\n"
"s_addk_i32 m0, 2048\n"
"ds_read_b64_tr_b16 a[14:15], v4 offset:12904\n"
"s_add_u32 s93, s70, s26\n"
"v_mfma_f32_16x16x32_bf16 v[188:191], a[24:27], a[32:35], 0\n"
"ds_read_b64_tr_b16 a[16:17], v5 offset:8256\n"
"v_mfma_f32_16x16x32_bf16 v[188:191], a[28:31], a[48:51], v[188:191]\n"
"ds_read_b64_tr_b16 a[18:19], v5 offset:8768\n"
"v_mfma_f32_16x16x32_bf16 v[192:195], a[28:31], a[52:55], 0\n"
"buffer_load_dwordx4 v25, s[36:39], s93 offen offset:0 lds\n"
"v_mfma_f32_16x16x32_bf16 v[192:195], a[24:27], a[36:39], v[192:195]\n"
"ds_read_b64_tr_b16 a[20:21], v5 offset:12384\n"
"s_nop 8\n"
"v_mfma_f32_16x16x32_bf16 v[196:199], a[24:27], a[40:43], 0\n"
"s_waitcnt lgkmcnt(5)\n"
"ds_read_b64_tr_b16 a[22:23], v5 offset:12896\n"
"v_mfma_f32_16x16x32_bf16 v[196:199], a[28:31], a[56:59], v[196:199]\n"
"ds_read_b64_tr_b16 a[24:25], v5 offset:8264\n"
"v_mfma_f32_16x16x32_bf16 v[200:203], a[0:3], a[32:35], 0\n"
"buffer_load_dwordx4 v26, s[36:39], s93 offen offset:1024 lds\n"
"v_mfma_f32_16x16x32_bf16 v[200:203], a[4:7], a[48:51], v[200:203]\n"
"ds_read_b64_tr_b16 a[26:27], v5 offset:8776\n"
"v_mfma_f32_16x16x32_bf16 v[204:207], a[4:7], a[52:55], 0\n"
"s_addk_i32 m0, 6208\n"
"ds_read_b64_tr_b16 a[28:29], v5 offset:12392\n"
"s_add_u32 s93, s71, s26\n"
"v_mfma_f32_16x16x32_bf16 v[204:207], a[0:3], a[36:39], v[204:207]\n"
"ds_read_b64_tr_b16 a[30:31], v5 offset:12904\n"
"v_mfma_f32_16x16x32_bf16 v[208:211], a[0:3], a[40:43], 0\n"
"s_waitcnt vmcnt(12)\n"
"ds_read_b128 a[64:67], v27 offset:16512\n"
"s_waitcnt vmcnt(8)\n"
"v_mfma_f32_16x16x32_bf16 v[208:211], a[4:7], a[56:59], v[208:211]\n"
"buffer_load_dwordx4 v25, s[36:39], s93 offen offset:0 lds\n"
"s_nop 7\n"
"v_mfma_f32_16x16x32_bf16 v[212:215], a[4:7], a[60:63], 0\n"
"s_waitcnt lgkmcnt(4)\n"
"ds_read_b128 a[80:83], v28 offset:16512\n"
"v_mfma_f32_16x16x32_bf16 v[212:215], a[0:3], a[44:47], v[212:215]\n"
"ds_read_b128 a[68:71], v27 offset:18560\n"
"v_mfma_f32_16x16x32_bf16 v[216:219], a[8:11], a[32:35], 0\n"
"ds_read_b128 a[84:87], v28 offset:18560\n"
"v_mfma_f32_16x16x32_bf16 v[216:219], a[12:15], a[48:51], v[216:219]\n"
"buffer_load_dwordx4 v26, s[36:39], s93 offen offset:1024 lds\n"
"v_mfma_f32_16x16x32_bf16 v[220:223], a[12:15], a[52:55], 0\n"
"s_addk_i32 m0, 2048\n"
"ds_read_b128 a[72:75], v27 offset:24768\n"
"s_add_u32 s93, s72, s26\n"
"v_mfma_f32_16x16x32_bf16 v[220:223], a[8:11], a[36:39], v[220:223]\n"
"ds_read_b128 a[88:91], v28 offset:24768\n"
"v_mfma_f32_16x16x32_bf16 v[224:227], a[8:11], a[40:43], 0\n"
"ds_read_b128 a[76:79], v27 offset:26816\n"
"v_mfma_f32_16x16x32_bf16 v[224:227], a[12:15], a[56:59], v[224:227]\n"
"s_waitcnt lgkmcnt(3)\n"
"buffer_load_dwordx4 v25, s[36:39], s93 offen offset:0 lds\n"
"s_nop 7\n"
"v_mfma_f32_16x16x32_bf16 v[228:231], a[12:15], a[60:63], 0\n"
"ds_read_b128 a[92:95], v28 offset:26816\n"
"v_mfma_f32_16x16x32_bf16 v[228:231], a[8:11], a[44:47], v[228:231]\n"
"v_mfma_f32_16x16x32_bf16 v[232:235], a[16:19], a[32:35], 0\n"
"s_barrier\n"
"v_mfma_f32_16x16x32_bf16 v[232:235], a[20:23], a[48:51], v[232:235]\n"
"ds_read_b64_tr_b16 a[0:1], v2 offset:16512\n"
"v_mfma_f32_16x16x32_bf16 v[236:239], a[20:23], a[52:55], 0\n"
"buffer_load_dwordx4 v26, s[36:39], s93 offen offset:1024 lds\n"
"v_mfma_f32_16x16x32_bf16 v[236:239], a[16:19], a[36:39], v[236:239]\n"
"s_add_u32 m0, s62, 33024\n"
"ds_read_b64_tr_b16 a[2:3], v2 offset:17024\n"
"s_add_u32 s93, s68, 0\n"
"v_mfma_f32_16x16x32_bf16 v[240:243], a[16:19], a[40:43], 0\n"
"ds_read_b64_tr_b16 a[4:5], v2 offset:20640\n"
"s_waitcnt lgkmcnt(3)\n"
"v_mfma_f32_16x16x32_bf16 v[240:243], a[20:23], a[56:59], v[240:243]\n"
"ds_read_b64_tr_b16 a[6:7], v2 offset:21152\n"
"s_nop 8\n"
"v_mfma_f32_16x16x32_bf16 v[244:247], a[24:27], a[32:35], 0\n"
"buffer_load_dwordx4 v6, s[40:43], s93 offen offset:0 lds\n"
"v_mfma_f32_16x16x32_bf16 v[244:247], a[28:31], a[48:51], v[244:247]\n"
"ds_read_b64_tr_b16 a[8:9], v2 offset:16520\n"
"v_mfma_f32_16x16x32_bf16 v[248:251], a[28:31], a[52:55], 0\n"
"ds_read_b64_tr_b16 a[10:11], v2 offset:17032\n"
"v_mfma_f32_16x16x32_bf16 v[248:251], a[24:27], a[36:39], v[248:251]\n"
"ds_read_b64_tr_b16 a[12:13], v2 offset:20648\n"
"v_mfma_f32_16x16x32_bf16 v[252:255], a[24:27], a[40:43], 0\n"
"s_waitcnt lgkmcnt(3)\n"
"ds_read_b64_tr_b16 a[14:15], v2 offset:21160\n"
"v_mfma_f32_16x16x32_bf16 v[252:255], a[28:31], a[56:59], v[252:255]\n"
"buffer_load_dwordx4 v7, s[40:43], s93 offen offset:1032 lds\n"
"v_mfma_f32_16x16x32_bf16 v[32:35], a[0:3], a[64:67], v[32:35]\n"
"ds_read_b64_tr_b16 a[16:17], v3 offset:16512\n"
"v_mfma_f32_16x16x32_bf16 v[32:35], a[4:7], a[80:83], v[32:35]\n"
"ds_read_b64_tr_b16 a[18:19], v3 offset:17024\n"
"s_nop 8\n"
"v_mfma_f32_16x16x32_bf16 v[36:39], a[4:7], a[84:87], v[36:39]\n"
"ds_read_b64_tr_b16 a[20:21], v3 offset:20640\n"
"v_mfma_f32_16x16x32_bf16 v[36:39], a[0:3], a[68:71], v[36:39]\n"
"buffer_load_dwordx4 v8, s[40:43], s93 offen offset:2064 lds\n"
"v_mfma_f32_16x16x32_bf16 v[40:43], a[0:3], a[72:75], v[40:43]\n"
"ds_read_b64_tr_b16 a[22:23], v3 offset:21152\n"
"v_mfma_f32_16x16x32_bf16 v[40:43], a[4:7], a[88:91], v[40:43]\n"
"ds_read_b64_tr_b16 a[24:25], v3 offset:16520\n"
"v_mfma_f32_16x16x32_bf16 v[44:47], a[4:7], a[92:95], v[44:47]\n"
"s_waitcnt lgkmcnt(5)\n"
"ds_read_b64_tr_b16 a[26:27], v3 offset:17032\n"
"v_mfma_f32_16x16x32_bf16 v[44:47], a[0:3], a[76:79], v[44:47]\n"
"buffer_load_dwordx4 v9, s[40:43], s93 offen offset:3096 lds\n"
"v_mfma_f32_16x16x32_bf16 v[48:51], a[8:11], a[64:67], v[48:51]\n"
"ds_read_b64_tr_b16 a[28:29], v3 offset:20648\n"
"v_mfma_f32_16x16x32_bf16 v[48:51], a[12:15], a[80:83], v[48:51]\n"
"s_addk_i32 m0, 8256\n"
"ds_read_b64_tr_b16 a[30:31], v3 offset:21160\n"
"s_add_u32 s93, s69, s27\n"
"s_nop 7\n"
"v_mfma_f32_16x16x32_bf16 v[52:55], a[12:15], a[84:87], v[52:55]\n"
"ds_read_b64_tr_b16 a[0:1], v4 offset:16512\n"
"v_mfma_f32_16x16x32_bf16 v[52:55], a[8:11], a[68:71], v[52:55]\n"
"ds_read_b64_tr_b16 a[2:3], v4 offset:17024\n"
"v_mfma_f32_16x16x32_bf16 v[56:59], a[8:11], a[72:75], v[56:59]\n"
"buffer_load_dwordx4 v25, s[36:39], s93 offen offset:0 lds\n"
"v_mfma_f32_16x16x32_bf16 v[56:59], a[12:15], a[88:91], v[56:59]\n"
"ds_read_b64_tr_b16 a[4:5], v4 offset:20640\n"
"v_mfma_f32_16x16x32_bf16 v[60:63], a[12:15], a[92:95], v[60:63]\n"
"s_waitcnt lgkmcnt(5)\n"
"ds_read_b64_tr_b16 a[6:7], v4 offset:21152\n"
"v_mfma_f32_16x16x32_bf16 v[60:63], a[8:11], a[76:79], v[60:63]\n"
"ds_read_b64_tr_b16 a[8:9], v4 offset:16520\n"
"v_mfma_f32_16x16x32_bf16 v[64:67], a[16:19], a[64:67], v[64:67]\n"
"buffer_load_dwordx4 v26, s[36:39], s93 offen offset:1024 lds\n"
"PG1_%=:\n"
"s_cbranch_execz PG5_%=\n"
"v_mfma_f32_16x16x32_bf16 v[64:67], a[20:23], a[80:83], v[64:67]\n"
"ds_read_b64_tr_b16 a[10:11], v4 offset:17032\n"
"s_nop 8\n"
"v_mfma_f32_16x16x32_bf16 v[68:71], a[20:23], a[84:87], v[68:71]\n"
"s_addk_i32 m0, 2048\n"
"ds_read_b64_tr_b16 a[12:13], v4 offset:20648\n"
"s_add_u32 s93, s70, s27\n"
"v_mfma_f32_16x16x32_bf16 v[68:71], a[16:19], a[68:71], v[68:71]\n"
"ds_read_b64_tr_b16 a[14:15], v4 offset:21160\n"
"v_mfma_f32_16x16x32_bf16 v[72:75], a[16:19], a[72:75], v[72:75]\n"
"s_waitcnt lgkmcnt(5)\n"
"ds_read_b64_tr_b16 a[16:17], v5 offset:16512\n"
"v_mfma_f32_16x16x32_bf16 v[72:75], a[20:23], a[88:91], v[72:75]\n"
"buffer_load_dwordx4 v25, s[36:39], s93 offen offset:0 lds\n"
"v_mfma_f32_16x16x32_bf16 v[76:79], a[24:27], a[64:67], v[76:79]\n"
"ds_read_b64_tr_b16 a[18:19], v5 offset:17024\n"
"v_mfma_f32_16x16x32_bf16 v[76:79], a[28:31], a[80:83], v[76:79]\n"
"ds_read_b64_tr_b16 a[20:21], v5 offset:20640\n"
"v_mfma_f32_16x16x32_bf16 v[80:83], a[28:31], a[84:87], v[80:83]\n"
"ds_read_b64_tr_b16 a[22:23], v5 offset:21152\n"
"v_mfma_f32_16x16x32_bf16 v[80:83], a[24:27], a[68:71], v[80:83]\n"
"buffer_load_dwordx4 v26, s[36:39], s93 offen offset:1024 lds\n"
"s_nop 7\n"
"v_mfma_f32_16x16x32_bf16 v[84:87], a[24:27], a[72:75], v[84:87]\n"
"s_waitcnt lgkmcnt(4)\n"
"ds_read_b64_tr_b16 a[24:25], v5 offset:16520\n"
"s_addk_i32 m0, 6208\n"
"v_mfma_f32_16x16x32_bf16 v[84:87], a[28:31], a[88:91], v[84:87]\n"
"s_add_u32 s93, s71, s27\n"
"ds_read_b64_tr_b16 a[26:27], v5 offset:17032\n"
"v_mfma_f32_16x16x32_bf16 v[88:91], a[0:3], a[64:67], v[88:91]\n"
"s_waitcnt vmcnt(12)\n"
"ds_read_b64_tr_b16 a[28:29], v5 offset:20648\n"
"s_waitcnt vmcnt(8)\n"
"v_mfma_f32_16x16x32_bf16 v[88:91], a[4:7], a[80:83], v[88:91]\n"
"buffer_load_dwordx4 v25, s[36:39], s93 offen offset:0 lds\n"
"v_mfma_f32_16x16x32_bf16 v[92:95], a[4:7], a[84:87], v[92:95]\n"
"ds_read_b64_tr_b16 a[30:31], v5 offset:21160\n"
"v_mfma_f32_16x16x32_bf16 v[92:95], a[0:3], a[68:71], v[92:95]\n"
"ds_read_b128 a[96:99], v27 offset:33024\n"
"v_mfma_f32_16x16x32_bf16 v[96:99], a[0:3], a[72:75], v[96:99]\n"
"ds_read_b128 a[112:115], v28 offset:33024\n"
"v_mfma_f32_16x16x32_bf16 v[96:99], a[4:7], a[88:91], v[96:99]\n"
"ds_read_b128 a[100:103], v27 offset:35072\n"
"s_nop 8\n"
"v_mfma_f32_16x16x32_bf16 v[100:103], a[4:7], a[92:95], v[100:103]\n"
"s_memrealtime s[2:3]\n"
"buffer_load_dwordx4 v26, s[36:39], s93 offen offset:1024 lds\n"
"v_mfma_f32_16x16x32_bf16 v[100:103], a[0:3], a[76:79], v[100:103]\n"
"s_addk_i32 m0, 2048\n"
"ds_read_b128 a[116:119], v28 offset:35072\n"
"s_add_u32 s93, s72, s27\n"
"v_mfma_f32_16x16x32_bf16 v[104:107], a[8:11], a[64:67], v[104:107]\n"
"ds_read_b128 a[104:107], v27 offset:41280\n"
"s_movk_i32 s27, 8256\n"
"v_mfma_f32_16x16x32_bf16 v[104:107], a[12:15], a[80:83], v[104:107]\n"
"ds_read_b128 a[120:123], v28 offset:41280\n"
"v_mfma_f32_16x16x32_bf16 v[108:111], a[12:15], a[84:87], v[108:111]\n"
"buffer_load_dwordx4 v25, s[36:39], s93 offen offset:0 lds\n"
"v_mfma_f32_16x16x32_bf16 v[108:111], a[8:11], a[68:71], v[108:111]\n"
"ds_read_b128 a[108:111], v27 offset:43328\n"
"v_mfma_f32_16x16x32_bf16 v[112:115], a[8:11], a[72:75], v[112:115]\n"
"s_waitcnt lgkmcnt(3)\n"
"ds_read_b128 a[124:127], v28 offset:43328\n"
"v_mfma_f32_16x16x32_bf16 v[112:115], a[12:15], a[88:91], v[112:115]\n"
"s_nop 10\n"
"v_mfma_f32_16x16x32_bf16 v[116:119], a[12:15], a[92:95], v[116:119]\n"
"v_mfma_f32_16x16x32_bf16 v[116:119], a[8:11], a[76:79], v[116:119]\n"
"s_waitcnt lgkmcnt(0)\n"
"buffer_load_dwordx4 v26, s[36:39], s93 offen offset:1024 lds\n"
"v_mfma_f32_16x16x32_bf16 v[120:123], a[16:19], a[64:67], v[120:123]\n"
"s_barrier\n"
"v_mfma_f32_16x16x32_bf16 v[120:123], a[20:23], a[80:83], v[120:123]\n"
"s_mov_b32 s79, s2\n"
"ds_read_b64_tr_b16 a[0:1], v2 offset:24768\n"
"s_add_u32 m0, s62, 0\n"
"v_mfma_f32_16x16x32_bf16 v[124:127], a[20:23], a[84:87], v[124:127]\n"
"s_add_u32 s93, s68, 256\n"
"ds_read_b64_tr_b16 a[2:3], v2 offset:25280\n"
"v_mfma_f32_16x16x32_bf16 v[124:127], a[16:19], a[68:71], v[124:127]\n"
"s_mov_b32 s1, 0\n"
"buffer_load_dwordx4 v6, s[40:43], s93 offen offset:0 lds\n"
"v_mfma_f32_16x16x32_bf16 v[128:131], a[16:19], a[72:75], v[128:131]\n"
"ds_read_b64_tr_b16 a[4:5], v2 offset:28896\n"
"v_mfma_f32_16x16x32_bf16 v[128:131], a[20:23], a[88:91], v[128:131]\n"
"ds_read_b64_tr_b16 a[6:7], v2 offset:29408\n"
"s_nop 8\n"
"v_mfma_f32_16x16x32_bf16 v[132:135], a[24:27], a[64:67], v[132:135]\n"
"ds_read_b64_tr_b16 a[8:9], v2 offset:24776\n"
"v_mfma_f32_16x16x32_bf16 v[132:135], a[28:31], a[80:83], v[132:135]\n"
"buffer_load_dwordx4 v7, s[40:43], s93 offen offset:1032 lds\n"
"v_mfma_f32_16x16x32_bf16 v[136:139], a[28:31], a[84:87], v[136:139]\n"
"ds_read_b64_tr_b16 a[10:11], v2 offset:25288\n"
"v_mfma_f32_16x16x32_bf16 v[136:139], a[24:27], a[68:71], v[136:139]\n"
"ds_read_b64_tr_b16 a[12:13], v2 offset:28904\n"
"v_mfma_f32_16x16x32_bf16 v[140:143], a[24:27], a[72:75], v[140:143]\n"
"s_waitcnt lgkmcnt(3)\n"
"ds_read_b64_tr_b16 a[14:15], v2 offset:29416\n"
"v_mfma_f32_16x16x32_bf16 v[140:143], a[28:31], a[88:91], v[140:143]\n"
"ds_read_b64_tr_b16 a[16:17], v3 offset:24768\n"
"v_mfma_f32_16x16x32_bf16 v[144:147], a[0:3], a[64:67], v[144:147]\n"
"buffer_load_dwordx4 v8, s[40:43], s93 offen offset:2064 lds\n"
"v_mfma_f32_16x16x32_bf16 v[144:147], a[4:7], a[80:83], v[144:147]\n"
"ds_read_b64_tr_b16 a[18:19], v3 offset:25280\n"
"v_mfma_f32_16x16x32_bf16 v[148:151], a[4:7], a[84:87], v[148:151]\n"
"ds_read_b64_tr_b16 a[20:21], v3 offset:28896\n"
"v_mfma_f32_16x16x32_bf16 v[148:151], a[0:3], a[68:71], v[148:151]\n"
"ds_read_b64_tr_b16 a[22:23], v3 offset:29408\n"
"v_mfma_f32_16x16x32_bf16 v[152:155], a[0:3], a[72:75], v[152:155]\n"
"buffer_load_dwordx4 v9, s[40:43], s93 offen offset:3096 lds\n"
"v_mfma_f32_16x16x32_bf16 v[152:155], a[4:7], a[88:91], v[152:155]\n"
"ds_read_b64_tr_b16 a[24:25], v3 offset:24776\n"
"v_mfma_f32_16x16x32_bf16 v[156:159], a[4:7], a[92:95], v[156:159]\n"
"s_waitcnt lgkmcnt(4)\n"
"ds_read_b64_tr_b16 a[26:27], v3 offset:25288\n"
"s_add_u32 m0, s62, 57792\n"
"v_mfma_f32_16x16x32_bf16 v[156:159], a[0:3], a[76:79], v[156:159]\n"
"s_add_u32 s93, s69, s28\n"
"ds_read_b64_tr_b16 a[28:29], v3 offset:28904\n"
"v_mfma_f32_16x16x32_bf16 v[160:163], a[8:11], a[64:67], v[160:163]\n"
"ds_read_b64_tr_b16 a[30:31], v3 offset:29416\n"
"v_mfma_f32_16x16x32_bf16 v[160:163], a[12:15], a[80:83], v[160:163]\n"
"buffer_load_dwordx4 v25, s[36:39], s93 offen offset:0 lds\n"
"v_mfma_f32_16x16x32_bf16 v[164:167], a[12:15], a[84:87], v[164:167]\n"
"ds_read_b64_tr_b16 a[0:1], v4 offset:24768\n"
"v_mfma_f32_16x16x32_bf16 v[164:167], a[8:11], a[68:71], v[164:167]\n"
"ds_read_b64_tr_b16 a[2:3], v4 offset:25280\n"
"v_mfma_f32_16x16x32_bf16 v[168:171], a[8:11], a[72:75], v[168:171]\n"
"ds_read_b64_tr_b16 a[4:5], v4 offset:28896\n"
"v_mfma_f32_16x16x32_bf16 v[168:171], a[12:15], a[88:91], v[168:171]\n"
"buffer_load_dwordx4 v26, s[36:39], s93 offen offset:1024 lds\n"
"v_mfma_f32_16x16x32_bf16 v[172:175], a[12:15], a[92:95], v[172:175]\n"
"s_waitcnt lgkmcnt(4)\n"
"ds_read_b64_tr_b16 a[6:7], v4 offset:29408\n"
"s_addk_i32 m0, 2048\n"
"v_mfma_f32_16x16x32_bf16 v[172:175], a[8:11], a[76:79], v[172:175]\n"
"s_add_u32 s93, s70, s28\n"
"ds_read_b64_tr_b16 a[8:9], v4 offset:24776\n"
"v_mfma_f32_16x16x32_bf16 v[176:179], a[16:19], a[64:67], v[176:179]\n"
"ds_read_b64_tr_b16 a[10:11], v4 offset:25288\n"
"v_mfma_f32_16x16x32_bf16 v[176:179], a[20:23], a[80:83], v[176:179]\n"
"buffer_load_dwordx4 v25, s[36:39], s93 offen offset:0 lds\n"
"v_mfma_f32_16x16x32_bf16 v[180:183], a[20:23], a[84:87], v[180:183]\n"
"ds_read_b64_tr_b16 a[12:13], v4 offset:28904\n"
"v_mfma_f32_16x16x32_bf16 v[180:183], a[16:19], a[68:71], v[180:183]\n"
"ds_read_b64_tr_b16 a[14:15], v4 offset:29416\n"
"v_mfma_f32_16x16x32_bf16 v[184:187], a[16:19], a[72:75], v[184:187]\n"
"s_waitcnt lgkmcnt(5)\n"
"ds_read_b64_tr_b16 a[16:17], v5 offset:24768\n"
"v_mfma_f32_16x16x32_bf16 v[184:187], a[20:23], a[88:91], v[184:187]\n"
"ds_read_b64_tr_b16 a[18:19], v5 offset:25280\n"
"v_mfma_f32_16x16x32_bf16 v[188:191], a[24:27], a[64:67], v[188:191]\n"
"buffer_load_dwordx4 v26, s[36:39], s93 offen offset:1024 lds\n"
"v_mfma_f32_16x16x32_bf16 v[188:191], a[28:31], a[80:83], v[188:191]\n"
"s_addk_i32 m0, 6208\n"
"ds_read_b64_tr_b16 a[20:21], v5 offset:28896\n"
"s_add_u32 s93, s71, s28\n"
"v_mfma_f32_16x16x32_bf16 v[192:195], a[28:31], a[84:87], v[192:195]\n"
"s_waitcnt vmcnt(12)\n"
"ds_read_b128 a[128:131], v27 offset:16512\n"
"s_waitcnt vmcnt(8)\n"
"v_mfma_f32_16x16x32_bf16 v[192:195], a[24:27], a[68:71], v[192:195]\n"
"ds_read_b128 a[144:147], v28 offset:16512\n"
"v_mfma_f32_16x16x32_bf16 v[196:199], a[24:27], a[72:75], v[196:199]\n"
"s_waitcnt lgkmcnt(5)\n"
"buffer_load_dwordx4 v25, s[36:39], s93 offen offset:0 lds\n"
"v_mfma_f32_16x16x32_bf16 v[196:199], a[28:31], a[88:91], v[196:199]\n"
"ds_read_b128 a[132:135], v27 offset:18560\n"
"v_mfma_f32_16x16x32_bf16 v[200:203], a[0:3], a[64:67], v[200:203]\n"
"s_memrealtime s[2:3]\n"
"s_memtime vcc\n"
"ds_read_b128 a[148:151], v28 offset:18560\n"
"v_mfma_f32_16x16x32_bf16 v[200:203], a[4:7], a[80:83], v[200:203]\n"
"ds_read_b128 a[136:139], v27 offset:24768\n"
"v_mfma_f32_16x16x32_bf16 v[204:207], a[4:7], a[84:87], v[204:207]\n"
"ds_read_b128 a[152:155], v28 offset:24768\n"
"v_mfma_f32_16x16x32_bf16 v[204:207], a[0:3], a[68:71], v[204:207]\n"
"buffer_load_dwordx4 v26, s[36:39], s93 offen offset:1024 lds\n"
"v_mfma_f32_16x16x32_bf16 v[208:211], a[0:3], a[72:75], v[208:211]\n"
"s_addk_i32 m0, 2048\n"
"ds_read_b128 a[140:143], v27 offset:26816\n"
"s_add_u32 s93, s72, s28\n"
"v_mfma_f32_16x16x32_bf16 v[208:211], a[4:7], a[88:91], v[208:211]\n"
"s_waitcnt lgkmcnt(3)\n"
"ds_read_b128 a[156:159], v28 offset:26816\n"
"v_mfma_f32_16x16x32_bf16 v[212:215], a[4:7], a[92:95], v[212:215]\n"
"ds_read_b64_tr_b16 a[22:23], v5 offset:29408\n"
"v_mfma_f32_16x16x32_bf16 v[212:215], a[0:3], a[76:79], v[212:215]\n"
"buffer_load_dwordx4 v25, s[36:39], s93 offen offset:0 lds\n"
"v_mfma_f32_16x16x32_bf16 v[216:219], a[8:11], a[64:67], v[216:219]\n"
"ds_read_b64_tr_b16 a[24:25], v5 offset:24776\n"
"v_mfma_f32_16x16x32_bf16 v[216:219], a[12:15], a[80:83], v[216:219]\n"
"ds_read_b64_tr_b16 a[26:27], v5 offset:25288\n"
"v_mfma_f32_16x16x32_bf16 v[220:223], a[12:15], a[84:87], v[220:223]\n"
"s_waitcnt lgkmcnt(3)\n"
"ds_read_b64_tr_b16 a[28:29], v5 offset:28904\n"
"v_mfma_f32_16x16x32_bf16 v[220:223], a[8:11], a[68:71], v[220:223]\n"
"ds_read_b64_tr_b16 a[30:31], v5 offset:29416\n"
"v_mfma_f32_16x16x32_bf16 v[224:227], a[8:11], a[72:75], v[224:227]\n"
"buffer_load_dwordx4 v26, s[36:39], s93 offen offset:1024 lds\n"
"v_mfma_f32_16x16x32_bf16 v[224:227], a[12:15], a[88:91], v[224:227]\n"
"s_add_u32 m0, s62, 8256\n"
"s_add_u32 s93, s88, 0\n"
"v_mfma_f32_16x16x32_bf16 v[228:231], a[12:15], a[92:95], v[228:231]\n"
"s_waitcnt lgkmcnt(4)\n"
"v_mfma_f32_16x16x32_bf16 v[228:231], a[8:11], a[76:79], v[228:231]\n"
"s_waitcnt lgkmcnt(0)\n"
"v_mfma_f32_16x16x32_bf16 v[232:235], a[16:19], a[64:67], v[232:235]\n"
"s_barrier\n"
"v_mfma_f32_16x16x32_bf16 v[232:235], a[20:23], a[80:83], v[232:235]\n"
"s_mov_b32 s99, s2\n"
"buffer_load_dwordx4 v6, s[40:43], s93 offen offset:0 lds\n"
"v_mfma_f32_16x16x32_bf16 v[236:239], a[20:23], a[84:87], v[236:239]\n"
"ds_read_b64_tr_b16 a[0:1], v2 offset:33024\n"
"v_mfma_f32_16x16x32_bf16 v[236:239], a[16:19], a[68:71], v[236:239]\n"
"s_mov_b32 s95, vcc_lo\n"
"ds_read_b64_tr_b16 a[2:3], v2 offset:33536\n"
"v_mfma_f32_16x16x32_bf16 v[240:243], a[16:19], a[72:75], v[240:243]\n"
"ds_read_b64_tr_b16 a[4:5], v2 offset:37152\n"
"v_mfma_f32_16x16x32_bf16 v[240:243], a[20:23], a[88:91], v[240:243]\n"
"s_addk_i32 s79, 63\n"
"ds_read_b64_tr_b16 a[6:7], v2 offset:37664\n"
"v_mfma_f32_16x16x32_bf16 v[244:247], a[24:27], a[64:67], v[244:247]\n"
"buffer_load_dwordx4 v7, s[40:43], s93 offen offset:1032 lds\n"
"v_mfma_f32_16x16x32_bf16 v[244:247], a[28:31], a[80:83], v[244:247]\n"
"s_sub_u32 s0, s79, s2\n"
"ds_read_b64_tr_b16 a[8:9], v2 offset:33032\n"
"v_mfma_f32_16x16x32_bf16 v[248:251], a[28:31], a[84:87], v[248:251]\n"
"ds_read_b64_tr_b16 a[10:11], v2 offset:33544\n"
"v_mfma_f32_16x16x32_bf16 v[248:251], a[24:27], a[68:71], v[248:251]\n"
"s_sub_u32 s0, s0, s23\n"
"buffer_load_dwordx4 v8, s[40:43], s93 offen offset:2064 lds\n"
"v_mfma_f32_16x16x32_bf16 v[252:255], a[24:27], a[72:75], v[252:255]\n"
"s_waitcnt lgkmcnt(2)\n"
"ds_read_b64_tr_b16 a[12:13], v2 offset:37160\n"
"v_mfma_f32_16x16x32_bf16 v[252:255], a[28:31], a[88:91], v[252:255]\n"
"s_max_i32 s0, s0, 0\n"
"ds_read_b64_tr_b16 a[14:15], v2 offset:37672\n"
"v_mfma_f32_16x16x32_bf16 v[32:35], a[0:3], a[96:99], v[32:35]\n"
"ds_read_b64_tr_b16 a[16:17], v3 offset:33024\n"
"v_mfma_f32_16x16x32_bf16 v[32:35], a[4:7], a[112:115], v[32:35]\n"
"s_add_u32 s79, s2, s0\n"
"buffer_load_dwordx4 v9, s[40:43], s93 offen offset:3096 lds\n"
"v_mfma_f32_16x16x32_bf16 v[36:39], a[4:7], a[116:119], v[36:39]\n"
"s_addk_i32 m0, 8256\n"
"ds_read_b64_tr_b16 a[18:19], v3 offset:33536\n"
"s_addk_i32 s93, 256\n"
"v_mfma_f32_16x16x32_bf16 v[36:39], a[0:3], a[100:103], v[36:39]\n"
"s_lshr_b64 vcc, s[0:1], 2\n"
"ds_read_b64_tr_b16 a[20:21], v3 offset:37152\n"
"v_mfma_f32_16x16x32_bf16 v[40:43], a[0:3], a[104:107], v[40:43]\n"
"ds_read_b64_tr_b16 a[22:23], v3 offset:37664\n"
"v_mfma_f32_16x16x32_bf16 v[40:43], a[4:7], a[120:123], v[40:43]\n"
"s_cbranch_vccnz PC4_%=\n"
"PB4_%=:\n"
"v_mfma_f32_16x16x32_bf16 v[44:47], a[4:7], a[124:127], v[44:47]\n"
"s_waitcnt lgkmcnt(4)\n"
"buffer_load_dwordx4 v6, s[40:43], s93 offen offset:0 lds\n"
"v_mfma_f32_16x16x32_bf16 v[44:47], a[0:3], a[108:111], v[44:47]\n"
"ds_read_b64_tr_b16 a[24:25], v3 offset:33032\n"
"v_mfma_f32_16x16x32_bf16 v[48:51], a[8:11], a[96:99], v[48:51]\n"
"ds_read_b64_tr_b16 a[26:27], v3 offset:33544\n"
"v_mfma_f32_16x16x32_bf16 v[48:51], a[12:15], a[112:115], v[48:51]\n"
"ds_read_b64_tr_b16 a[28:29], v3 offset:37160\n"
"v_mfma_f32_16x16x32_bf16 v[52:55], a[12:15], a[116:119], v[52:55]\n"
"buffer_load_dwordx4 v7, s[40:43], s93 offen offset:1032 lds\n"
"v_mfma_f32_16x16x32_bf16 v[52:55], a[8:11], a[100:103], v[52:55]\n"
"ds_read_b64_tr_b16 a[30:31], v3 offset:37672\n"
"v_mfma_f32_16x16x32_bf16 v[56:59], a[8:11], a[104:107], v[56:59]\n"
"ds_read_b64_tr_b16 a[0:1], v4 offset:33024\n"
"v_mfma_f32_16x16x32_bf16 v[56:59], a[12:15], a[120:123], v[56:59]\n"
"ds_read_b64_tr_b16 a[2:3], v4 offset:33536\n"
"v_mfma_f32_16x16x32_bf16 v[60:63], a[12:15], a[124:127], v[60:63]\n"
"s_waitcnt lgkmcnt(5)\n"
"buffer_load_dwordx4 v8, s[40:43], s93 offen offset:2064 lds\n"
"v_mfma_f32_16x16x32_bf16 v[60:63], a[8:11], a[108:111], v[60:63]\n"
"ds_read_b64_tr_b16 a[4:5], v4 offset:37152\n"
"v_mfma_f32_16x16x32_bf16 v[64:67], a[16:19], a[96:99], v[64:67]\n"
"ds_read_b64_tr_b16 a[6:7], v4 offset:37664\n"
"v_mfma_f32_16x16x32_bf16 v[64:67], a[20:23], a[112:115], v[64:67]\n"
"ds_read_b64_tr_b16 a[8:9], v4 offset:33032\n"
"v_mfma_f32_16x16x32_bf16 v[68:71], a[20:23], a[116:119], v[68:71]\n"
"ds_read_b64_tr_b16 a[10:11], v4 offset:33544\n"
"v_mfma_f32_16x16x32_bf16 v[68:71], a[16:19], a[100:103], v[68:71]\n"
"buffer_load_dwordx4 v9, s[40:43], s93 offen offset:3096 lds\n"
"v_mfma_f32_16x16x32_bf16 v[72:75], a[16:19], a[104:107], v[72:75]\n"
"s_waitcnt lgkmcnt(4)\n"
"ds_read_b64_tr_b16 a[12:13], v4 offset:37160\n"
"s_addk_i32 m0, 24768\n"
"v_mfma_f32_16x16x32_bf16 v[72:75], a[20:23], a[120:123], v[72:75]\n"
"s_add_u32 s93, s69, s29\n"
"ds_read_b64_tr_b16 a[14:15], v4 offset:37672\n"
"s_waitcnt vmcnt(12)\n"
"v_mfma_f32_16x16x32_bf16 v[76:79], a[24:27], a[96:99], v[76:79]\n"
"s_waitcnt vmcnt(8)\n"
"ds_read_b128 a[160:163], v27 offset:33024\n"
"v_mfma_f32_16x16x32_bf16 v[76:79], a[28:31], a[112:115], v[76:79]\n"
"buffer_load_dwordx4 v25, s[36:39], s93 offen offset:0 lds\n"
"v_mfma_f32_16x16x32_bf16 v[80:83], a[28:31], a[116:119], v[80:83]\n"
"ds_read_b128 a[176:179], v28 offset:33024\n"
"v_mfma_f32_16x16x32_bf16 v[80:83], a[24:27], a[100:103], v[80:83]\n"
"ds_read_b128 a[164:167], v27 offset:35072\n"
"v_mfma_f32_16x16x32_bf16 v[84:87], a[24:27], a[104:107], v[84:87]\n"
"s_waitcnt lgkmcnt(5)\n"
"ds_read_b128 a[180:183], v28 offset:35072\n"
"v_mfma_f32_16x16x32_bf16 v[84:87], a[28:31], a[120:123], v[84:87]\n"
"buffer_load_dwordx4 v26, s[36:39], s93 offen offset:1024 lds\n"
"v_mfma_f32_16x16x32_bf16 v[88:91], a[0:3], a[96:99], v[88:91]\n"
"ds_read_b128 a[168:171], v27 offset:41280\n"
"v_mfma_f32_16x16x32_bf16 v[88:91], a[4:7], a[112:115], v[88:91]\n"
"s_addk_i32 m0, 2048\n"
"ds_read_b128 a[184:187], v28 offset:41280\n"
"s_add_u32 s93, s70, s29\n"
"v_mfma_f32_16x16x32_bf16 v[92:95], a[4:7], a[116:119], v[92:95]\n"
"ds_read_b128 a[172:175], v27 offset:43328\n"
"v_mfma_f32_16x16x32_bf16 v[92:95], a[0:3], a[100:103], v[92:95]\n"
"s_waitcnt lgkmcnt(3)\n"
"ds_read_b128 a[188:191], v28 offset:43328\n"
"v_mfma_f32_16x16x32_bf16 v[96:99], a[0:3], a[104:107], v[96:99]\n"
"buffer_load_dwordx4 v25, s[36:39], s93 offen offset:0 lds\n"
"v_mfma_f32_16x16x32_bf16 v[96:99], a[4:7], a[120:123], v[96:99]\n"
"ds_read_b64_tr_b16 a[16:17], v5 offset:33024\n"
"v_mfma_f32_16x16x32_bf16 v[100:103], a[4:7], a[124:127], v[100:103]\n"
"ds_read_b64_tr_b16 a[18:19], v5 offset:33536\n"
"v_mfma_f32_16x16x32_bf16 v[100:103], a[0:3], a[108:111], v[100:103]\n"
"ds_read_b64_tr_b16 a[20:21], v5 offset:37152\n"
"v_mfma_f32_16x16x32_bf16 v[104:107], a[8:11], a[96:99], v[104:107]\n"
"s_memrealtime s[2:3]\n"
"ds_read_b64_tr_b16 a[22:23], v5 offset:37664\n"
"s_waitcnt lgkmcnt(4)\n"
"v_mfma_f32_16x16x32_bf16 v[104:107], a[12:15], a[112:115], v[104:107]\n"
"buffer_load_dwordx4 v26, s[36:39], s93 offen offset:1024 lds\n"
"v_mfma_f32_16x16x32_bf16 v[108:111], a[12:15], a[116:119], v[108:111]\n"
"s_addk_i32 m0, 6208\n"
"ds_read_b64_tr_b16 a[24:25], v5 offset:33032\n"
"s_add_u32 s93, s71, s29\n"
"v_mfma_f32_16x16x32_bf16 v[108:111], a[8:11], a[100:103], v[108:111]\n"
"ds_read_b64_tr_b16 a[26:27], v5 offset:33544\n"
"v_mfma_f32_16x16x32_bf16 v[112:115], a[8:11], a[104:107], v[112:115]\n"
"ds_read_b64_tr_b16 a[28:29], v5 offset:37160\n"
"v_mfma_f32_16x16x32_bf16 v[112:115], a[12:15], a[120:123], v[112:115]\n"
"ds_read_b64_tr_b16 a[30:31], v5 offset:37672\n"
"v_mfma_f32_16x16x32_bf16 v[116:119], a[12:15], a[124:127], v[116:119]\n"
"s_waitcnt lgkmcnt(4)\n"
"buffer_load_dwordx4 v25, s[36:39], s93 offen offset:0 lds\n"
"v_mfma_f32_16x16x32_bf16 v[116:119], a[8:11], a[108:111], v[116:119]\n"
"s_waitcnt lgkmcnt(0)\n"
"v_mfma_f32_16x16x32_bf16 v[120:123], a[16:19], a[96:99], v[120:123]\n"
"s_barrier\n"
"v_mfma_f32_16x16x32_bf16 v[120:123], a[20:23], a[112:115], v[120:123]\n"
"s_addk_i32 s79, 63\n"
"buffer_load_dwordx4 v26, s[36:39], s93 offen offset:1024 lds\n"
"v_mfma_f32_16x16x32_bf16 v[124:127], a[20:23], a[116:119], v[124:127]\n"
"s_addk_i32 m0, 2048\n"
"ds_read_b64_tr_b16 a[0:1], v2 offset:0\n"
"s_add_u32 s93, s72, s29\n"
"v_mfma_f32_16x16x32_bf16 v[124:127], a[16:19], a[100:103], v[124:127]\n"
"s_sub_u32 s0, s79, s2\n"
"ds_read_b64_tr_b16 a[2:3], v2 offset:512\n"
"v_mfma_f32_16x16x32_bf16 v[128:131], a[16:19], a[104:107], v[128:131]\n"
"ds_read_b64_tr_b16 a[4:5], v2 offset:4128\n"
"v_mfma_f32_16x16x32_bf16 v[128:131], a[20:23], a[120:123], v[128:131]\n"
"s_sub_u32 s0, s0, s23\n"
"ds_read_b64_tr_b16 a[6:7], v2 offset:4640\n"
"v_mfma_f32_16x16x32_bf16 v[132:135], a[24:27], a[96:99], v[132:135]\n"
"buffer_load_dwordx4 v25, s[36:39], s93 offen offset:0 lds\n"
"v_mfma_f32_16x16x32_bf16 v[132:135], a[28:31], a[112:115], v[132:135]\n"
"s_max_i32 s0, s0, 0\n"
"ds_read_b64_tr_b16 a[8:9], v2 offset:8\n"
"v_mfma_f32_16x16x32_bf16 v[136:139], a[28:31], a[116:119], v[136:139]\n"
"ds_read_b64_tr_b16 a[10:11], v2 offset:520\n"
"v_mfma_f32_16x16x32_bf16 v[136:139], a[24:27], a[100:103], v[136:139]\n"
"s_add_u32 s79, s2, s0\n"
"ds_read_b64_tr_b16 a[12:13], v2 offset:4136\n"
"v_mfma_f32_16x16x32_bf16 v[140:143], a[24:27], a[104:107], v[140:143]\n"
"s_waitcnt lgkmcnt(3)\n"
"buffer_load_dwordx4 v26, s[36:39], s93 offen offset:1024 lds\n"
"v_mfma_f32_16x16x32_bf16 v[140:143], a[28:31], a[120:123], v[140:143]\n"
"s_addk_i32 m0, -26816\n"
"ds_read_b64_tr_b16 a[14:15], v2 offset:4648\n"
"s_add_u32 s93, s89, 0\n"
"v_mfma_f32_16x16x32_bf16 v[144:147], a[0:3], a[96:99], v[144:147]\n"
"s_lshr_b64 vcc, s[0:1], 2\n"
"ds_read_b64_tr_b16 a[16:17], v3 offset:0\n"
"v_mfma_f32_16x16x32_bf16 v[144:147], a[4:7], a[112:115], v[144:147]\n"
"ds_read_b64_tr_b16 a[18:19], v3 offset:512\n"
"v_mfma_f32_16x16x32_bf16 v[148:151], a[4:7], a[116:119], v[148:151]\n"
"s_cbranch_vccnz PC5_%=\n"
"PB5_%=:\n"
"v_mfma_f32_16x16x32_bf16 v[148:151], a[0:3], a[100:103], v[148:151]\n"
"buffer_load_dwordx4 v6, s[40:43], s93 offen offset:0 lds\n"
"v_mfma_f32_16x16x32_bf16 v[152:155], a[0:3], a[104:107], v[152:155]\n"
"ds_read_b64_tr_b16 a[20:21], v3 offset:4128\n"
"v_mfma_f32_16x16x32_bf16 v[152:155], a[4:7], a[120:123], v[152:155]\n"
"ds_read_b64_tr_b16 a[22:23], v3 offset:4640\n"
"v_mfma_f32_16x16x32_bf16 v[156:159], a[4:7], a[124:127], v[156:159]\n"
"s_waitcnt lgkmcnt(4)\n"
"ds_read_b64_tr_b16 a[24:25], v3 offset:8\n"
"v_mfma_f32_16x16x32_bf16 v[156:159], a[0:3], a[108:111], v[156:159]\n"
"buffer_load_dwordx4 v7, s[40:43], s93 offen offset:1032 lds\n"
"v_mfma_f32_16x16x32_bf16 v[160:163], a[8:11], a[96:99], v[160:163]\n"
"ds_read_b64_tr_b16 a[26:27], v3 offset:520\n"
"v_mfma_f32_16x16x32_bf16 v[160:163], a[12:15], a[112:115], v[160:163]\n"
"ds_read_b64_tr_b16 a[28:29], v3 offset:4136\n"
"v_mfma_f32_16x16x32_bf16 v[164:167], a[12:15], a[116:119], v[164:167]\n"
"ds_read_b64_tr_b16 a[30:31], v3 offset:4648\n"
"v_mfma_f32_16x16x32_bf16 v[164:167], a[8:11], a[100:103], v[164:167]\n"
"buffer_load_dwordx4 v8, s[40:43], s93 offen offset:2064 lds\n"
"v_mfma_f32_16x16x32_bf16 v[168:171], a[8:11], a[104:107], v[168:171]\n"
"ds_read_b64_tr_b16 a[0:1], v4 offset:0\n"
"v_mfma_f32_16x16x32_bf16 v[168:171], a[12:15], a[120:123], v[168:171]\n"
"ds_read_b64_tr_b16 a[2:3], v4 offset:512\n"
"v_mfma_f32_16x16x32_bf16 v[172:175], a[12:15], a[124:127], v[172:175]\n"
"s_waitcnt lgkmcnt(5)\n"
"ds_read_b64_tr_b16 a[4:5], v4 offset:4128\n"
"v_mfma_f32_16x16x32_bf16 v[172:175], a[8:11], a[108:111], v[172:175]\n"
"buffer_load_dwordx4 v9, s[40:43], s93 offen offset:3096 lds\n"
"v_mfma_f32_16x16x32_bf16 v[176:179], a[16:19], a[96:99], v[176:179]\n"
"ds_read_b64_tr_b16 a[6:7], v4 offset:4640\n"
"v_mfma_f32_16x16x32_bf16 v[176:179], a[20:23], a[112:115], v[176:179]\n"
"s_add_u32 m0, s62, 57792\n"
"ds_read_b64_tr_b16 a[8:9], v4 offset:8\n"
"s_add_u32 s93, s69, s30\n"
"v_mfma_f32_16x16x32_bf16 v[180:183], a[20:23], a[116:119], v[180:183]\n"
"ds_read_b64_tr_b16 a[10:11], v4 offset:520\n"
"v_mfma_f32_16x16x32_bf16 v[180:183], a[16:19], a[100:103], v[180:183]\n"
"ds_read_b64_tr_b16 a[12:13], v4 offset:4136\n"
"v_mfma_f32_16x16x32_bf16 v[184:187], a[16:19], a[104:107], v[184:187]\n"
"s_waitcnt lgkmcnt(5)\n"
"buffer_load_dwordx4 v25, s[36:39], s93 offen offset:0 lds\n"
"v_mfma_f32_16x16x32_bf16 v[184:187], a[20:23], a[120:123], v[184:187]\n"
"ds_read_b64_tr_b16 a[14:15], v4 offset:4648\n"
"v_mfma_f32_16x16x32_bf16 v[188:191], a[24:27], a[96:99], v[188:191]\n"
"ds_read_b64_tr_b16 a[16:17], v5 offset:0\n"
"v_mfma_f32_16x16x32_bf16 v[188:191], a[28:31], a[112:115], v[188:191]\n"
"ds_read_b64_tr_b16 a[18:19], v5 offset:512\n"
"v_mfma_f32_16x16x32_bf16 v[192:195], a[28:31], a[116:119], v[192:195]\n"
"buffer_load_dwordx4 v26, s[36:39], s93 offen offset:1024 lds\n"
"v_mfma_f32_16x16x32_bf16 v[192:195], a[24:27], a[100:103], v[192:195]\n"
"s_addk_i32 m0, 2048\n"
"ds_read_b64_tr_b16 a[20:21], v5 offset:4128\n"
"s_add_u32 s93, s70, s30\n"
"v_mfma_f32_16x16x32_bf16 v[196:199], a[24:27], a[104:107], v[196:199]\n"
"s_waitcnt lgkmcnt(4)\n"
"ds_read_b64_tr_b16 a[22:23], v5 offset:4640\n"
"v_mfma_f32_16x16x32_bf16 v[196:199], a[28:31], a[120:123], v[196:199]\n"
"ds_read_b64_tr_b16 a[24:25], v5 offset:8\n"
"v_mfma_f32_16x16x32_bf16 v[200:203], a[0:3], a[96:99], v[200:203]\n"
"buffer_load_dwordx4 v25, s[36:39], s93 offen offset:0 lds\n"
"v_mfma_f32_16x16x32_bf16 v[200:203], a[4:7], a[112:115], v[200:203]\n"
"ds_read_b64_tr_b16 a[26:27], v5 offset:520\n"
"v_mfma_f32_16x16x32_bf16 v[204:207], a[4:7], a[116:119], v[204:207]\n"
"s_waitcnt vmcnt(11)\n"
"ds_read_b64_tr_b16 a[28:29], v5 offset:4136\n"
"s_waitcnt vmcnt(7)\n"
"v_mfma_f32_16x16x32_bf16 v[204:207], a[0:3], a[100:103], v[204:207]\n"
"ds_read_b64_tr_b16 a[30:31], v5 offset:4648\n"
"v_mfma_f32_16x16x32_bf16 v[208:211], a[0:3], a[104:107], v[208:211]\n"
"ds_read_b128 a[192:195], v27 offset:16512\n"
"v_mfma_f32_16x16x32_bf16 v[208:211], a[4:7], a[120:123], v[208:211]\n"
"buffer_load_dwordx4 v26, s[36:39], s93 offen offset:1024 lds\n"
"v_mfma_f32_16x16x32_bf16 v[212:215], a[4:7], a[124:127], v[212:215]\n"
"s_waitcnt lgkmcnt(4)\n"
"ds_read_b128 a[208:211], v28 offset:16512\n"
"s_addk_i32 m0, 6208\n"
"v_mfma_f32_16x16x32_bf16 v[212:215], a[0:3], a[108:111], v[212:215]\n"
"s_add_u32 s93, s71, s30\n"
"ds_read_b128 a[196:199], v27 offset:18560\n"
"s_add_u32 s71, s20, -4\n"
"v_mfma_f32_16x16x32_bf16 v[216:219], a[8:11], a[96:99], v[216:219]\n"
"s_memrealtime s[2:3]\n"
"ds_read_b128 a[212:215], v28 offset:18560\n"
"s_and_b32 s71, s71, 15\n"
"v_mfma_f32_16x16x32_bf16 v[216:219], a[12:15], a[112:115], v[216:219]\n"
"s_lshl_b32 s71, s71, 2\n"
"buffer_load_dwordx4 v25, s[36:39], s93 offen offset:0 lds\n"
"v_mfma_f32_16x16x32_bf16 v[220:223], a[12:15], a[116:119], v[220:223]\n"
"s_add_u32 s71, s71, s60\n"
"ds_read_b128 a[200:203], v27 offset:24768\n"
"s_mul_i32 s71, s71, 15360\n"
"v_mfma_f32_16x16x32_bf16 v[220:223], a[8:11], a[100:103], v[220:223]\n"
"ds_read_b128 a[216:219], v28 offset:24768\n"
"v_mfma_f32_16x16x32_bf16 v[224:227], a[8:11], a[104:107], v[224:227]\n"
"ds_read_b128 a[204:207], v27 offset:26816\n"
"v_mfma_f32_16x16x32_bf16 v[224:227], a[12:15], a[120:123], v[224:227]\n"
"s_waitcnt lgkmcnt(3)\n"
"ds_read_b128 a[220:223], v28 offset:26816\n"
"v_mfma_f32_16x16x32_bf16 v[228:231], a[12:15], a[124:127], v[228:231]\n"
"buffer_load_dwordx4 v26, s[36:39], s93 offen offset:1024 lds\n"
"v_mfma_f32_16x16x32_bf16 v[228:231], a[8:11], a[108:111], v[228:231]\n"
"s_waitcnt lgkmcnt(0)\n"
"s_addk_i32 m0, 2048\n"
"s_add_u32 s93, s72, s30\n"
"v_mfma_f32_16x16x32_bf16 v[232:235], a[16:19], a[96:99], v[232:235]\n"
"s_barrier\n"
"v_mfma_f32_16x16x32_bf16 v[232:235], a[20:23], a[112:115], v[232:235]\n"
"s_addk_i32 s79, 63\n"
"ds_read_b64_tr_b16 a[0:1], v2 offset:8256\n"
"s_add_u32 s72, s20, -8\n"
"v_mfma_f32_16x16x32_bf16 v[236:239], a[20:23], a[116:119], v[236:239]\n"
"buffer_load_dwordx4 v25, s[36:39], s93 offen offset:0 lds\n"
"v_mfma_f32_16x16x32_bf16 v[236:239], a[16:19], a[100:103], v[236:239]\n"
"s_sub_u32 s0, s79, s2\n"
"ds_read_b64_tr_b16 a[2:3], v2 offset:8768\n"
"s_and_b32 s72, s72, 15\n"
"v_mfma_f32_16x16x32_bf16 v[240:243], a[16:19], a[104:107], v[240:243]\n"
"ds_read_b64_tr_b16 a[4:5], v2 offset:12384\n"
"s_lshl_b32 s72, s72, 2\n"
"v_mfma_f32_16x16x32_bf16 v[240:243], a[20:23], a[120:123], v[240:243]\n"
"s_sub_u32 s0, s0, s23\n"
"ds_read_b64_tr_b16 a[6:7], v2 offset:12896\n"
"s_add_u32 s72, s72, s60\n"
"v_mfma_f32_16x16x32_bf16 v[244:247], a[24:27], a[96:99], v[244:247]\n"
"s_mul_i32 s72, s72, 15360\n"
"buffer_load_dwordx4 v26, s[36:39], s93 offen offset:1024 lds\n"
"v_mfma_f32_16x16x32_bf16 v[244:247], a[28:31], a[112:115], v[244:247]\n"
"s_max_i32 s0, s0, 0\n"
"ds_read_b64_tr_b16 a[8:9], v2 offset:8264\n"
"v_mfma_f32_16x16x32_bf16 v[248:251], a[28:31], a[116:119], v[248:251]\n"
"s_add_u32 m0, s62, 33024\n"
"ds_read_b64_tr_b16 a[10:11], v2 offset:8776\n"
"s_add_u32 s93, s91, 0\n"
"v_mfma_f32_16x16x32_bf16 v[248:251], a[24:27], a[100:103], v[248:251]\n"
"s_add_u32 s79, s2, s0\n"
"ds_read_b64_tr_b16 a[12:13], v2 offset:12392\n"
"v_mfma_f32_16x16x32_bf16 v[252:255], a[24:27], a[104:107], v[252:255]\n"
"s_waitcnt lgkmcnt(3)\n"
"ds_read_b64_tr_b16 a[14:15], v2 offset:12904\n"
"v_mfma_f32_16x16x32_bf16 v[252:255], a[28:31], a[120:123], v[252:255]\n"
"s_lshr_b64 vcc, s[0:1], 2\n"
"buffer_load_dwordx4 v6, s[40:43], s93 offen offset:0 lds\n"
"v_mfma_f32_16x16x32_bf16 v[32:35], a[0:3], a[128:131], v[32:35]\n"
"ds_read_b64_tr_b16 a[16:17], v3 offset:8256\n"
"v_mfma_f32_16x16x32_bf16 v[32:35], a[4:7], a[144:147], v[32:35]\n"
"s_add_u32 m0, s63, 41280\n"
"ds_read_b64_tr_b16 a[18:19], v3 offset:8768\n"
"s_add_u32 s81, s64, 0\n"
"v_mfma_f32_16x16x32_bf16 v[36:39], a[4:7], a[148:151], v[36:39]\n"
"s_cbranch_vccnz PC6_%=\n"
"PB6_%=:\n"
"v_mfma_f32_16x16x32_bf16 v[36:39], a[0:3], a[132:135], v[36:39]\n"
"ds_read_b64_tr_b16 a[20:21], v3 offset:12384\n"
"v_mfma_f32_16x16x32_bf16 v[40:43], a[0:3], a[136:139], v[40:43]\n"
"buffer_load_dwordx4 v11, s[44:47], s81 offen offset:0 nt lds\n"
"v_mfma_f32_16x16x32_bf16 v[40:43], a[4:7], a[152:155], v[40:43]\n"
"s_add_u32 m0, s62, 33024\n"
"ds_read_b64_tr_b16 a[22:23], v3 offset:12896\n"
"v_mfma_f32_16x16x32_bf16 v[44:47], a[4:7], a[156:159], v[44:47]\n"
"s_waitcnt lgkmcnt(4)\n"
"ds_read_b64_tr_b16 a[24:25], v3 offset:8264\n"
"v_mfma_f32_16x16x32_bf16 v[44:47], a[0:3], a[140:143], v[44:47]\n"
"ds_read_b64_tr_b16 a[26:27], v3 offset:8776\n"
"v_mfma_f32_16x16x32_bf16 v[48:51], a[8:11], a[128:131], v[48:51]\n"
"buffer_load_dwordx4 v7, s[40:43], s93 offen offset:1032 lds\n"
"v_mfma_f32_16x16x32_bf16 v[48:51], a[12:15], a[144:147], v[48:51]\n"
"ds_read_b64_tr_b16 a[28:29], v3 offset:12392\n"
"v_mfma_f32_16x16x32_bf16 v[52:55], a[12:15], a[148:151], v[52:55]\n"
"ds_read_b64_tr_b16 a[30:31], v3 offset:12904\n"
"v_mfma_f32_16x16x32_bf16 v[52:55], a[8:11], a[132:135], v[52:55]\n"
"ds_read_b64_tr_b16 a[0:1], v4 offset:8256\n"
"v_mfma_f32_16x16x32_bf16 v[56:59], a[8:11], a[136:139], v[56:59]\n"
"buffer_load_dwordx4 v8, s[40:43], s93 offen offset:2064 lds\n"
"v_mfma_f32_16x16x32_bf16 v[56:59], a[12:15], a[152:155], v[56:59]\n"
"ds_read_b64_tr_b16 a[2:3], v4 offset:8768\n"
"v_mfma_f32_16x16x32_bf16 v[60:63], a[12:15], a[156:159], v[60:63]\n"
"s_waitcnt lgkmcnt(4)\n"
"ds_read_b64_tr_b16 a[4:5], v4 offset:12384\n"
"s_add_u32 m0, s63, 41280\n"
"v_mfma_f32_16x16x32_bf16 v[60:63], a[8:11], a[140:143], v[60:63]\n"
"ds_read_b64_tr_b16 a[6:7], v4 offset:12896\n"
"v_mfma_f32_16x16x32_bf16 v[64:67], a[16:19], a[128:131], v[64:67]\n"
"ds_read_b64_tr_b16 a[8:9], v4 offset:8264\n"
"v_mfma_f32_16x16x32_bf16 v[64:67], a[20:23], a[144:147], v[64:67]\n"
"buffer_load_dwordx4 v12, s[44:47], s81 offen offset:1024 nt lds\n"
"v_mfma_f32_16x16x32_bf16 v[68:71], a[20:23], a[148:151], v[68:71]\n"
"PG2_%=:\n"
"s_cbranch_execz PG6_%=\n"
"s_add_u32 m0, s62, 33024\n"
"ds_read_b64_tr_b16 a[10:11], v4 offset:8776\n"
"v_mfma_f32_16x16x32_bf16 v[68:71], a[16:19], a[132:135], v[68:71]\n"
"ds_read_b64_tr_b16 a[12:13], v4 offset:12392\n"
"v_mfma_f32_16x16x32_bf16 v[72:75], a[16:19], a[136:139], v[72:75]\n"
"s_waitcnt lgkmcnt(5)\n"
"ds_read_b64_tr_b16 a[14:15], v4 offset:12904\n"
"v_mfma_f32_16x16x32_bf16 v[72:75], a[20:23], a[152:155], v[72:75]\n"
"buffer_load_dwordx4 v9, s[40:43], s93 offen offset:3096 lds\n"
"v_mfma_f32_16x16x32_bf16 v[76:79], a[24:27], a[128:131], v[76:79]\n"
"ds_read_b64_tr_b16 a[16:17], v5 offset:8256\n"
"v_mfma_f32_16x16x32_bf16 v[76:79], a[28:31], a[144:147], v[76:79]\n"
"s_add_u32 m0, s62, 0\n"
"ds_read_b64_tr_b16 a[18:19], v5 offset:8768\n"
"s_add_u32 s93, s94, 128\n"
"v_mfma_f32_16x16x32_bf16 v[80:83], a[28:31], a[148:151], v[80:83]\n"
"ds_read_b64_tr_b16 a[20:21], v5 offset:12384\n"
"v_mfma_f32_16x16x32_bf16 v[80:83], a[24:27], a[132:135], v[80:83]\n"
"ds_read_b64_tr_b16 a[22:23], v5 offset:12896\n"
"v_mfma_f32_16x16x32_bf16 v[84:87], a[24:27], a[136:139], v[84:87]\n"
"s_waitcnt lgkmcnt(5)\n"
"buffer_load_dwordx4 v6, s[40:43], s93 offen offset:0 lds\n"
"v_mfma_f32_16x16x32_bf16 v[84:87], a[28:31], a[152:155], v[84:87]\n"
"s_add_u32 m0, s63, 41280\n"
"ds_read_b64_tr_b16 a[24:25], v5 offset:8264\n"
"v_mfma_f32_16x16x32_bf16 v[88:91], a[0:3], a[128:131], v[88:91]\n"
"ds_read_b64_tr_b16 a[26:27], v5 offset:8776\n"
"v_mfma_f32_16x16x32_bf16 v[88:91], a[4:7], a[144:147], v[88:91]\n"
"ds_read_b64_tr_b16 a[28:29], v5 offset:12392\n"
"v_mfma_f32_16x16x32_bf16 v[92:95], a[4:7], a[148:151], v[92:95]\n"
"buffer_load_dwordx4 v13, s[44:47], s81 offen offset:2048 nt lds\n"
"v_mfma_f32_16x16x32_bf16 v[92:95], a[0:3], a[132:135], v[92:95]\n"
"ds_read_b64_tr_b16 a[30:31], v5 offset:12904\n"
"v_mfma_f32_16x16x32_bf16 v[96:99], a[0:3], a[136:139], v[96:99]\n"
"s_add_u32 m0, s62, 0\n"
"v_mfma_f32_16x16x32_bf16 v[96:99], a[4:7], a[152:155], v[96:99]\n"
"v_mfma_f32_16x16x32_bf16 v[100:103], a[4:7], a[156:159], v[100:103]\n"
"s_waitcnt lgkmcnt(3)\n"
"s_waitcnt vmcnt(12)\n"
"s_waitcnt vmcnt(8)\n"
"v_mfma_f32_16x16x32_bf16 v[100:103], a[0:3], a[140:143], v[100:103]\n"
"buffer_load_dwordx4 v7, s[40:43], s93 offen offset:1032 lds\n"
"v_mfma_f32_16x16x32_bf16 v[104:107], a[8:11], a[128:131], v[104:107]\n"
"s_memrealtime s[2:3]\n"
"s_memtime vcc\n"
"ds_read_b128 a[224:227], v27 offset:33024\n"
"v_mfma_f32_16x16x32_bf16 v[104:107], a[12:15], a[144:147], v[104:107]\n"
"ds_read_b128 a[240:243], v28 offset:33024\n"
"v_mfma_f32_16x16x32_bf16 v[108:111], a[12:15], a[148:151], v[108:111]\n"
"ds_read_b128 a[228:231], v27 offset:35072\n"
"v_mfma_f32_16x16x32_bf16 v[108:111], a[8:11], a[132:135], v[108:111]\n"
"buffer_load_dwordx4 v8, s[40:43], s93 offen offset:2064 lds\n"
"v_mfma_f32_16x16x32_bf16 v[112:115], a[8:11], a[136:139], v[112:115]\n"
"s_add_u32 m0, s63, 41280\n"
"ds_read_b128 a[244:247], v28 offset:35072\n"
"v_mfma_f32_16x16x32_bf16 v[112:115], a[12:15], a[152:155], v[112:115]\n"
"ds_read_b128 a[232:235], v27 offset:41280\n"
"v_mfma_f32_16x16x32_bf16 v[116:119], a[12:15], a[156:159], v[116:119]\n"
"ds_read_b128 a[248:251], v28 offset:41280\n"
"v_mfma_f32_16x16x32_bf16 v[116:119], a[8:11], a[140:143], v[116:119]\n"
"s_waitcnt lgkmcnt(0)\n"
"buffer_load_dwordx4 v14, s[44:47], s81 offen offset:3072 nt lds\n"
"v_mfma_f32_16x16x32_bf16 v[120:123], a[16:19], a[128:131], v[120:123]\n"
"s_barrier\n"
"v_mfma_f32_16x16x32_bf16 v[120:123], a[20:23], a[144:147], v[120:123]\n"
"s_add_u32 m0, s62, 0\n"
"ds_read_b128 a[236:239], v27 offset:43328\n"
"s_sub_u32 s99, s2, s99\n"
"v_mfma_f32_16x16x32_bf16 v[124:127], a[20:23], a[148:151], v[124:127]\n"
"ds_read_b128 a[252:255], v28 offset:43328\n"
"v_mfma_f32_16x16x32_bf16 v[124:127], a[16:19], a[132:135], v[124:127]\n"
"s_sub_u32 s95, vcc_lo, s95\n"
"ds_read_b64_tr_b16 a[0:1], v2 offset:16512\n"
"v_mfma_f32_16x16x32_bf16 v[128:131], a[16:19], a[136:139], v[128:131]\n"
"ds_read_b64_tr_b16 a[2:3], v2 offset:17024\n"
"v_mfma_f32_16x16x32_bf16 v[128:131], a[20:23], a[152:155], v[128:131]\n"
"s_mulk_i32 s99, 154\n"
"ds_read_b64_tr_b16 a[4:5], v2 offset:20640\n"
"v_mfma_f32_16x16x32_bf16 v[132:135], a[24:27], a[128:131], v[132:135]\n"
"ds_read_b64_tr_b16 a[6:7], v2 offset:21152\n"
"v_mfma_f32_16x16x32_bf16 v[132:135], a[28:31], a[144:147], v[132:135]\n"
"s_mulk_i32 s95, 10\n"
"buffer_load_dwordx4 v9, s[40:43], s93 offen offset:3096 lds\n"
"v_mfma_f32_16x16x32_bf16 v[136:139], a[28:31], a[148:151], v[136:139]\n"
"s_addk_i32 m0, 8256\n"
"ds_read_b64_tr_b16 a[8:9], v2 offset:16520\n"
"s_add_u32 s93, s89, 256\n"
"v_mfma_f32_16x16x32_bf16 v[136:139], a[24:27], a[132:135], v[136:139]\n"
"s_waitcnt lgkmcnt(5)\n"
"buffer_load_dwordx4 v6, s[40:43], s93 offen offset:0 lds\n"
"v_mfma_f32_16x16x32_bf16 v[140:143], a[24:27], a[136:139], v[140:143]\n"
"s_add_u32 m0, s63, 49536\n"
"ds_read_b64_tr_b16 a[10:11], v2 offset:17032\n"
"s_addk_i32 s81, 16384\n"
"v_mfma_f32_16x16x32_bf16 v[140:143], a[28:31], a[152:155], v[140:143]\n"
"s_waitcnt lgkmcnt(2)\n"
"ds_read_b64_tr_b16 a[12:13], v2 offset:20648\n"
"v_mfma_f32_16x16x32_bf16 v[144:147], a[0:3], a[128:131], v[144:147]\n"
"ds_read_b64_tr_b16 a[14:15], v2 offset:21160\n"
"v_mfma_f32_16x16x32_bf16 v[144:147], a[4:7], a[144:147], v[144:147]\n"
"s_cmp_lt_u32 s95, s99\n"
"s_cselect_b32 s23, 5, s23\n"
"v_mfma_f32_16x16x32_bf16 v[148:151], a[4:7], a[148:151], v[148:151]\n"
"buffer_load_dwordx4 v11, s[44:47], s81 offen offset:0 nt lds\n"
"v_mfma_f32_16x16x32_bf16 v[148:151], a[0:3], a[132:135], v[148:151]\n"
"s_add_u32 m0, s62, 8256\n"
"ds_read_b64_tr_b16 a[16:17], v3 offset:16512\n"
"s_lshl_b32 s95, s23, 8\n"
"v_mfma_f32_16x16x32_bf16 v[152:155], a[0:3], a[136:139], v[152:155]\n"
"ds_read_b64_tr_b16 a[18:19], v3 offset:17024\n"
"v_mfma_f32_16x16x32_bf16 v[152:155], a[4:7], a[152:155], v[152:155]\n"
"s_sub_u32 s77, s77, s95\n"
"buffer_load_dwordx4 v7, s[40:43], s93 offen offset:1032 lds\n"
"v_mfma_f32_16x16x32_bf16 v[156:159], a[4:7], a[156:159], v[156:159]\n"
"s_waitcnt lgkmcnt(2)\n"
"ds_read_b64_tr_b16 a[20:21], v3 offset:20640\n"
"v_mfma_f32_16x16x32_bf16 v[156:159], a[0:3], a[140:143], v[156:159]\n"
"s_addk_i32 s79, 63\n"
"ds_read_b64_tr_b16 a[22:23], v3 offset:21152\n"
"v_mfma_f32_16x16x32_bf16 v[160:163], a[8:11], a[128:131], v[160:163]\n"
"ds_read_b64_tr_b16 a[24:25], v3 offset:16520\n"
"v_mfma_f32_16x16x32_bf16 v[160:163], a[12:15], a[144:147], v[160:163]\n"
"s_sub_u32 s0, s79, s2\n"
"ds_read_b64_tr_b16 a[26:27], v3 offset:17032\n"
"v_mfma_f32_16x16x32_bf16 v[164:167], a[12:15], a[148:151], v[164:167]\n"
"buffer_load_dwordx4 v8, s[40:43], s93 offen offset:2064 lds\n"
"v_mfma_f32_16x16x32_bf16 v[164:167], a[8:11], a[132:135], v[164:167]\n"
"s_add_u32 m0, s63, 49536\n"
"ds_read_b64_tr_b16 a[28:29], v3 offset:20648\n"
"s_sub_u32 s0, s0, s23\n"
"v_mfma_f32_16x16x32_bf16 v[168:171], a[8:11], a[136:139], v[168:171]\n"
"ds_read_b64_tr_b16 a[30:31], v3 offset:21160\n"
"v_mfma_f32_16x16x32_bf16 v[168:171], a[12:15], a[152:155], v[168:171]\n"
"s_max_i32 s0, s0, 0\n"
"ds_read_b64_tr_b16 a[0:1], v4 offset:16512\n"
"v_mfma_f32_16x16x32_bf16 v[172:175], a[12:15], a[156:159], v[172:175]\n"
"s_waitcnt lgkmcnt(5)\n"
"buffer_load_dwordx4 v12, s[44:47], s81 offen offset:1024 nt lds\n"
"v_mfma_f32_16x16x32_bf16 v[172:175], a[8:11], a[140:143], v[172:175]\n"
"s_add_u32 m0, s62, 8256\n"
"ds_read_b64_tr_b16 a[2:3], v4 offset:17024\n"
"s_add_u32 s79, s2, s0\n"
"v_mfma_f32_16x16x32_bf16 v[176:179], a[16:19], a[128:131], v[176:179]\n"
"ds_read_b64_tr_b16 a[4:5], v4 offset:20640\n"
"v_mfma_f32_16x16x32_bf16 v[176:179], a[20:23], a[144:147], v[176:179]\n"
"s_lshr_b64 vcc, s[0:1], 2\n"
"ds_read_b64_tr_b16 a[6:7], v4 offset:21152\n"
"v_mfma_f32_16x16x32_bf16 v[180:183], a[20:23], a[148:151], v[180:183]\n"
"buffer_load_dwordx4 v9, s[40:43], s93 offen offset:3096 lds\n"
"v_mfma_f32_16x16x32_bf16 v[180:183], a[16:19], a[132:135], v[180:183]\n"
"s_cbranch_vccnz PC7_%=\n"
"PB7_%=:\n"
"v_mfma_f32_16x16x32_bf16 v[184:187], a[16:19], a[136:139], v[184:187]\n"
"s_waitcnt lgkmcnt(3)\n"
"ds_read_b64_tr_b16 a[8:9], v4 offset:16520\n"
"v_mfma_f32_16x16x32_bf16 v[184:187], a[20:23], a[152:155], v[184:187]\n"
"ds_read_b64_tr_b16 a[10:11], v4 offset:17032\n"
"v_mfma_f32_16x16x32_bf16 v[188:191], a[24:27], a[128:131], v[188:191]\n"
"ds_read_b64_tr_b16 a[12:13], v4 offset:20648\n"
"v_mfma_f32_16x16x32_bf16 v[188:191], a[28:31], a[144:147], v[188:191]\n"
"ds_read_b64_tr_b16 a[14:15], v4 offset:21160\n"
"v_mfma_f32_16x16x32_bf16 v[192:195], a[28:31], a[148:151], v[192:195]\n"
"s_add_u32 m0, s63, 49536\n"
"ds_read_b64_tr_b16 a[16:17], v5 offset:16512\n"
"v_mfma_f32_16x16x32_bf16 v[192:195], a[24:27], a[132:135], v[192:195]\n"
"ds_read_b64_tr_b16 a[18:19], v5 offset:17024\n"
"v_mfma_f32_16x16x32_bf16 v[196:199], a[24:27], a[136:139], v[196:199]\n"
"s_waitcnt lgkmcnt(6)\n"
"ds_read_b64_tr_b16 a[20:21], v5 offset:20640\n"
"v_mfma_f32_16x16x32_bf16 v[196:199], a[28:31], a[152:155], v[196:199]\n"
"buffer_load_dwordx4 v13, s[44:47], s81 offen offset:2048 nt lds\n"
"v_mfma_f32_16x16x32_bf16 v[200:203], a[0:3], a[128:131], v[200:203]\n"
"ds_read_b64_tr_b16 a[22:23], v5 offset:21152\n"
"v_mfma_f32_16x16x32_bf16 v[200:203], a[4:7], a[144:147], v[200:203]\n"
"ds_read_b64_tr_b16 a[24:25], v5 offset:16520\n"
"v_mfma_f32_16x16x32_bf16 v[204:207], a[4:7], a[148:151], v[204:207]\n"
"ds_read_b64_tr_b16 a[26:27], v5 offset:17032\n"
"v_mfma_f32_16x16x32_bf16 v[204:207], a[0:3], a[132:135], v[204:207]\n"
"ds_read_b64_tr_b16 a[28:29], v5 offset:20648\n"
"v_mfma_f32_16x16x32_bf16 v[208:211], a[0:3], a[136:139], v[208:211]\n"
"ds_read_b64_tr_b16 a[30:31], v5 offset:21160\n"
"v_mfma_f32_16x16x32_bf16 v[208:211], a[4:7], a[152:155], v[208:211]\n"
"v_mfma_f32_16x16x32_bf16 v[212:215], a[4:7], a[156:159], v[212:215]\n"
"s_waitcnt lgkmcnt(5)\n"
"v_mfma_f32_16x16x32_bf16 v[212:215], a[0:3], a[140:143], v[212:215]\n"
"v_mfma_f32_16x16x32_bf16 v[216:219], a[8:11], a[128:131], v[216:219]\n"
"s_memrealtime s[2:3]\n"
"v_mfma_f32_16x16x32_bf16 v[216:219], a[12:15], a[144:147], v[216:219]\n"
"v_mfma_f32_16x16x32_bf16 v[220:223], a[12:15], a[148:151], v[220:223]\n"
"v_mfma_f32_16x16x32_bf16 v[220:223], a[8:11], a[132:135], v[220:223]\n"
"v_mfma_f32_16x16x32_bf16 v[224:227], a[8:11], a[136:139], v[224:227]\n"
"v_mfma_f32_16x16x32_bf16 v[224:227], a[12:15], a[152:155], v[224:227]\n"
"buffer_load_dwordx4 v14, s[44:47], s81 offen offset:3072 nt lds\n"
"v_mfma_f32_16x16x32_bf16 v[228:231], a[12:15], a[156:159], v[228:231]\n"
"s_waitcnt lgkmcnt(4)\n"
"v_mfma_f32_16x16x32_bf16 v[228:231], a[8:11], a[140:143], v[228:231]\n"
"s_waitcnt lgkmcnt(0)\n"
"v_mfma_f32_16x16x32_bf16 v[232:235], a[16:19], a[128:131], v[232:235]\n"
"s_barrier\n"
"v_mfma_f32_16x16x32_bf16 v[232:235], a[20:23], a[144:147], v[232:235]\n"
"s_add_u32 m0, s62, 16512\n"
"ds_read_b64_tr_b16 a[0:1], v2 offset:24768\n"
"s_add_u32 s93, s91, 256\n"
"v_mfma_f32_16x16x32_bf16 v[236:239], a[20:23], a[148:151], v[236:239]\n"
"s_addk_i32 s79, 62\n"
"ds_read_b64_tr_b16 a[2:3], v2 offset:25280\n"
"v_mfma_f32_16x16x32_bf16 v[236:239], a[16:19], a[132:135], v[236:239]\n"
"ds_read_b64_tr_b16 a[4:5], v2 offset:28896\n"
"v_mfma_f32_16x16x32_bf16 v[240:243], a[16:19], a[136:139], v[240:243]\n"
"ds_read_b64_tr_b16 a[6:7], v2 offset:29408\n"
"s_sub_u32 s0, s79, s2\n"
"v_mfma_f32_16x16x32_bf16 v[240:243], a[20:23], a[152:155], v[240:243]\n"
"buffer_load_dwordx4 v6, s[40:43], s93 offen offset:0 lds\n"
"v_mfma_f32_16x16x32_bf16 v[244:247], a[24:27], a[128:131], v[244:247]\n"
"s_sub_u32 s0, s0, s23\n"
"ds_read_b64_tr_b16 a[8:9], v2 offset:24776\n"
"v_mfma_f32_16x16x32_bf16 v[244:247], a[28:31], a[144:147], v[244:247]\n"
"s_add_u32 m0, s63, 57792\n"
"ds_read_b64_tr_b16 a[10:11], v2 offset:25288\n"
"s_addk_i32 s81, 16384\n"
"v_mfma_f32_16x16x32_bf16 v[248:251], a[28:31], a[148:151], v[248:251]\n"
"s_max_i32 s0, s0, 0\n"
"ds_read_b64_tr_b16 a[12:13], v2 offset:28904\n"
"v_mfma_f32_16x16x32_bf16 v[248:251], a[24:27], a[132:135], v[248:251]\n"
"ds_read_b64_tr_b16 a[14:15], v2 offset:29416\n"
"v_mfma_f32_16x16x32_bf16 v[252:255], a[24:27], a[136:139], v[252:255]\n"
"s_waitcnt lgkmcnt(4)\n"
"buffer_load_dwordx4 v11, s[44:47], s81 offen offset:0 nt lds\n"
"v_mfma_f32_16x16x32_bf16 v[252:255], a[28:31], a[152:155], v[252:255]\n"
"s_add_u32 m0, s62, 16512\n"
"ds_read_b64_tr_b16 a[16:17], v3 offset:24768\n"
"s_add_u32 s79, s2, s0\n"
"v_mfma_f32_16x16x32_bf16 v[32:35], a[0:3], a[160:163], v[32:35]\n"
"ds_read_b64_tr_b16 a[18:19], v3 offset:25280\n"
"v_mfma_f32_16x16x32_bf16 v[32:35], a[4:7], a[176:179], v[32:35]\n"
"s_lshr_b64 vcc, s[0:1], 2\n"
"ds_read_b64_tr_b16 a[20:21], v3 offset:28896\n"
"v_mfma_f32_16x16x32_bf16 v[36:39], a[4:7], a[180:183], v[36:39]\n"
"buffer_load_dwordx4 v7, s[40:43], s93 offen offset:1032 lds\n"
"v_mfma_f32_16x16x32_bf16 v[36:39], a[0:3], a[164:167], v[36:39]\n"
"s_cbranch_vccnz PC8_%=\n"
"PB8_%=:\n"
"v_mfma_f32_16x16x32_bf16 v[40:43], a[0:3], a[168:171], v[40:43]\n"
"ds_read_b64_tr_b16 a[22:23], v3 offset:29408\n"
"v_mfma_f32_16x16x32_bf16 v[40:43], a[4:7], a[184:187], v[40:43]\n"
"ds_read_b64_tr_b16 a[24:25], v3 offset:24776\n"
"v_mfma_f32_16x16x32_bf16 v[44:47], a[4:7], a[188:191], v[44:47]\n"
"s_waitcnt lgkmcnt(4)\n"
"ds_read_b64_tr_b16 a[26:27], v3 offset:25288\n"
"v_mfma_f32_16x16x32_bf16 v[44:47], a[0:3], a[172:175], v[44:47]\n"
"buffer_load_dwordx4 v8, s[40:43], s93 offen offset:2064 lds\n"
"v_mfma_f32_16x16x32_bf16 v[48:51], a[8:11], a[160:163], v[48:51]\n"
"s_add_u32 m0, s63, 57792\n"
"ds_read_b64_tr_b16 a[28:29], v3 offset:28904\n"
"v_mfma_f32_16x16x32_bf16 v[48:51], a[12:15], a[176:179], v[48:51]\n"
"ds_read_b64_tr_b16 a[30:31], v3 offset:29416\n"
"v_mfma_f32_16x16x32_bf16 v[52:55], a[12:15], a[180:183], v[52:55]\n"
"ds_read_b64_tr_b16 a[0:1], v4 offset:24768\n"
"v_mfma_f32_16x16x32_bf16 v[52:55], a[8:11], a[164:167], v[52:55]\n"
"buffer_load_dwordx4 v12, s[44:47], s81 offen offset:1024 nt lds\n"
"v_mfma_f32_16x16x32_bf16 v[56:59], a[8:11], a[168:171], v[56:59]\n"
"ds_read_b64_tr_b16 a[2:3], v4 offset:25280\n"
"v_mfma_f32_16x16x32_bf16 v[56:59], a[12:15], a[184:187], v[56:59]\n"
"s_add_u32 m0, s62, 16512\n"
"ds_read_b64_tr_b16 a[4:5], v4 offset:28896\n"
"v_mfma_f32_16x16x32_bf16 v[60:63], a[12:15], a[188:191], v[60:63]\n"
"s_waitcnt lgkmcnt(5)\n"
"ds_read_b64_tr_b16 a[6:7], v4 offset:29408\n"
"v_mfma_f32_16x16x32_bf16 v[60:63], a[8:11], a[172:175], v[60:63]\n"
"ds_read_b64_tr_b16 a[8:9], v4 offset:24776\n"
"v_mfma_f32_16x16x32_bf16 v[64:67], a[16:19], a[160:163], v[64:67]\n"
"buffer_load_dwordx4 v9, s[40:43], s93 offen offset:3096 lds\n"
"v_mfma_f32_16x16x32_bf16 v[64:67], a[20:23], a[176:179], v[64:67]\n"
"ds_read_b64_tr_b16 a[10:11], v4 offset:25288\n"
"v_mfma_f32_16x16x32_bf16 v[68:71], a[20:23], a[180:183], v[68:71]\n"
"ds_read_b64_tr_b16 a[12:13], v4 offset:28904\n"
"v_mfma_f32_16x16x32_bf16 v[68:71], a[16:19], a[164:167], v[68:71]\n"
"ds_read_b64_tr_b16 a[14:15], v4 offset:29416\n"
"v_mfma_f32_16x16x32_bf16 v[72:75], a[16:19], a[168:171], v[72:75]\n"
"s_waitcnt lgkmcnt(5)\n"
"ds_read_b64_tr_b16 a[16:17], v5 offset:24768\n"
"v_mfma_f32_16x16x32_bf16 v[72:75], a[20:23], a[184:187], v[72:75]\n"
"ds_read_b64_tr_b16 a[18:19], v5 offset:25280\n"
"v_mfma_f32_16x16x32_bf16 v[76:79], a[24:27], a[160:163], v[76:79]\n"
"s_add_u32 m0, s63, 57792\n"
"ds_read_b64_tr_b16 a[20:21], v5 offset:28896\n"
"v_mfma_f32_16x16x32_bf16 v[76:79], a[28:31], a[176:179], v[76:79]\n"
"ds_read_b64_tr_b16 a[22:23], v5 offset:29408\n"
"v_mfma_f32_16x16x32_bf16 v[80:83], a[28:31], a[180:183], v[80:83]\n"
"v_mfma_f32_16x16x32_bf16 v[80:83], a[24:27], a[164:167], v[80:83]\n"
"buffer_load_dwordx4 v13, s[44:47], s81 offen offset:2048 nt lds\n"
"v_mfma_f32_16x16x32_bf16 v[84:87], a[24:27], a[168:171], v[84:87]\n"
"s_waitcnt lgkmcnt(4)\n"
"ds_read_b64_tr_b16 a[24:25], v5 offset:24776\n"
"v_mfma_f32_16x16x32_bf16 v[84:87], a[28:31], a[184:187], v[84:87]\n"
"ds_read_b64_tr_b16 a[26:27], v5 offset:25288\n"
"v_mfma_f32_16x16x32_bf16 v[32:35], a[0:3], a[192:195], v[32:35]\n"
"ds_read_b64_tr_b16 a[28:29], v5 offset:28904\n"
"v_mfma_f32_16x16x32_bf16 v[32:35], a[4:7], a[208:211], v[32:35]\n"
"ds_read_b64_tr_b16 a[30:31], v5 offset:29416\n"
"v_mfma_f32_16x16x32_bf16 v[36:39], a[4:7], a[212:215], v[36:39]\n"
"v_mfma_f32_16x16x32_bf16 v[36:39], a[0:3], a[196:199], v[36:39]\n"
"v_mfma_f32_16x16x32_bf16 v[40:43], a[0:3], a[200:203], v[40:43]\n"
"v_mfma_f32_16x16x32_bf16 v[40:43], a[4:7], a[216:219], v[40:43]\n"
"v_mfma_f32_16x16x32_bf16 v[44:47], a[4:7], a[220:223], v[44:47]\n"
"v_mfma_f32_16x16x32_bf16 v[44:47], a[0:3], a[204:207], v[44:47]\n"
"v_mfma_f32_16x16x32_bf16 v[48:51], a[8:11], a[192:195], v[48:51]\n"
"s_memrealtime s[2:3]\n"
"v_mfma_f32_16x16x32_bf16 v[48:51], a[12:15], a[208:211], v[48:51]\n"
"v_mfma_f32_16x16x32_bf16 v[52:55], a[12:15], a[212:215], v[52:55]\n"
"buffer_load_dwordx4 v14, s[44:47], s81 offen offset:3072 nt lds\n"
"v_mfma_f32_16x16x32_bf16 v[52:55], a[8:11], a[196:199], v[52:55]\n"
"v_mfma_f32_16x16x32_bf16 v[56:59], a[8:11], a[200:203], v[56:59]\n"
"v_mfma_f32_16x16x32_bf16 v[56:59], a[12:15], a[216:219], v[56:59]\n"
"v_mfma_f32_16x16x32_bf16 v[60:63], a[12:15], a[220:223], v[60:63]\n"
"s_waitcnt lgkmcnt(4)\n"
"v_mfma_f32_16x16x32_bf16 v[60:63], a[8:11], a[204:207], v[60:63]\n"
"s_waitcnt vmcnt(22) lgkmcnt(0)\n"
"v_mfma_f32_16x16x32_bf16 v[64:67], a[16:19], a[192:195], v[64:67]\n"
"s_barrier\n"
"v_mfma_f32_16x16x32_bf16 v[64:67], a[20:23], a[208:211], v[64:67]\n"
"s_addk_i32 s79, 62\n"
"ds_read_b64_tr_b16 a[0:1], v2 offset:33024\n"
"v_mfma_f32_16x16x32_bf16 v[68:71], a[20:23], a[212:215], v[68:71]\n"
"ds_read_b64_tr_b16 a[2:3], v2 offset:33536\n"
"v_mfma_f32_16x16x32_bf16 v[68:71], a[16:19], a[196:199], v[68:71]\n"
"s_sub_u32 s0, s79, s2\n"
"ds_read_b64_tr_b16 a[4:5], v2 offset:37152\n"
"v_mfma_f32_16x16x32_bf16 v[72:75], a[16:19], a[200:203], v[72:75]\n"
"ds_read_b64_tr_b16 a[6:7], v2 offset:37664\n"
"v_mfma_f32_16x16x32_bf16 v[72:75], a[20:23], a[216:219], v[72:75]\n"
"s_sub_u32 s0, s0, s23\n"
"ds_read_b64_tr_b16 a[8:9], v2 offset:33032\n"
"v_mfma_f32_16x16x32_bf16 v[76:79], a[24:27], a[192:195], v[76:79]\n"
"ds_read_b64_tr_b16 a[10:11], v2 offset:33544\n"
"v_mfma_f32_16x16x32_bf16 v[76:79], a[28:31], a[208:211], v[76:79]\n"
"s_max_i32 s0, s0, 0\n"
"buffer_load_dword v30, v0, s[56:59], s90 offen offset:0\n"
"v_mfma_f32_16x16x32_bf16 v[80:83], a[28:31], a[212:215], v[80:83]\n"
"ds_read_b64_tr_b16 a[12:13], v2 offset:37160\n"
"v_mfma_f32_16x16x32_bf16 v[80:83], a[24:27], a[196:199], v[80:83]\n"
"s_add_u32 m0, s62, 24768\n"
"ds_read_b64_tr_b16 a[14:15], v2 offset:37672\n"
"s_add_u32 s93, s94, 384\n"
"v_mfma_f32_16x16x32_bf16 v[84:87], a[24:27], a[200:203], v[84:87]\n"
"s_waitcnt lgkmcnt(4)\n"
"ds_read_b64_tr_b16 a[16:17], v3 offset:33024\n"
"s_add_u32 s79, s2, s0\n"
"v_mfma_f32_16x16x32_bf16 v[84:87], a[28:31], a[216:219], v[84:87]\n"
"s_add_u32 s94, s20, -12\n"
"ds_read_b64_tr_b16 a[18:19], v3 offset:33536\n"
"s_and_b32 s94, s94, 15\n"
"v_mfma_f32_16x16x32_bf16 v[32:35], a[0:3], a[224:227], v[32:35]\n"
"s_lshr_b64 vcc, s[0:1], 2\n"
"buffer_load_dwordx4 v6, s[40:43], s93 offen offset:0 lds\n"
"v_mfma_f32_16x16x32_bf16 v[32:35], a[4:7], a[240:243], v[32:35]\n"
"s_lshl_b32 s94, s94, 2\n"
"ds_read_b64_tr_b16 a[20:21], v3 offset:37152\n"
"s_add_u32 s94, s94, s60\n"
"v_mfma_f32_16x16x32_bf16 v[36:39], a[4:7], a[244:247], v[36:39]\n"
"s_cbranch_vccnz PC9_%=\n"
"PB9_%=:\n"
"v_mfma_f32_16x16x32_bf16 v[36:39], a[0:3], a[228:231], v[36:39]\n"
"s_mul_i32 s94, s94, 15360\n"
"ds_read_b64_tr_b16 a[22:23], v3 offset:37664\n"
"v_mul_f32 v32, s82, v32\n"
"v_mfma_f32_16x16x32_bf16 v[40:43], a[0:3], a[232:235], v[40:43]\n"
"buffer_load_dwordx4 v7, s[40:43], s93 offen offset:1032 lds\n"
"v_mfma_f32_16x16x32_bf16 v[40:43], a[4:7], a[248:251], v[40:43]\n"
"ds_read_b64_tr_b16 a[24:25], v3 offset:33032\n"
"v_mul_f32 v33, s82, v33\n"
"v_mfma_f32_16x16x32_bf16 v[44:47], a[4:7], a[252:255], v[44:47]\n"
"s_waitcnt lgkmcnt(3)\n"
"ds_read_b64_tr_b16 a[26:27], v3 offset:33544\n"
"v_mul_f32 v34, s82, v34\n"
"v_mfma_f32_16x16x32_bf16 v[44:47], a[0:3], a[236:239], v[44:47]\n"
"ds_read_b64_tr_b16 a[28:29], v3 offset:37160\n"
"v_mul_f32 v35, s82, v35\n"
"v_mfma_f32_16x16x32_bf16 v[48:51], a[8:11], a[224:227], v[48:51]\n"
"ds_read_b64_tr_b16 a[30:31], v3 offset:37672\n"
"v_mul_f32 v36, s82, v36\n"
"v_mfma_f32_16x16x32_bf16 v[48:51], a[12:15], a[240:243], v[48:51]\n"
"buffer_load_dwordx4 v8, s[40:43], s93 offen offset:2064 lds\n"
"v_mfma_f32_16x16x32_bf16 v[52:55], a[12:15], a[244:247], v[52:55]\n"
"ds_read_b64_tr_b16 a[0:1], v4 offset:33024\n"
"v_mul_f32 v44, s82, v44\n"
"v_mfma_f32_16x16x32_bf16 v[52:55], a[8:11], a[228:231], v[52:55]\n"
"ds_read_b64_tr_b16 a[2:3], v4 offset:33536\n"
"v_mul_f32 v45, s82, v45\n"
"v_mfma_f32_16x16x32_bf16 v[56:59], a[8:11], a[232:235], v[56:59]\n"
"ds_read_b64_tr_b16 a[4:5], v4 offset:37152\n"
"v_mul_f32 v46, s82, v46\n"
"v_mfma_f32_16x16x32_bf16 v[56:59], a[12:15], a[248:251], v[56:59]\n"
"ds_read_b64_tr_b16 a[6:7], v4 offset:37664\n"
"v_mul_f32 v47, s82, v47\n"
"v_mfma_f32_16x16x32_bf16 v[60:63], a[12:15], a[252:255], v[60:63]\n"
"s_waitcnt lgkmcnt(5)\n"
"buffer_load_dwordx4 v9, s[40:43], s93 offen offset:3096 lds\n"
"v_mfma_f32_16x16x32_bf16 v[60:63], a[8:11], a[236:239], v[60:63]\n"
"ds_read_b64_tr_b16 a[8:9], v4 offset:33032\n"
"v_cvt_pk_bf16_f32 v44, v44, v45\n"
"v_mfma_f32_16x16x32_bf16 v[64:67], a[16:19], a[224:227], v[64:67]\n"
"ds_read_b64_tr_b16 a[10:11], v4 offset:33544\n"
"v_cvt_pk_bf16_f32 v46, v46, v47\n"
"v_mfma_f32_16x16x32_bf16 v[64:67], a[20:23], a[240:243], v[64:67]\n"
"ds_read_b64_tr_b16 a[12:13], v4 offset:37160\n"
"v_accvgpr_write_b32 a128, v44\n"
"v_mfma_f32_16x16x32_bf16 v[68:71], a[20:23], a[244:247], v[68:71]\n"
"ds_read_b64_tr_b16 a[14:15], v4 offset:37672\n"
"v_mul_f32 v60, s82, v60\n"
"v_mfma_f32_16x16x32_bf16 v[68:71], a[16:19], a[228:231], v[68:71]\n"
"v_mul_f32 v61, s82, v61\n"
"v_mul_f32 v62, s82, v62\n"
"v_mfma_f32_16x16x32_bf16 v[72:75], a[16:19], a[232:235], v[72:75]\n"
"s_waitcnt lgkmcnt(4)\n"
"ds_read_b64_tr_b16 a[16:17], v5 offset:33024\n"
"v_mul_f32 v63, s82, v63\n"
"v_mfma_f32_16x16x32_bf16 v[72:75], a[20:23], a[248:251], v[72:75]\n"
"ds_read_b64_tr_b16 a[18:19], v5 offset:33536\n"
"v_cvt_pk_bf16_f32 v60, v60, v61\n"
"v_mfma_f32_16x16x32_bf16 v[76:79], a[24:27], a[224:227], v[76:79]\n"
"buffer_load_dword v30, v0, s[56:59], s90 offen offset:2048\n"
"v_mfma_f32_16x16x32_bf16 v[76:79], a[28:31], a[240:243], v[76:79]\n"
"ds_read_b64_tr_b16 a[20:21], v5 offset:37152\n"
"v_cvt_pk_bf16_f32 v62, v62, v63\n"
"v_mfma_f32_16x16x32_bf16 v[80:83], a[28:31], a[244:247], v[80:83]\n"
"ds_read_b64_tr_b16 a[22:23], v5 offset:37664\n"
"v_accvgpr_write_b32 a129, v46\n"
"v_mfma_f32_16x16x32_bf16 v[80:83], a[24:27], a[228:231], v[80:83]\n"
"ds_read_b128 v[44:47], v10 offset:768\n"
"v_accvgpr_write_b32 a130, v60\n"
"v_mfma_f32_16x16x32_bf16 v[84:87], a[24:27], a[232:235], v[84:87]\n"
"ds_read_b64_tr_b16 a[24:25], v5 offset:33032\n"
"v_accvgpr_write_b32 a131, v62\n"
"v_mfma_f32_16x16x32_bf16 v[84:87], a[28:31], a[248:251], v[84:87]\n"
"ds_read_b64_tr_b16 a[26:27], v5 offset:33544\n"
"v_mul_f32 v37, s82, v37\n"
"v_mfma_f32_16x16x32_bf16 v[88:91], a[0:3], a[160:163], v[88:91]\n"
"ds_read_b64_tr_b16 a[28:29], v5 offset:37160\n"
"v_mul_f32 v38, s82, v38\n"
"v_mfma_f32_16x16x32_bf16 v[88:91], a[4:7], a[176:179], v[88:91]\n"
"ds_read_b64_tr_b16 a[30:31], v5 offset:37672\n"
"v_mul_f32 v39, s82, v39\n"
"v_mfma_f32_16x16x32_bf16 v[92:95], a[4:7], a[180:183], v[92:95]\n"
"ds_write_b128 v23, a[128:131] offset:0\n"
"v_mfma_f32_16x16x32_bf16 v[92:95], a[0:3], a[164:167], v[92:95]\n"
"v_mul_f32 v40, s82, v40\n"
"v_mul_f32 v41, s82, v41\n"
"v_mfma_f32_16x16x32_bf16 v[96:99], a[0:3], a[168:171], v[96:99]\n"
"v_mul_f32 v42, s82, v42\n"
"v_mul_f32 v43, s82, v43\n"
"v_mfma_f32_16x16x32_bf16 v[96:99], a[4:7], a[184:187], v[96:99]\n"
"v_cvt_pk_bf16_f32 v32, v32, v33\n"
"v_cvt_pk_bf16_f32 v34, v34, v35\n"
"v_mfma_f32_16x16x32_bf16 v[100:103], a[4:7], a[188:191], v[100:103]\n"
"s_waitcnt lgkmcnt(3)\n"
"v_cvt_pk_bf16_f32 v36, v36, v37\n"
"v_cvt_pk_bf16_f32 v38, v38, v39\n"
"v_mfma_f32_16x16x32_bf16 v[100:103], a[0:3], a[172:175], v[100:103]\n"
"v_cvt_pk_bf16_f32 v40, v40, v41\n"
"v_cvt_pk_bf16_f32 v42, v42, v43\n"
"v_mfma_f32_16x16x32_bf16 v[104:107], a[8:11], a[160:163], v[104:107]\n"
"s_memrealtime s[2:3]\n"
"v_accvgpr_write_b32 a32, v32\n"
"v_accvgpr_write_b32 a33, v34\n"
"v_mfma_f32_16x16x32_bf16 v[104:107], a[12:15], a[176:179], v[104:107]\n"
"ds_read_b128 v[32:35], v10 offset:0\n"
"v_accvgpr_write_b32 a64, v36\n"
"v_mfma_f32_16x16x32_bf16 v[108:111], a[12:15], a[180:183], v[108:111]\n"
"v_accvgpr_write_b32 a65, v38\n"
"v_accvgpr_write_b32 a96, v40\n"
"v_mfma_f32_16x16x32_bf16 v[108:111], a[8:11], a[164:167], v[108:111]\n"
"ds_read_b128 v[36:39], v10 offset:256\n"
"v_accvgpr_write_b32 a97, v42\n"
"v_mfma_f32_16x16x32_bf16 v[112:115], a[8:11], a[168:171], v[112:115]\n"
"ds_read_b128 v[40:43], v10 offset:512\n"
"v_mul_f32 v48, s82, v48\n"
"v_mfma_f32_16x16x32_bf16 v[112:115], a[12:15], a[184:187], v[112:115]\n"
"v_mul_f32 v49, s82, v49\n"
"v_mul_f32 v50, s82, v50\n"
"v_mfma_f32_16x16x32_bf16 v[116:119], a[12:15], a[188:191], v[116:119]\n"
"v_mul_f32 v51, s82, v51\n"
"v_cvt_pk_bf16_f32 v48, v48, v49\n"
"v_mfma_f32_16x16x32_bf16 v[116:119], a[8:11], a[172:175], v[116:119]\n"
"s_waitcnt vmcnt(22) lgkmcnt(0)\n"
"v_cvt_pk_bf16_f32 v50, v50, v51\n"
"v_mul_f32 v64, s82, v64\n"
"v_mfma_f32_16x16x32_bf16 v[120:123], a[16:19], a[160:163], v[120:123]\n"
"s_barrier\n"
"v_mfma_f32_16x16x32_bf16 v[120:123], a[20:23], a[176:179], v[120:123]\n"
"s_add_u32 m0, s63, 66048\n"
"ds_read_b64_tr_b16 a[0:1], v2 offset:0\n"
"v_mul_f32 v65, s82, v65\n"
"v_mfma_f32_16x16x32_bf16 v[124:127], a[20:23], a[180:183], v[124:127]\n"
"s_addk_i32 s81, 16384\n"
"ds_read_b64_tr_b16 a[2:3], v2 offset:512\n"
"v_mul_f32 v66, s82, v66\n"
"v_mfma_f32_16x16x32_bf16 v[124:127], a[16:19], a[164:167], v[124:127]\n"
"s_addk_i32 s79, 62\n"
"ds_read_b64_tr_b16 a[4:5], v2 offset:4128\n"
"v_mul_f32 v67, s82, v67\n"
"v_mfma_f32_16x16x32_bf16 v[128:131], a[16:19], a[168:171], v[128:131]\n"
"ds_read_b64_tr_b16 a[6:7], v2 offset:4640\n"
"v_mul_f32 v76, s82, v76\n"
"v_mfma_f32_16x16x32_bf16 v[128:131], a[20:23], a[184:187], v[128:131]\n"
"s_sub_u32 s0, s79, s2\n"
"buffer_load_dwordx4 v11, s[44:47], s81 offen offset:0 nt lds\n"
"v_mfma_f32_16x16x32_bf16 v[132:135], a[24:27], a[160:163], v[132:135]\n"
"ds_read_b64_tr_b16 a[8:9], v2 offset:8\n"
"v_mul_f32 v77, s82, v77\n"
"v_mfma_f32_16x16x32_bf16 v[132:135], a[28:31], a[176:179], v[132:135]\n"
"s_sub_u32 s0, s0, s23\n"
"ds_read_b64_tr_b16 a[10:11], v2 offset:520\n"
"v_mul_f32 v78, s82, v78\n"
"v_mfma_f32_16x16x32_bf16 v[136:139], a[28:31], a[180:183], v[136:139]\n"
"ds_read_b64_tr_b16 a[12:13], v2 offset:4136\n"
"v_mul_f32 v79, s82, v79\n"
"v_mfma_f32_16x16x32_bf16 v[136:139], a[24:27], a[164:167], v[136:139]\n"
"s_max_i32 s0, s0, 0\n"
"ds_read_b64_tr_b16 a[14:15], v2 offset:4648\n"
"v_accvgpr_write_b32 a34, v48\n"
"v_mfma_f32_16x16x32_bf16 v[140:143], a[24:27], a[168:171], v[140:143]\n"
"s_waitcnt lgkmcnt(4)\n"
"ds_read_b64_tr_b16 a[16:17], v3 offset:0\n"
"v_accvgpr_write_b32 a35, v50\n"
"v_mfma_f32_16x16x32_bf16 v[140:143], a[28:31], a[184:187], v[140:143]\n"
"s_add_u32 s79, s2, s0\n"
"ds_read_b64_tr_b16 a[18:19], v3 offset:512\n"
"v_cvt_pk_bf16_f32 v64, v64, v65\n"
"v_mfma_f32_16x16x32_bf16 v[88:91], a[0:3], a[192:195], v[88:91]\n"
"ds_read_b64_tr_b16 a[20:21], v3 offset:4128\n"
"v_cvt_pk_bf16_f32 v66, v66, v67\n"
"v_mfma_f32_16x16x32_bf16 v[88:91], a[4:7], a[208:211], v[88:91]\n"
"s_lshr_b64 vcc, s[0:1], 2\n"
"ds_read_b64_tr_b16 a[22:23], v3 offset:4640\n"
"v_cvt_pk_bf16_f32 v76, v76, v77\n"
"v_mfma_f32_16x16x32_bf16 v[92:95], a[4:7], a[212:215], v[92:95]\n"
"ds_read_b64_tr_b16 a[24:25], v3 offset:8\n"
"v_cvt_pk_bf16_f32 v78, v78, v79\n"
"v_mfma_f32_16x16x32_bf16 v[92:95], a[0:3], a[196:199], v[92:95]\n"
"s_cbranch_vccnz PC10_%=\n"
"PB10_%=:\n"
"v_mul_f32 v68, s82, v68\n"
"v_mfma_f32_16x16x32_bf16 v[96:99], a[0:3], a[200:203], v[96:99]\n"
"ds_read_b64_tr_b16 a[26:27], v3 offset:520\n"
"v_mul_f32 v69, s82, v69\n"
"v_mfma_f32_16x16x32_bf16 v[96:99], a[4:7], a[216:219], v[96:99]\n"
"ds_read_b64_tr_b16 a[28:29], v3 offset:4136\n"
"v_mul_f32 v70, s82, v70\n"
"v_mfma_f32_16x16x32_bf16 v[100:103], a[4:7], a[220:223], v[100:103]\n"
"s_waitcnt lgkmcnt(5)\n"
"buffer_load_dwordx4 v12, s[44:47], s81 offen offset:1024 nt lds\n"
"v_mfma_f32_16x16x32_bf16 v[100:103], a[0:3], a[204:207], v[100:103]\n"
"ds_read_b64_tr_b16 a[30:31], v3 offset:4648\n"
"v_mul_f32 v71, s82, v71\n"
"v_mfma_f32_16x16x32_bf16 v[104:107], a[8:11], a[192:195], v[104:107]\n"
"ds_read_b64_tr_b16 a[0:1], v4 offset:0\n"
"v_mul_f32 v80, s82, v80\n"
"v_mfma_f32_16x16x32_bf16 v[104:107], a[12:15], a[208:211], v[104:107]\n"
"ds_read_b64_tr_b16 a[2:3], v4 offset:512\n"
"v_mul_f32 v81, s82, v81\n"
"v_mfma_f32_16x16x32_bf16 v[108:111], a[12:15], a[212:215], v[108:111]\n"
"ds_read_b64_tr_b16 a[4:5], v4 offset:4128\n"
"v_mul_f32 v82, s82, v82\n"
"v_mfma_f32_16x16x32_bf16 v[108:111], a[8:11], a[196:199], v[108:111]\n"
"ds_read_b64_tr_b16 a[6:7], v4 offset:4640\n"
"v_mul_f32 v83, s82, v83\n"
"v_mfma_f32_16x16x32_bf16 v[112:115], a[8:11], a[200:203], v[112:115]\n"
"v_mul_f32 v52, s82, v52\n"
"v_mul_f32 v53, s82, v53\n"
"v_mfma_f32_16x16x32_bf16 v[112:115], a[12:15], a[216:219], v[112:115]\n"
"v_mul_f32 v54, s82, v54\n"
"v_mul_f32 v55, s82, v55\n"
"v_mfma_f32_16x16x32_bf16 v[116:119], a[12:15], a[220:223], v[116:119]\n"
"s_waitcnt lgkmcnt(4)\n"
"ds_read_b64_tr_b16 a[12:13], v4 offset:4136\n"
"v_accvgpr_write_b32 a36, v64\n"
"v_mfma_f32_16x16x32_bf16 v[116:119], a[8:11], a[204:207], v[116:119]\n"
"PG3_%=:\n"
"s_cbranch_execz PG7_%=\n"
"ds_read_b64_tr_b16 a[8:9], v4 offset:8\n"
"v_accvgpr_write_b32 a37, v66\n"
"v_mfma_f32_16x16x32_bf16 v[120:123], a[16:19], a[192:195], v[120:123]\n"
"ds_read_b64_tr_b16 a[10:11], v4 offset:520\n"
"v_accvgpr_write_b32 a38, v76\n"
"v_mfma_f32_16x16x32_bf16 v[120:123], a[20:23], a[208:211], v[120:123]\n"
"ds_read_b64_tr_b16 a[14:15], v4 offset:4648\n"
"v_accvgpr_write_b32 a39, v78\n"
"v_mfma_f32_16x16x32_bf16 v[124:127], a[20:23], a[212:215], v[124:127]\n"
"v_cvt_pk_bf16_f32 v68, v68, v69\n"
"v_cvt_pk_bf16_f32 v70, v70, v71\n"
"v_mfma_f32_16x16x32_bf16 v[124:127], a[16:19], a[196:199], v[124:127]\n"
"buffer_load_dwordx4 v13, s[44:47], s81 offen offset:2048 nt lds\n"
"v_mfma_f32_16x16x32_bf16 v[128:131], a[16:19], a[200:203], v[128:131]\n"
"ds_read_b64_tr_b16 a[16:17], v5 offset:0\n"
"v_cvt_pk_bf16_f32 v80, v80, v81\n"
"v_mfma_f32_16x16x32_bf16 v[128:131], a[20:23], a[216:219], v[128:131]\n"
"ds_read_b64_tr_b16 a[18:19], v5 offset:512\n"
"v_cvt_pk_bf16_f32 v82, v82, v83\n"
"v_mfma_f32_16x16x32_bf16 v[132:135], a[24:27], a[192:195], v[132:135]\n"
"ds_read_b64_tr_b16 a[20:21], v5 offset:4128\n"
"v_cvt_pk_bf16_f32 v52, v52, v53\n"
"v_mfma_f32_16x16x32_bf16 v[132:135], a[28:31], a[208:211], v[132:135]\n"
"ds_read_b64_tr_b16 a[22:23], v5 offset:4640\n"
"v_cvt_pk_bf16_f32 v54, v54, v55\n"
"v_mfma_f32_16x16x32_bf16 v[136:139], a[28:31], a[212:215], v[136:139]\n"
"v_mul_f32 v56, s82, v56\n"
"v_mul_f32 v57, s82, v57\n"
"v_mfma_f32_16x16x32_bf16 v[136:139], a[24:27], a[196:199], v[136:139]\n"
"v_mul_f32 v58, s82, v58\n"
"v_mul_f32 v59, s82, v59\n"
"v_mfma_f32_16x16x32_bf16 v[140:143], a[24:27], a[200:203], v[140:143]\n"
"s_waitcnt lgkmcnt(4)\n"
"ds_read_b64_tr_b16 a[24:25], v5 offset:8\n"
"v_accvgpr_write_b32 a68, v68\n"
"v_mfma_f32_16x16x32_bf16 v[140:143], a[28:31], a[216:219], v[140:143]\n"
"ds_read_b64_tr_b16 a[26:27], v5 offset:520\n"
"v_accvgpr_write_b32 a69, v70\n"
"v_mfma_f32_16x16x32_bf16 v[88:91], a[0:3], a[224:227], v[88:91]\n"
"ds_read_b64_tr_b16 a[28:29], v5 offset:4136\n"
"v_accvgpr_write_b32 a70, v80\n"
"v_mfma_f32_16x16x32_bf16 v[88:91], a[4:7], a[240:243], v[88:91]\n"
"ds_read_b64_tr_b16 a[30:31], v5 offset:4648\n"
"v_accvgpr_write_b32 a71, v82\n"
"v_mfma_f32_16x16x32_bf16 v[92:95], a[4:7], a[244:247], v[92:95]\n"
"v_accvgpr_write_b32 a66, v52\n"
"v_accvgpr_write_b32 a67, v54\n"
"v_mfma_f32_16x16x32_bf16 v[92:95], a[0:3], a[228:231], v[92:95]\n"
"v_cvt_pk_bf16_f32 v56, v56, v57\n"
"v_cvt_pk_bf16_f32 v58, v58, v59\n"
"v_mfma_f32_16x16x32_bf16 v[96:99], a[0:3], a[232:235], v[96:99]\n"
"buffer_load_dwordx4 v14, s[44:47], s81 offen offset:3072 nt lds\n"
"v_mfma_f32_16x16x32_bf16 v[96:99], a[4:7], a[248:251], v[96:99]\n"
"v_mul_f32 v72, s82, v72\n"
"v_mul_f32 v73, s82, v73\n"
"v_mfma_f32_16x16x32_bf16 v[100:103], a[4:7], a[252:255], v[100:103]\n"
"v_mul_f32 v74, s82, v74\n"
"v_mul_f32 v75, s82, v75\n"
"v_mfma_f32_16x16x32_bf16 v[100:103], a[0:3], a[236:239], v[100:103]\n"
"v_mul_f32 v84, s82, v84\n"
"v_mul_f32 v85, s82, v85\n"
"v_mfma_f32_16x16x32_bf16 v[104:107], a[8:11], a[224:227], v[104:107]\n"
"s_memrealtime s[2:3]\n"
"v_mul_f32 v86, s82, v86\n"
"v_mul_f32 v87, s82, v87\n"
"v_mfma_f32_16x16x32_bf16 v[104:107], a[12:15], a[240:243], v[104:107]\n"
"v_accvgpr_write_b32 a98, v56\n"
"v_accvgpr_write_b32 a99, v58\n"
"v_mfma_f32_16x16x32_bf16 v[108:111], a[12:15], a[244:247], v[108:111]\n"
"v_mul_f32 v100, s82, v100\n"
"v_mul_f32 v101, s82, v101\n"
"v_mfma_f32_16x16x32_bf16 v[108:111], a[8:11], a[228:231], v[108:111]\n"
"v_mul_f32 v102, s82, v102\n"
"v_mul_f32 v103, s82, v103\n"
"v_mfma_f32_16x16x32_bf16 v[112:115], a[8:11], a[232:235], v[112:115]\n"
"v_cvt_pk_bf16_f32 v100, v100, v101\n"
"v_cvt_pk_bf16_f32 v102, v102, v103\n"
"v_mfma_f32_16x16x32_bf16 v[112:115], a[12:15], a[248:251], v[112:115]\n"
"v_accvgpr_write_b32 a136, v100\n"
"v_accvgpr_write_b32 a137, v102\n"
"v_mfma_f32_16x16x32_bf16 v[116:119], a[12:15], a[252:255], v[116:119]\n"
"s_waitcnt lgkmcnt(4)\n"
"v_cvt_pk_bf16_f32 v72, v72, v73\n"
"v_cvt_pk_bf16_f32 v74, v74, v75\n"
"v_mfma_f32_16x16x32_bf16 v[116:119], a[8:11], a[236:239], v[116:119]\n"
"s_waitcnt vmcnt(20) lgkmcnt(0)\n"
"v_cvt_pk_bf16_f32 v84, v84, v85\n"
"v_cvt_pk_bf16_f32 v86, v86, v87\n"
"v_mfma_f32_16x16x32_bf16 v[120:123], a[16:19], a[224:227], v[120:123]\n"
"s_barrier\n"
"v_mfma_f32_16x16x32_bf16 v[120:123], a[20:23], a[240:243], v[120:123]\n"
"s_add_u32 s90, s90, 4096\n"
"ds_read_b64_tr_b16 a[0:1], v2 offset:8256\n"
"v_accvgpr_write_b32 a100, v72\n"
"v_mfma_f32_16x16x32_bf16 v[124:127], a[20:23], a[244:247], v[124:127]\n"
"s_addk_i32 s79, 66\n"
"buffer_load_dword v30, v0, s[56:59], s90 offen offset:0\n"
"v_mfma_f32_16x16x32_bf16 v[124:127], a[16:19], a[228:231], v[124:127]\n"
"ds_read_b64_tr_b16 a[2:3], v2 offset:8768\n"
"v_mul_f32 v116, s82, v116\n"
"v_mfma_f32_16x16x32_bf16 v[128:131], a[16:19], a[232:235], v[128:131]\n"
"ds_read_b64_tr_b16 a[4:5], v2 offset:12384\n"
"v_mul_f32 v117, s82, v117\n"
"v_mfma_f32_16x16x32_bf16 v[128:131], a[20:23], a[248:251], v[128:131]\n"
"s_sub_u32 s0, s79, s2\n"
"ds_read_b64_tr_b16 a[6:7], v2 offset:12896\n"
"v_mul_f32 v118, s82, v118\n"
"v_mfma_f32_16x16x32_bf16 v[132:135], a[24:27], a[224:227], v[132:135]\n"
"ds_read_b64_tr_b16 a[8:9], v2 offset:8264\n"
"v_mul_f32 v119, s82, v119\n"
"v_mfma_f32_16x16x32_bf16 v[132:135], a[28:31], a[240:243], v[132:135]\n"
"s_sub_u32 s0, s0, s23\n"
"ds_read_b64_tr_b16 a[10:11], v2 offset:8776\n"
"v_cvt_pk_bf16_f32 v116, v116, v117\n"
"v_mfma_f32_16x16x32_bf16 v[136:139], a[28:31], a[244:247], v[136:139]\n"
"ds_read_b64_tr_b16 a[12:13], v2 offset:12392\n"
"v_cvt_pk_bf16_f32 v118, v118, v119\n"
"v_mfma_f32_16x16x32_bf16 v[136:139], a[24:27], a[228:231], v[136:139]\n"
"s_max_i32 s0, s0, 0\n"
"ds_read_b64_tr_b16 a[14:15], v2 offset:12904\n"
"v_accvgpr_write_b32 a138, v116\n"
"v_mfma_f32_16x16x32_bf16 v[140:143], a[24:27], a[232:235], v[140:143]\n"
"s_waitcnt lgkmcnt(4)\n"
"ds_read_b64_tr_b16 a[16:17], v3 offset:8256\n"
"v_accvgpr_write_b32 a139, v118\n"
"v_mfma_f32_16x16x32_bf16 v[140:143], a[28:31], a[248:251], v[140:143]\n"
"s_add_u32 s79, s2, s0\n"
"ds_read_b64_tr_b16 a[18:19], v3 offset:8768\n"
"v_accvgpr_write_b32 a101, v74\n"
"v_mfma_f32_16x16x32_bf16 v[144:147], a[0:3], a[160:163], v[144:147]\n"
"ds_read_b64_tr_b16 a[20:21], v3 offset:12384\n"
"v_accvgpr_write_b32 a102, v84\n"
"v_mfma_f32_16x16x32_bf16 v[144:147], a[4:7], a[176:179], v[144:147]\n"
"s_lshr_b64 vcc, s[0:1], 2\n"
"ds_read_b64_tr_b16 a[22:23], v3 offset:12896\n"
"v_accvgpr_write_b32 a103, v86\n"
"v_mfma_f32_16x16x32_bf16 v[148:151], a[4:7], a[180:183], v[148:151]\n"
"ds_read_b64_tr_b16 a[24:25], v3 offset:8264\n"
"v_mul_f32 v88, s82, v88\n"
"v_mfma_f32_16x16x32_bf16 v[148:151], a[0:3], a[164:167], v[148:151]\n"
"s_cbranch_vccnz PC11_%=\n"
"PB11_%=:\n"
"v_mul_f32 v89, s82, v89\n"
"v_mfma_f32_16x16x32_bf16 v[152:155], a[0:3], a[168:171], v[152:155]\n"
"ds_read_b64_tr_b16 a[26:27], v3 offset:8776\n"
"v_mul_f32 v90, s82, v90\n"
"v_mfma_f32_16x16x32_bf16 v[152:155], a[4:7], a[184:187], v[152:155]\n"
"ds_read_b64_tr_b16 a[28:29], v3 offset:12392\n"
"v_mul_f32 v91, s82, v91\n"
"v_mfma_f32_16x16x32_bf16 v[156:159], a[4:7], a[188:191], v[156:159]\n"
"s_waitcnt lgkmcnt(5)\n"
"ds_read_b64_tr_b16 a[30:31], v3 offset:12904\n"
"v_mul_f32 v104, s82, v104\n"
"v_mfma_f32_16x16x32_bf16 v[156:159], a[0:3], a[172:175], v[156:159]\n"
"ds_read_b64_tr_b16 a[0:1], v4 offset:8256\n"
"v_mul_f32 v105, s82, v105\n"
"v_mfma_f32_16x16x32_bf16 v[160:163], a[8:11], a[160:163], v[160:163]\n"
"ds_read_b64_tr_b16 a[2:3], v4 offset:8768\n"
"v_mul_f32 v106, s82, v106\n"
"v_mfma_f32_16x16x32_bf16 v[160:163], a[12:15], a[176:179], v[160:163]\n"
"ds_read_b64_tr_b16 a[4:5], v4 offset:12384\n"
"v_mul_f32 v107, s82, v107\n"
"v_mfma_f32_16x16x32_bf16 v[164:167], a[12:15], a[180:183], v[164:167]\n"
"ds_read_b64_tr_b16 a[6:7], v4 offset:12896\n"
"v_cvt_pk_bf16_f32 v88, v88, v89\n"
"v_mfma_f32_16x16x32_bf16 v[164:167], a[8:11], a[164:167], v[164:167]\n"
"ds_read_b128 a[132:135], v24 offset:0\n"
"v_cvt_pk_bf16_f32 v90, v90, v91\n"
"v_mfma_f32_16x16x32_bf16 v[168:171], a[8:11], a[168:171], v[168:171]\n"
"ds_write_b128 v23, a[136:139] offset:1024\n"
"v_mfma_f32_16x16x32_bf16 v[168:171], a[12:15], a[184:187], v[168:171]\n"
"s_add_u32 m0, s63, 33024\n"
"v_cvt_pk_bf16_f32 v104, v104, v105\n"
"v_cvt_pk_bf16_f32 v106, v106, v107\n"
"v_mfma_f32_16x16x32_bf16 v[172:175], a[12:15], a[188:191], v[172:175]\n"
"s_waitcnt lgkmcnt(5)\n"
"ds_read_b64_tr_b16 a[12:13], v4 offset:12392\n"
"v_mul_f32 v120, s82, v120\n"
"v_mfma_f32_16x16x32_bf16 v[172:175], a[8:11], a[172:175], v[172:175]\n"
"s_addk_i32 s81, 16384\n"
"ds_read_b64_tr_b16 a[8:9], v4 offset:8264\n"
"v_mul_f32 v121, s82, v121\n"
"v_mfma_f32_16x16x32_bf16 v[176:179], a[16:19], a[160:163], v[176:179]\n"
"s_waitcnt lgkmcnt(3)\n"
"buffer_load_dwordx4 v11, s[44:47], s81 offen offset:0 nt lds\n"
"v_mfma_f32_16x16x32_bf16 v[176:179], a[20:23], a[176:179], v[176:179]\n"
"ds_read_b64_tr_b16 a[10:11], v4 offset:8776\n"
"v_mul_f32 v122, s82, v122\n"
"v_mfma_f32_16x16x32_bf16 v[180:183], a[20:23], a[180:183], v[180:183]\n"
"ds_read_b64_tr_b16 a[14:15], v4 offset:12904\n"
"v_mul_f32 v123, s82, v123\n"
"v_mfma_f32_16x16x32_bf16 v[180:183], a[16:19], a[164:167], v[180:183]\n"
"v_mul_f32 v132, s82, v132\n"
"v_mul_f32 v133, s82, v133\n"
"v_mfma_f32_16x16x32_bf16 v[184:187], a[16:19], a[168:171], v[184:187]\n"
"ds_read_b64_tr_b16 a[16:17], v5 offset:8256\n"
"v_mul_f32 v134, s82, v134\n"
"v_mfma_f32_16x16x32_bf16 v[184:187], a[20:23], a[184:187], v[184:187]\n"
"ds_read_b64_tr_b16 a[18:19], v5 offset:8768\n"
"v_mul_f32 v135, s82, v135\n"
"v_mfma_f32_16x16x32_bf16 v[188:191], a[24:27], a[160:163], v[188:191]\n"
"ds_read_b64_tr_b16 a[20:21], v5 offset:12384\n"
"v_accvgpr_write_b32 a40, v88\n"
"v_mfma_f32_16x16x32_bf16 v[188:191], a[28:31], a[176:179], v[188:191]\n"
"ds_read_b64_tr_b16 a[22:23], v5 offset:12896\n"
"v_accvgpr_write_b32 a41, v90\n"
"v_mfma_f32_16x16x32_bf16 v[192:195], a[28:31], a[180:183], v[192:195]\n"
"v_accvgpr_write_b32 a42, v104\n"
"v_accvgpr_write_b32 a43, v106\n"
"v_mfma_f32_16x16x32_bf16 v[192:195], a[24:27], a[164:167], v[192:195]\n"
"v_cvt_pk_bf16_f32 v120, v120, v121\n"
"v_cvt_pk_bf16_f32 v122, v122, v123\n"
"v_mfma_f32_16x16x32_bf16 v[196:199], a[24:27], a[168:171], v[196:199]\n"
"ds_read_b64_tr_b16 a[24:25], v5 offset:8264\n"
"v_cvt_pk_bf16_f32 v132, v132, v133\n"
"v_mfma_f32_16x16x32_bf16 v[196:199], a[28:31], a[184:187], v[196:199]\n"
"ds_read_b64_tr_b16 a[26:27], v5 offset:8776\n"
"v_cvt_pk_bf16_f32 v134, v134, v135\n"
"v_mfma_f32_16x16x32_bf16 v[144:147], a[0:3], a[192:195], v[144:147]\n"
"ds_read_b64_tr_b16 a[28:29], v5 offset:12392\n"
"v_mul_f32 v124, s82, v124\n"
"v_mfma_f32_16x16x32_bf16 v[144:147], a[4:7], a[208:211], v[144:147]\n"
"buffer_load_dwordx4 v12, s[44:47], s81 offen offset:1024 nt lds\n"
"v_mfma_f32_16x16x32_bf16 v[148:151], a[4:7], a[212:215], v[148:151]\n"
"ds_read_b64_tr_b16 a[30:31], v5 offset:12904\n"
"v_mul_f32 v125, s82, v125\n"
"v_mfma_f32_16x16x32_bf16 v[148:151], a[0:3], a[196:199], v[148:151]\n"
"v_mul_f32 v126, s82, v126\n"
"v_mul_f32 v127, s82, v127\n"
"v_mfma_f32_16x16x32_bf16 v[152:155], a[0:3], a[200:203], v[152:155]\n"
"v_mul_f32 v136, s82, v136\n"
"v_mul_f32 v137, s82, v137\n"
"v_mfma_f32_16x16x32_bf16 v[152:155], a[4:7], a[216:219], v[152:155]\n"
"v_mul_f32 v138, s82, v138\n"
"v_mul_f32 v139, s82, v139\n"
"v_mfma_f32_16x16x32_bf16 v[156:159], a[4:7], a[220:223], v[156:159]\n"
"s_waitcnt lgkmcnt(2)\n"
"v_mul_f32 v92, s82, v92\n"
"v_mul_f32 v93, s82, v93\n"
"v_mfma_f32_16x16x32_bf16 v[156:159], a[0:3], a[204:207], v[156:159]\n"
"v_mul_f32 v94, s82, v94\n"
"v_mul_f32 v95, s82, v95\n"
"v_mfma_f32_16x16x32_bf16 v[160:163], a[8:11], a[192:195], v[160:163]\n"
"s_memrealtime s[2:3]\n"
"v_mul_f32 v108, s82, v108\n"
"v_mul_f32 v109, s82, v109\n"
"v_mfma_f32_16x16x32_bf16 v[160:163], a[12:15], a[208:211], v[160:163]\n"
"v_mul_f32 v110, s82, v110\n"
"v_mul_f32 v111, s82, v111\n"
"v_mfma_f32_16x16x32_bf16 v[164:167], a[12:15], a[212:215], v[164:167]\n"
"v_accvgpr_write_b32 a44, v120\n"
"v_accvgpr_write_b32 a45, v122\n"
"v_mfma_f32_16x16x32_bf16 v[164:167], a[8:11], a[196:199], v[164:167]\n"
"v_accvgpr_write_b32 a46, v132\n"
"v_accvgpr_write_b32 a47, v134\n"
"v_mfma_f32_16x16x32_bf16 v[168:171], a[8:11], a[200:203], v[168:171]\n"
"v_cvt_pk_bf16_f32 v124, v124, v125\n"
"v_cvt_pk_bf16_f32 v126, v126, v127\n"
"v_mfma_f32_16x16x32_bf16 v[168:171], a[12:15], a[216:219], v[168:171]\n"
"v_cvt_pk_bf16_f32 v136, v136, v137\n"
"v_cvt_pk_bf16_f32 v138, v138, v139\n"
"v_mfma_f32_16x16x32_bf16 v[172:175], a[12:15], a[220:223], v[172:175]\n"
"v_cvt_pk_bf16_f32 v92, v92, v93\n"
"v_cvt_pk_bf16_f32 v94, v94, v95\n"
"v_mfma_f32_16x16x32_bf16 v[172:175], a[8:11], a[204:207], v[172:175]\n"
"s_waitcnt vmcnt(15) lgkmcnt(0)\n"
"buffer_load_dwordx4 v13, s[44:47], s81 offen offset:2048 nt lds\n"
"v_mfma_f32_16x16x32_bf16 v[176:179], a[16:19], a[192:195], v[176:179]\n"
"s_barrier\n"
"v_mfma_f32_16x16x32_bf16 v[176:179], a[20:23], a[208:211], v[176:179]\n"
"s_addk_i32 s79, 66\n"
"ds_read_b64_tr_b16 a[0:1], v2 offset:16512\n"
"v_cvt_pk_bf16_f32 v108, v108, v109\n"
"v_mfma_f32_16x16x32_bf16 v[180:183], a[20:23], a[212:215], v[180:183]\n"
"ds_read_b64_tr_b16 a[2:3], v2 offset:17024\n"
"v_cvt_pk_bf16_f32 v110, v110, v111\n"
"v_mfma_f32_16x16x32_bf16 v[180:183], a[16:19], a[196:199], v[180:183]\n"
"s_sub_u32 s0, s79, s2\n"
"ds_read_b64_tr_b16 a[4:5], v2 offset:20640\n"
"v_mul_f32 v96, s82, v96\n"
"v_mfma_f32_16x16x32_bf16 v[184:187], a[16:19], a[200:203], v[184:187]\n"
"ds_read_b64_tr_b16 a[6:7], v2 offset:21152\n"
"v_mul_f32 v97, s82, v97\n"
"v_mfma_f32_16x16x32_bf16 v[184:187], a[20:23], a[216:219], v[184:187]\n"
"s_sub_u32 s0, s0, s23\n"
"ds_read_b64_tr_b16 a[8:9], v2 offset:16520\n"
"v_mul_f32 v98, s82, v98\n"
"v_mfma_f32_16x16x32_bf16 v[188:191], a[24:27], a[192:195], v[188:191]\n"
"ds_read_b64_tr_b16 a[10:11], v2 offset:17032\n"
"v_mul_f32 v99, s82, v99\n"
"v_mfma_f32_16x16x32_bf16 v[188:191], a[28:31], a[208:211], v[188:191]\n"
"s_max_i32 s0, s0, 0\n"
"ds_read_b64_tr_b16 a[12:13], v2 offset:20648\n"
"v_mul_f32 v112, s82, v112\n"
"v_mfma_f32_16x16x32_bf16 v[192:195], a[28:31], a[212:215], v[192:195]\n"
"ds_read_b64_tr_b16 a[14:15], v2 offset:21160\n"
"v_mul_f32 v113, s82, v113\n"
"v_mfma_f32_16x16x32_bf16 v[192:195], a[24:27], a[196:199], v[192:195]\n"
"s_add_u32 s79, s2, s0\n"
"ds_read_b64_tr_b16 a[16:17], v3 offset:16512\n"
"v_mul_f32 v114, s82, v114\n"
"v_mfma_f32_16x16x32_bf16 v[196:199], a[24:27], a[200:203], v[196:199]\n"
"s_waitcnt lgkmcnt(5)\n"
"ds_read_b64_tr_b16 a[18:19], v3 offset:17024\n"
"v_mul_f32 v115, s82, v115\n"
"v_mfma_f32_16x16x32_bf16 v[196:199], a[28:31], a[216:219], v[196:199]\n"
"s_lshr_b64 vcc, s[0:1], 2\n"
"ds_read_b64_tr_b16 a[20:21], v3 offset:20640\n"
"v_accvgpr_write_b32 a76, v124\n"
"v_mfma_f32_16x16x32_bf16 v[144:147], a[0:3], a[224:227], v[144:147]\n"
"buffer_load_dwordx4 v14, s[44:47], s81 offen offset:3072 nt lds\n"
"v_mfma_f32_16x16x32_bf16 v[144:147], a[4:7], a[240:243], v[144:147]\n"
"s_cbranch_vccnz PC12_%=\n"
"PB12_%=:\n"
"v_accvgpr_write_b32 a77, v126\n"
"v_mfma_f32_16x16x32_bf16 v[148:151], a[4:7], a[244:247], v[148:151]\n"
"ds_read_b64_tr_b16 a[22:23], v3 offset:21152\n"
"v_accvgpr_write_b32 a78, v136\n"
"v_mfma_f32_16x16x32_bf16 v[148:151], a[0:3], a[228:231], v[148:151]\n"
"ds_read_b64_tr_b16 a[24:25], v3 offset:16520\n"
"v_accvgpr_write_b32 a79, v138\n"
"v_mfma_f32_16x16x32_bf16 v[152:155], a[0:3], a[232:235], v[152:155]\n"
"ds_read_b64_tr_b16 a[26:27], v3 offset:17032\n"
"v_accvgpr_write_b32 a72, v92\n"
"v_mfma_f32_16x16x32_bf16 v[152:155], a[4:7], a[248:251], v[152:155]\n"
"ds_read_b64_tr_b16 a[28:29], v3 offset:20648\n"
"v_accvgpr_write_b32 a73, v94\n"
"v_mfma_f32_16x16x32_bf16 v[156:159], a[4:7], a[252:255], v[156:159]\n"
"s_waitcnt lgkmcnt(4)\n"
"ds_read_b64_tr_b16 a[30:31], v3 offset:21160\n"
"v_accvgpr_write_b32 a74, v108\n"
"v_mfma_f32_16x16x32_bf16 v[156:159], a[0:3], a[236:239], v[156:159]\n"
"ds_read_b64_tr_b16 a[0:1], v4 offset:16512\n"
"v_accvgpr_write_b32 a75, v110\n"
"v_mfma_f32_16x16x32_bf16 v[160:163], a[8:11], a[224:227], v[160:163]\n"
"ds_read_b64_tr_b16 a[2:3], v4 offset:17024\n"
"v_cvt_pk_bf16_f32 v96, v96, v97\n"
"v_mfma_f32_16x16x32_bf16 v[160:163], a[12:15], a[240:243], v[160:163]\n"
"ds_read_b64_tr_b16 a[4:5], v4 offset:20640\n"
"v_cvt_pk_bf16_f32 v98, v98, v99\n"
"v_mfma_f32_16x16x32_bf16 v[164:167], a[12:15], a[244:247], v[164:167]\n"
"ds_read_b64_tr_b16 a[6:7], v4 offset:21152\n"
"v_mul_f32 v156, s82, v156\n"
"v_mfma_f32_16x16x32_bf16 v[164:167], a[8:11], a[228:231], v[164:167]\n"
"v_mul_f32 v157, s82, v157\n"
"v_mul_f32 v158, s82, v158\n"
"v_mfma_f32_16x16x32_bf16 v[168:171], a[8:11], a[232:235], v[168:171]\n"
"v_mul_f32 v159, s82, v159\n"
"v_cvt_pk_bf16_f32 v156, v156, v157\n"
"v_mfma_f32_16x16x32_bf16 v[168:171], a[12:15], a[248:251], v[168:171]\n"
"buffer_load_dword v30, v0, s[56:59], s90 offen offset:2048\n"
"v_mfma_f32_16x16x32_bf16 v[172:175], a[12:15], a[252:255], v[172:175]\n"
"s_waitcnt lgkmcnt(3)\n"
"ds_read_b64_tr_b16 a[12:13], v4 offset:20648\n"
"v_cvt_pk_bf16_f32 v158, v158, v159\n"
"v_mfma_f32_16x16x32_bf16 v[172:175], a[8:11], a[236:239], v[172:175]\n"
"ds_read_b64_tr_b16 a[8:9], v4 offset:16520\n"
"v_accvgpr_write_b32 a144, v156\n"
"v_mfma_f32_16x16x32_bf16 v[176:179], a[16:19], a[224:227], v[176:179]\n"
"ds_read_b64_tr_b16 a[10:11], v4 offset:17032\n"
"v_accvgpr_write_b32 a145, v158\n"
"v_mfma_f32_16x16x32_bf16 v[176:179], a[20:23], a[240:243], v[176:179]\n"
"ds_read_b64_tr_b16 a[14:15], v4 offset:21160\n"
"v_cvt_pk_bf16_f32 v112, v112, v113\n"
"v_mfma_f32_16x16x32_bf16 v[180:183], a[20:23], a[244:247], v[180:183]\n"
"v_mul_f32 v172, s82, v172\n"
"v_mul_f32 v173, s82, v173\n"
"v_mfma_f32_16x16x32_bf16 v[180:183], a[16:19], a[228:231], v[180:183]\n"
"v_mul_f32 v174, s82, v174\n"
"v_mul_f32 v175, s82, v175\n"
"v_mfma_f32_16x16x32_bf16 v[184:187], a[16:19], a[232:235], v[184:187]\n"
"ds_read_b64_tr_b16 a[16:17], v5 offset:16512\n"
"v_cvt_pk_bf16_f32 v172, v172, v173\n"
"v_mfma_f32_16x16x32_bf16 v[184:187], a[20:23], a[248:251], v[184:187]\n"
"ds_read_b64_tr_b16 a[18:19], v5 offset:17024\n"
"v_cvt_pk_bf16_f32 v174, v174, v175\n"
"v_mfma_f32_16x16x32_bf16 v[188:191], a[24:27], a[224:227], v[188:191]\n"
"ds_read_b64_tr_b16 a[20:21], v5 offset:20640\n"
"v_accvgpr_write_b32 a146, v172\n"
"v_mfma_f32_16x16x32_bf16 v[188:191], a[28:31], a[240:243], v[188:191]\n"
"ds_read_b64_tr_b16 a[22:23], v5 offset:21152\n"
"v_accvgpr_write_b32 a147, v174\n"
"v_mfma_f32_16x16x32_bf16 v[192:195], a[28:31], a[244:247], v[192:195]\n"
"ds_write_b128 v23, a[144:147] offset:0\n"
"v_mfma_f32_16x16x32_bf16 v[192:195], a[24:27], a[228:231], v[192:195]\n"
"v_cvt_pk_bf16_f32 v114, v114, v115\n"
"v_mul_f32 v128, s82, v128\n"
"v_mfma_f32_16x16x32_bf16 v[196:199], a[24:27], a[232:235], v[196:199]\n"
"s_waitcnt lgkmcnt(5)\n"
"ds_read_b64_tr_b16 a[24:25], v5 offset:16520\n"
"v_mul_f32 v129, s82, v129\n"
"v_mfma_f32_16x16x32_bf16 v[196:199], a[28:31], a[248:251], v[196:199]\n"
"ds_read_b64_tr_b16 a[26:27], v5 offset:17032\n"
"v_mul_f32 v130, s82, v130\n"
"v_mfma_f32_16x16x32_bf16 v[200:203], a[0:3], a[160:163], v[200:203]\n"
"ds_read_b64_tr_b16 a[28:29], v5 offset:20648\n"
"v_mul_f32 v131, s82, v131\n"
"v_mfma_f32_16x16x32_bf16 v[200:203], a[4:7], a[176:179], v[200:203]\n"
"ds_read_b64_tr_b16 a[30:31], v5 offset:21160\n"
"v_mul_f32 v140, s82, v140\n"
"v_mfma_f32_16x16x32_bf16 v[204:207], a[4:7], a[180:183], v[204:207]\n"
"v_mul_f32 v141, s82, v141\n"
"v_mul_f32 v142, s82, v142\n"
"v_mfma_f32_16x16x32_bf16 v[204:207], a[0:3], a[164:167], v[204:207]\n"
"v_mul_f32 v143, s82, v143\n"
"v_accvgpr_write_b32 a104, v96\n"
"v_mfma_f32_16x16x32_bf16 v[208:211], a[0:3], a[168:171], v[208:211]\n"
"v_accvgpr_write_b32 a105, v98\n"
"v_accvgpr_write_b32 a106, v112\n"
"v_mfma_f32_16x16x32_bf16 v[208:211], a[4:7], a[184:187], v[208:211]\n"
"v_accvgpr_write_b32 a107, v114\n"
"v_cvt_pk_bf16_f32 v128, v128, v129\n"
"v_mfma_f32_16x16x32_bf16 v[212:215], a[4:7], a[188:191], v[212:215]\n"
"v_cvt_pk_bf16_f32 v130, v130, v131\n"
"v_cvt_pk_bf16_f32 v140, v140, v141\n"
"v_mfma_f32_16x16x32_bf16 v[212:215], a[0:3], a[172:175], v[212:215]\n"
"v_cvt_pk_bf16_f32 v142, v142, v143\n"
"v_accvgpr_write_b32 a108, v128\n"
"v_mfma_f32_16x16x32_bf16 v[216:219], a[8:11], a[160:163], v[216:219]\n"
"s_memrealtime s[2:3]\n"
"v_accvgpr_write_b32 a109, v130\n"
"v_accvgpr_write_b32 a110, v140\n"
"v_mfma_f32_16x16x32_bf16 v[216:219], a[12:15], a[176:179], v[216:219]\n"
"s_add_u32 m0, s63, 0\n"
"v_accvgpr_write_b32 a111, v142\n"
"v_mul_f32 v144, s82, v144\n"
"v_mfma_f32_16x16x32_bf16 v[220:223], a[12:15], a[180:183], v[220:223]\n"
"s_addk_i32 s81, 16384\n"
"v_mul_f32 v145, s82, v145\n"
"v_mul_f32 v146, s82, v146\n"
"v_mfma_f32_16x16x32_bf16 v[220:223], a[8:11], a[164:167], v[220:223]\n"
"v_mul_f32 v147, s82, v147\n"
"v_mul_f32 v160, s82, v160\n"
"v_mfma_f32_16x16x32_bf16 v[224:227], a[8:11], a[168:171], v[224:227]\n"
"buffer_load_dwordx4 v11, s[44:47], s81 offen offset:0 nt lds\n"
"v_mfma_f32_16x16x32_bf16 v[224:227], a[12:15], a[184:187], v[224:227]\n"
"v_mul_f32 v161, s82, v161\n"
"v_mul_f32 v162, s82, v162\n"
"v_mfma_f32_16x16x32_bf16 v[228:231], a[12:15], a[188:191], v[228:231]\n"
"s_waitcnt lgkmcnt(5)\n"
"v_mul_f32 v163, s82, v163\n"
"v_cvt_pk_bf16_f32 v144, v144, v145\n"
"v_mfma_f32_16x16x32_bf16 v[228:231], a[8:11], a[172:175], v[228:231]\n"
"s_waitcnt vmcnt(12) lgkmcnt(0)\n"
"v_cvt_pk_bf16_f32 v146, v146, v147\n"
"v_cvt_pk_bf16_f32 v160, v160, v161\n"
"v_mfma_f32_16x16x32_bf16 v[232:235], a[16:19], a[160:163], v[232:235]\n"
"s_barrier\n"
"v_mfma_f32_16x16x32_bf16 v[232:235], a[20:23], a[176:179], v[232:235]\n"
"s_addk_i32 s79, 66\n"
"ds_read_b64_tr_b16 a[0:1], v2 offset:24768\n"
"v_cvt_pk_bf16_f32 v162, v162, v163\n"
"v_mfma_f32_16x16x32_bf16 v[236:239], a[20:23], a[180:183], v[236:239]\n"
"ds_read_b64_tr_b16 a[2:3], v2 offset:25280\n"
"v_mul_f32 v176, s82, v176\n"
"v_mfma_f32_16x16x32_bf16 v[236:239], a[16:19], a[164:167], v[236:239]\n"
"s_sub_u32 s0, s79, s2\n"
"ds_read_b64_tr_b16 a[4:5], v2 offset:28896\n"
"v_mul_f32 v177, s82, v177\n"
"v_mfma_f32_16x16x32_bf16 v[240:243], a[16:19], a[168:171], v[240:243]\n"
"ds_read_b64_tr_b16 a[6:7], v2 offset:29408\n"
"v_mul_f32 v178, s82, v178\n"
"v_mfma_f32_16x16x32_bf16 v[240:243], a[20:23], a[184:187], v[240:243]\n"
"s_sub_u32 s0, s0, s23\n"
"ds_read_b64_tr_b16 a[8:9], v2 offset:24776\n"
"v_mul_f32 v179, s82, v179\n"
"v_mfma_f32_16x16x32_bf16 v[244:247], a[24:27], a[160:163], v[244:247]\n"
"ds_read_b64_tr_b16 a[10:11], v2 offset:25288\n"
"v_mul_f32 v188, s82, v188\n"
"v_mfma_f32_16x16x32_bf16 v[244:247], a[28:31], a[176:179], v[244:247]\n"
"s_max_i32 s0, s0, 0\n"
"ds_read_b64_tr_b16 a[12:13], v2 offset:28904\n"
"v_mul_f32 v189, s82, v189\n"
"v_mfma_f32_16x16x32_bf16 v[248:251], a[28:31], a[180:183], v[248:251]\n"
"ds_read_b64_tr_b16 a[14:15], v2 offset:29416\n"
"v_mul_f32 v190, s82, v190\n"
"v_mfma_f32_16x16x32_bf16 v[248:251], a[24:27], a[164:167], v[248:251]\n"
"s_add_u32 s79, s2, s0\n"
"buffer_load_dwordx4 v12, s[44:47], s81 offen offset:1024 nt lds\n"
"v_mfma_f32_16x16x32_bf16 v[252:255], a[24:27], a[168:171], v[252:255]\n"
"s_waitcnt lgkmcnt(4)\n"
"ds_read_b64_tr_b16 a[16:17], v3 offset:24768\n"
"v_mul_f32 v191, s82, v191\n"
"v_mfma_f32_16x16x32_bf16 v[252:255], a[28:31], a[184:187], v[252:255]\n"
"s_lshr_b64 vcc, s[0:1], 2\n"
"ds_read_b64_tr_b16 a[18:19], v3 offset:25280\n"
"v_accvgpr_write_b32 a48, v144\n"
"v_mfma_f32_16x16x32_bf16 v[200:203], a[0:3], a[192:195], v[200:203]\n"
"ds_read_b64_tr_b16 a[20:21], v3 offset:28896\n"
"v_accvgpr_write_b32 a49, v146\n"
"v_mfma_f32_16x16x32_bf16 v[200:203], a[4:7], a[208:211], v[200:203]\n"
"s_cbranch_vccnz PC13_%=\n"
"PB13_%=:\n"
"v_accvgpr_write_b32 a50, v160\n"
"v_mfma_f32_16x16x32_bf16 v[204:207], a[4:7], a[212:215], v[204:207]\n"
"ds_read_b64_tr_b16 a[22:23], v3 offset:29408\n"
"v_accvgpr_write_b32 a51, v162\n"
"v_mfma_f32_16x16x32_bf16 v[204:207], a[0:3], a[196:199], v[204:207]\n"
"ds_read_b64_tr_b16 a[24:25], v3 offset:24776\n"
"v_cvt_pk_bf16_f32 v176, v176, v177\n"
"v_mfma_f32_16x16x32_bf16 v[208:211], a[0:3], a[200:203], v[208:211]\n"
"ds_read_b64_tr_b16 a[26:27], v3 offset:25288\n"
"v_cvt_pk_bf16_f32 v178, v178, v179\n"
"v_mfma_f32_16x16x32_bf16 v[208:211], a[4:7], a[216:219], v[208:211]\n"
"ds_read_b64_tr_b16 a[28:29], v3 offset:28904\n"
"v_cvt_pk_bf16_f32 v188, v188, v189\n"
"v_mfma_f32_16x16x32_bf16 v[212:215], a[4:7], a[220:223], v[212:215]\n"
"s_waitcnt lgkmcnt(5)\n"
"ds_read_b64_tr_b16 a[30:31], v3 offset:29416\n"
"v_cvt_pk_bf16_f32 v190, v190, v191\n"
"v_mfma_f32_16x16x32_bf16 v[212:215], a[0:3], a[204:207], v[212:215]\n"
"ds_read_b64_tr_b16 a[0:1], v4 offset:24768\n"
"v_mul_f32 v180, s82, v180\n"
"v_mfma_f32_16x16x32_bf16 v[216:219], a[8:11], a[192:195], v[216:219]\n"
"ds_read_b64_tr_b16 a[2:3], v4 offset:25280\n"
"v_mul_f32 v181, s82, v181\n"
"v_mfma_f32_16x16x32_bf16 v[216:219], a[12:15], a[208:211], v[216:219]\n"
"ds_read_b64_tr_b16 a[4:5], v4 offset:28896\n"
"v_mul_f32 v182, s82, v182\n"
"v_mfma_f32_16x16x32_bf16 v[220:223], a[12:15], a[212:215], v[220:223]\n"
"buffer_load_dwordx4 v13, s[44:47], s81 offen offset:2048 nt lds\n"
"v_mfma_f32_16x16x32_bf16 v[220:223], a[8:11], a[196:199], v[220:223]\n"
"ds_read_b64_tr_b16 a[6:7], v4 offset:29408\n"
"v_mul_f32 v183, s82, v183\n"
"v_mfma_f32_16x16x32_bf16 v[224:227], a[8:11], a[200:203], v[224:227]\n"
"ds_read_b128 a[140:143], v24 offset:1024\n"
"v_mul_f32 v192, s82, v192\n"
"v_mfma_f32_16x16x32_bf16 v[224:227], a[12:15], a[216:219], v[224:227]\n"
"v_mul_f32 v193, s82, v193\n"
"v_mul_f32 v194, s82, v194\n"
"v_mfma_f32_16x16x32_bf16 v[228:231], a[12:15], a[220:223], v[228:231]\n"
"s_waitcnt lgkmcnt(4)\n"
"ds_read_b64_tr_b16 a[12:13], v4 offset:28904\n"
"v_mul_f32 v195, s82, v195\n"
"v_mfma_f32_16x16x32_bf16 v[228:231], a[8:11], a[204:207], v[228:231]\n"
"ds_read_b64_tr_b16 a[8:9], v4 offset:24776\n"
"v_mul_f32 v148, s82, v148\n"
"v_mfma_f32_16x16x32_bf16 v[232:235], a[16:19], a[192:195], v[232:235]\n"
"ds_read_b64_tr_b16 a[10:11], v4 offset:25288\n"
"v_mul_f32 v149, s82, v149\n"
"v_mfma_f32_16x16x32_bf16 v[232:235], a[20:23], a[208:211], v[232:235]\n"
"s_waitcnt lgkmcnt(3)\n"
"ds_read_b64_tr_b16 a[14:15], v4 offset:29416\n"
"v_mul_f32 v150, s82, v150\n"
"v_mfma_f32_16x16x32_bf16 v[236:239], a[20:23], a[212:215], v[236:239]\n"
"v_mul_f32 v151, s82, v151\n"
"v_mul_f32 v164, s82, v164\n"
"v_mfma_f32_16x16x32_bf16 v[236:239], a[16:19], a[196:199], v[236:239]\n"
"v_mul_f32 v165, s82, v165\n"
"v_mul_f32 v166, s82, v166\n"
"v_mfma_f32_16x16x32_bf16 v[240:243], a[16:19], a[200:203], v[240:243]\n"
"ds_read_b64_tr_b16 a[16:17], v5 offset:24768\n"
"v_mul_f32 v167, s82, v167\n"
"v_mfma_f32_16x16x32_bf16 v[240:243], a[20:23], a[216:219], v[240:243]\n"
"ds_read_b64_tr_b16 a[18:19], v5 offset:25280\n"
"v_accvgpr_write_b32 a52, v176\n"
"v_mfma_f32_16x16x32_bf16 v[244:247], a[24:27], a[192:195], v[244:247]\n"
"ds_read_b64_tr_b16 a[20:21], v5 offset:28896\n"
"v_accvgpr_write_b32 a53, v178\n"
"v_mfma_f32_16x16x32_bf16 v[244:247], a[28:31], a[208:211], v[244:247]\n"
"ds_read_b64_tr_b16 a[22:23], v5 offset:29408\n"
"v_accvgpr_write_b32 a54, v188\n"
"v_mfma_f32_16x16x32_bf16 v[248:251], a[28:31], a[212:215], v[248:251]\n"
"buffer_load_dwordx4 v14, s[44:47], s81 offen offset:3072 nt lds\n"
"v_mfma_f32_16x16x32_bf16 v[248:251], a[24:27], a[196:199], v[248:251]\n"
"v_accvgpr_write_b32 a55, v190\n"
"v_cvt_pk_bf16_f32 v180, v180, v181\n"
"v_mfma_f32_16x16x32_bf16 v[252:255], a[24:27], a[200:203], v[252:255]\n"
"ds_read_b64_tr_b16 a[24:25], v5 offset:24776\n"
"v_cvt_pk_bf16_f32 v182, v182, v183\n"
"v_mfma_f32_16x16x32_bf16 v[252:255], a[28:31], a[216:219], v[252:255]\n"
"ds_read_b64_tr_b16 a[26:27], v5 offset:25288\n"
"v_cvt_pk_bf16_f32 v192, v192, v193\n"
"v_mfma_f32_16x16x32_bf16 v[200:203], a[0:3], a[224:227], v[200:203]\n"
"ds_read_b64_tr_b16 a[28:29], v5 offset:28904\n"
"v_cvt_pk_bf16_f32 v194, v194, v195\n"
"v_mfma_f32_16x16x32_bf16 v[200:203], a[4:7], a[240:243], v[200:203]\n"
"ds_read_b64_tr_b16 a[30:31], v5 offset:29416\n"
"v_cvt_pk_bf16_f32 v148, v148, v149\n"
"v_mfma_f32_16x16x32_bf16 v[204:207], a[4:7], a[244:247], v[204:207]\n"
"v_cvt_pk_bf16_f32 v150, v150, v151\n"
"v_cvt_pk_bf16_f32 v164, v164, v165\n"
"v_mfma_f32_16x16x32_bf16 v[204:207], a[0:3], a[228:231], v[204:207]\n"
"v_cvt_pk_bf16_f32 v166, v166, v167\n"
"v_mul_f32 v152, s82, v152\n"
"v_mfma_f32_16x16x32_bf16 v[208:211], a[0:3], a[232:235], v[208:211]\n"
"v_mul_f32 v153, s82, v153\n"
"v_mul_f32 v154, s82, v154\n"
"v_mfma_f32_16x16x32_bf16 v[208:211], a[4:7], a[248:251], v[208:211]\n"
"v_mul_f32 v155, s82, v155\n"
"v_mul_f32 v168, s82, v168\n"
"v_mfma_f32_16x16x32_bf16 v[212:215], a[4:7], a[252:255], v[212:215]\n"
"s_waitcnt lgkmcnt(2)\n"
"v_mul_f32 v169, s82, v169\n"
"v_mul_f32 v170, s82, v170\n"
"v_mfma_f32_16x16x32_bf16 v[212:215], a[0:3], a[236:239], v[212:215]\n"
"PG4_%=:\n"
"s_cbranch_execz PG8_%=\n"
"s_addk_i32 m0, 8256\n"
"v_mul_f32 v171, s82, v171\n"
"v_accvgpr_write_b32 a84, v180\n"
"v_mfma_f32_16x16x32_bf16 v[216:219], a[8:11], a[224:227], v[216:219]\n"
"s_memrealtime s[2:3]\n"
"v_accvgpr_write_b32 a85, v182\n"
"v_accvgpr_write_b32 a86, v192\n"
"v_mfma_f32_16x16x32_bf16 v[216:219], a[12:15], a[240:243], v[216:219]\n"
"s_addk_i32 s81, 16384\n"
"buffer_load_dwordx4 v11, s[44:47], s81 offen offset:0 nt lds\n"
"v_mfma_f32_16x16x32_bf16 v[220:223], a[12:15], a[244:247], v[220:223]\n"
"v_accvgpr_write_b32 a87, v194\n"
"v_accvgpr_write_b32 a80, v148\n"
"v_mfma_f32_16x16x32_bf16 v[220:223], a[8:11], a[228:231], v[220:223]\n"
"v_accvgpr_write_b32 a81, v150\n"
"v_accvgpr_write_b32 a82, v164\n"
"v_mfma_f32_16x16x32_bf16 v[224:227], a[8:11], a[232:235], v[224:227]\n"
"v_accvgpr_write_b32 a83, v166\n"
"v_cvt_pk_bf16_f32 v152, v152, v153\n"
"v_mfma_f32_16x16x32_bf16 v[224:227], a[12:15], a[248:251], v[224:227]\n"
"v_cvt_pk_bf16_f32 v154, v154, v155\n"
"v_cvt_pk_bf16_f32 v168, v168, v169\n"
"v_mfma_f32_16x16x32_bf16 v[228:231], a[12:15], a[252:255], v[228:231]\n"
"v_cvt_pk_bf16_f32 v170, v170, v171\n"
"v_mul_f32 v184, s82, v184\n"
"v_mfma_f32_16x16x32_bf16 v[228:231], a[8:11], a[236:239], v[228:231]\n"
"s_waitcnt lgkmcnt(0)\n"
"v_mul_f32 v185, s82, v185\n"
"v_mul_f32 v186, s82, v186\n"
"v_mfma_f32_16x16x32_bf16 v[232:235], a[16:19], a[224:227], v[232:235]\n"
"s_barrier\n"
"v_mfma_f32_16x16x32_bf16 v[232:235], a[20:23], a[240:243], v[232:235]\n"
"s_addk_i32 s79, 64\n"
"ds_read_b128 a[0:3], v15 offset:41280\n"
"v_mul_f32 v187, s82, v187\n"
"v_mfma_f32_16x16x32_bf16 v[236:239], a[20:23], a[244:247], v[236:239]\n"
"ds_read_b128 a[4:7], v16 offset:41280\n"
"v_mul_f32 v196, s82, v196\n"
"v_mfma_f32_16x16x32_bf16 v[236:239], a[16:19], a[228:231], v[236:239]\n"
"s_sub_u32 s0, s79, s2\n"
"ds_read_b128 a[148:151], v24 offset:0\n"
"v_mul_f32 v197, s82, v197\n"
"v_mfma_f32_16x16x32_bf16 v[240:243], a[16:19], a[232:235], v[240:243]\n"
"ds_read_b128 a[8:11], v17 offset:41280\n"
"v_mul_f32 v198, s82, v198\n"
"v_mfma_f32_16x16x32_bf16 v[240:243], a[20:23], a[248:251], v[240:243]\n"
"s_sub_u32 s0, s0, s23\n"
"v_mul_f32 v199, s82, v199\n"
"v_accvgpr_write_b32 a112, v152\n"
"v_mfma_f32_16x16x32_bf16 v[244:247], a[24:27], a[224:227], v[244:247]\n"
"buffer_load_dwordx4 v12, s[44:47], s81 offen offset:1024 nt lds\n"
"v_mfma_f32_16x16x32_bf16 v[244:247], a[28:31], a[240:243], v[244:247]\n"
"s_sub_u32 s79, s77, s2\n"
"ds_read_b128 a[12:15], v18 offset:41280\n"
"v_accvgpr_write_b32 a113, v154\n"
"v_mfma_f32_16x16x32_bf16 v[248:251], a[28:31], a[244:247], v[248:251]\n"
"s_waitcnt lgkmcnt(2)\n"
"v_accvgpr_write_b32 a114, v168\n"
"v_accvgpr_write_b32 a115, v170\n"
"v_mfma_f32_16x16x32_bf16 v[248:251], a[24:27], a[228:231], v[248:251]\n"
"s_addk_i32 s79, 1085\n"
"v_cvt_pk_bf16_f32 v184, v184, v185\n"
"v_cvt_pk_bf16_f32 v186, v186, v187\n"
"v_mfma_f32_16x16x32_bf16 v[252:255], a[24:27], a[232:235], v[252:255]\n"
"v_cvt_pk_bf16_f32 v196, v196, v197\n"
"v_cvt_pk_bf16_f32 v198, v198, v199\n"
"v_mfma_f32_16x16x32_bf16 v[252:255], a[28:31], a[248:251], v[252:255]\n"
"v_accvgpr_write_b32 a116, v184\n"
"v_accvgpr_write_b32 a117, v186\n"
"v_mfma_f32_16x16x32_bf16 v[48:51], a[0:3], a[32:35], v[32:35]\n"
"s_max_i32 s0, s0, s79\n"
"ds_read_b128 a[16:19], v15 offset:41536\n"
"v_accvgpr_write_b32 a118, v196\n"
"v_mfma_f32_16x16x32_bf16 v[48:51], a[4:7], a[36:39], v[48:51]\n"
"v_accvgpr_write_b32 a119, v198\n"
"v_mul_f32 v212, s82, v212\n"
"v_mfma_f32_16x16x32_bf16 v[52:55], a[4:7], a[68:71], v[36:39]\n"
"s_min_i32 s0, s0, 40\n"
"v_mul_f32 v213, s82, v213\n"
"v_mul_f32 v214, s82, v214\n"
"v_mfma_f32_16x16x32_bf16 v[52:55], a[0:3], a[64:67], v[52:55]\n"
"ds_read_b128 a[20:23], v16 offset:41536\n"
"v_mul_f32 v215, s82, v215\n"
"v_mfma_f32_16x16x32_bf16 v[56:59], a[0:3], a[96:99], v[40:43]\n"
"s_max_i32 s0, s0, 0\n"
"v_mul_f32 v228, s82, v228\n"
"v_mul_f32 v229, s82, v229\n"
"v_mfma_f32_16x16x32_bf16 v[56:59], a[4:7], a[100:103], v[56:59]\n"
"v_mul_f32 v230, s82, v230\n"
"v_mul_f32 v231, s82, v231\n"
"v_mfma_f32_16x16x32_bf16 v[60:63], a[4:7], a[132:135], v[44:47]\n"
"s_waitcnt lgkmcnt(2)\n"
"v_cvt_pk_bf16_f32 v212, v212, v213\n"
"v_cvt_pk_bf16_f32 v214, v214, v215\n"
"v_mfma_f32_16x16x32_bf16 v[60:63], a[0:3], a[128:131], v[60:63]\n"
"s_add_u32 s79, s2, s0\n"
"v_cvt_pk_bf16_f32 v228, v228, v229\n"
"v_cvt_pk_bf16_f32 v230, v230, v231\n"
"v_mfma_f32_16x16x32_bf16 v[48:51], a[8:11], a[40:43], v[48:51]\n"
"buffer_load_dwordx4 v13, s[44:47], s81 offen offset:2048 nt lds\n"
"v_mfma_f32_16x16x32_bf16 v[48:51], a[12:15], a[44:47], v[48:51]\n"
"s_lshr_b64 vcc, s[0:1], 2\n"
"ds_read_b128 a[24:27], v19 offset:41280\n"
"v_accvgpr_write_b32 a152, v212\n"
"v_mfma_f32_16x16x32_bf16 v[52:55], a[12:15], a[76:79], v[52:55]\n"
"v_accvgpr_write_b32 a153, v214\n"
"v_accvgpr_write_b32 a154, v228\n"
"v_mfma_f32_16x16x32_bf16 v[52:55], a[8:11], a[72:75], v[52:55]\n"
"s_cbranch_vccnz PC14_%=\n"
"PB14_%=:\n"
"v_accvgpr_write_b32 a155, v230\n"
"v_mfma_f32_16x16x32_bf16 v[56:59], a[8:11], a[104:107], v[56:59]\n"
"ds_read_b128 a[28:31], v20 offset:41280\n"
"v_mul_f32 v200, s82, v200\n"
"v_mfma_f32_16x16x32_bf16 v[56:59], a[12:15], a[108:111], v[56:59]\n"
"ds_write_b128 v23, a[152:155] offset:1024\n"
"v_mfma_f32_16x16x32_bf16 v[60:63], a[12:15], a[140:143], v[60:63]\n"
"s_waitcnt lgkmcnt(3)\n"
"ds_read_b128 a[0:3], v21 offset:41280\n"
"v_mul_f32 v201, s82, v201\n"
"v_mfma_f32_16x16x32_bf16 v[60:63], a[8:11], a[136:139], v[60:63]\n"
"v_mul_f32 v202, s82, v202\n"
"v_mul_f32 v203, s82, v203\n"
"v_mfma_f32_16x16x32_bf16 v[48:51], a[16:19], a[48:51], v[48:51]\n"
"v_mul_f32 v216, s82, v216\n"
"v_mul_f32 v217, s82, v217\n"
"v_mfma_f32_16x16x32_bf16 v[48:51], a[20:23], a[52:55], v[48:51]\n"
"ds_read_b128 a[4:7], v22 offset:41280\n"
"v_mul_f32 v218, s82, v218\n"
"v_mfma_f32_16x16x32_bf16 v[52:55], a[20:23], a[84:87], v[52:55]\n"
"s_waitcnt lgkmcnt(2)\n"
"v_mul_f32 v219, s82, v219\n"
"v_cvt_pk_bf16_f32 v200, v200, v201\n"
"v_mfma_f32_16x16x32_bf16 v[52:55], a[16:19], a[80:83], v[52:55]\n"
"s_barrier\n"
"v_mfma_f32_16x16x32_bf16 v[56:59], a[16:19], a[112:115], v[56:59]\n"
"ds_read_b128 a[8:11], v19 offset:41536\n"
"v_cvt_pk_bf16_f32 v202, v202, v203\n"
"v_mfma_f32_16x16x32_bf16 v[56:59], a[20:23], a[116:119], v[56:59]\n"
"buffer_load_dwordx4 v14, s[44:47], s81 offen offset:3072 nt lds\n"
"v_mfma_f32_16x16x32_bf16 v[60:63], a[20:23], a[148:151], v[60:63]\n"
"ds_read_b128 a[156:159], v24 offset:1024\n"
"v_cvt_pk_bf16_f32 v216, v216, v217\n"
"v_mfma_f32_16x16x32_bf16 v[60:63], a[16:19], a[144:147], v[60:63]\n"
"ds_read_b128 a[12:15], v20 offset:41536\n"
"v_cvt_pk_bf16_f32 v218, v218, v219\n"
"v_mfma_f32_16x16x32_bf16 v[64:67], a[24:27], a[32:35], v[32:35]\n"
"v_mul_f32 v232, s82, v232\n"
"v_mul_f32 v233, s82, v233\n"
"v_mfma_f32_16x16x32_bf16 v[64:67], a[28:31], a[36:39], v[64:67]\n"
"v_mul_f32 v234, s82, v234\n"
"v_mul_f32 v235, s82, v235\n"
"v_mfma_f32_16x16x32_bf16 v[68:71], a[28:31], a[68:71], v[36:39]\n"
"ds_read_b128 a[16:19], v21 offset:41536\n"
"v_mul_f32 v244, s82, v244\n"
"v_mfma_f32_16x16x32_bf16 v[68:71], a[24:27], a[64:67], v[68:71]\n"
"v_mul_f32 v245, s82, v245\n"
"v_mul_f32 v246, s82, v246\n"
"v_mfma_f32_16x16x32_bf16 v[72:75], a[24:27], a[96:99], v[40:43]\n"
"s_waitcnt lgkmcnt(3)\n"
"v_mul_f32 v247, s82, v247\n"
"v_accvgpr_write_b32 a56, v200\n"
"v_mfma_f32_16x16x32_bf16 v[72:75], a[28:31], a[100:103], v[72:75]\n"
"ds_read_b128 a[20:23], v22 offset:41536\n"
"v_accvgpr_write_b32 a57, v202\n"
"v_mfma_f32_16x16x32_bf16 v[64:67], a[0:3], a[40:43], v[64:67]\n"
"v_accvgpr_write_b32 a58, v216\n"
"v_accvgpr_write_b32 a59, v218\n"
"v_mfma_f32_16x16x32_bf16 v[64:67], a[4:7], a[44:47], v[64:67]\n"
"s_addk_i32 m0, 8256\n"
"v_cvt_pk_bf16_f32 v232, v232, v233\n"
"v_cvt_pk_bf16_f32 v234, v234, v235\n"
"v_mfma_f32_16x16x32_bf16 v[68:71], a[4:7], a[76:79], v[68:71]\n"
"s_addk_i32 s81, 16384\n"
"ds_read_b128 a[24:27], v17 offset:41536\n"
"v_cvt_pk_bf16_f32 v244, v244, v245\n"
"v_mfma_f32_16x16x32_bf16 v[68:71], a[0:3], a[72:75], v[68:71]\n"
"v_cvt_pk_bf16_f32 v246, v246, v247\n"
"v_mul_f32 v236, s82, v236\n"
"v_mfma_f32_16x16x32_bf16 v[72:75], a[0:3], a[104:107], v[72:75]\n"
"s_waitcnt lgkmcnt(2)\n"
"buffer_load_dwordx4 v11, s[44:47], s81 offen offset:0 nt lds\n"
"v_mfma_f32_16x16x32_bf16 v[72:75], a[4:7], a[108:111], v[72:75]\n"
"s_memrealtime s[2:3]\n"
"ds_read_b128 a[28:31], v18 offset:41536\n"
"v_mul_f32 v237, s82, v237\n"
"v_mfma_f32_16x16x32_bf16 v[64:67], a[8:11], a[48:51], v[64:67]\n"
"v_mul_f32 v238, s82, v238\n"
"v_mul_f32 v239, s82, v239\n"
"v_mfma_f32_16x16x32_bf16 v[64:67], a[12:15], a[52:55], v[64:67]\n"
"v_mul_f32 v248, s82, v248\n"
"v_mul_f32 v249, s82, v249\n"
"v_mfma_f32_16x16x32_bf16 v[68:71], a[12:15], a[84:87], v[68:71]\n"
"v_mul_f32 v250, s82, v250\n"
"v_mul_f32 v251, s82, v251\n"
"v_mfma_f32_16x16x32_bf16 v[68:71], a[8:11], a[80:83], v[68:71]\n"
"v_mul_f32 v204, s82, v204\n"
"v_mul_f32 v205, s82, v205\n"
"v_mfma_f32_16x16x32_bf16 v[72:75], a[8:11], a[112:115], v[72:75]\n"
"s_waitcnt lgkmcnt(2)\n"
"v_mul_f32 v206, s82, v206\n"
"v_mul_f32 v207, s82, v207\n"
"v_mfma_f32_16x16x32_bf16 v[72:75], a[12:15], a[116:119], v[72:75]\n"
"v_mul_f32 v220, s82, v220\n"
"v_mul_f32 v221, s82, v221\n"
"v_mfma_f32_16x16x32_bf16 v[64:67], a[16:19], a[56:59], v[64:67]\n"
"v_mul_f32 v222, s82, v222\n"
"v_mul_f32 v223, s82, v223\n"
"v_accvgpr_write_b32 a60, v232\n"
"v_accvgpr_write_b32 a61, v234\n"
"v_accvgpr_write_b32 a62, v244\n"
"v_accvgpr_write_b32 a63, v246\n"
"s_nop 1\n"
"v_mfma_f32_16x16x32_bf16 v[64:67], a[20:23], a[60:63], v[64:67]\n"
"s_waitcnt lgkmcnt(0)\n"
"v_cvt_pk_bf16_f32 v236, v236, v237\n"
"v_cvt_pk_bf16_f32 v238, v238, v239\n"
"v_accvgpr_write_b32 a92, v236\n"
"v_accvgpr_write_b32 a93, v238\n"
"v_cvt_pk_bf16_f32 v248, v248, v249\n"
"v_accvgpr_write_b32 a94, v248\n"
"v_cvt_pk_bf16_f32 v250, v250, v251\n"
"v_accvgpr_write_b32 a95, v250\n"
"s_nop 1\n"
"v_mfma_f32_16x16x32_bf16 v[68:71], a[20:23], a[92:95], v[68:71]\n"
"s_barrier\n"
"v_cvt_pk_bf16_f32 v204, v204, v205\n"
"v_accvgpr_write_b32 a88, v204\n"
"v_cvt_pk_bf16_f32 v206, v206, v207\n"
"v_accvgpr_write_b32 a89, v206\n"
"v_cvt_pk_bf16_f32 v220, v220, v221\n"
"v_accvgpr_write_b32 a90, v220\n"
"v_cvt_pk_bf16_f32 v222, v222, v223\n"
"v_accvgpr_write_b32 a91, v222\n"
"s_nop 1\n"
"v_mfma_f32_16x16x32_bf16 v[68:71], a[16:19], a[88:91], v[68:71]\n"
"s_addk_i32 s79, 64\n"
"ds_read_b128 a[0:3], v15 offset:49536\n"
"v_mul_f32 v208, s82, v208\n"
"v_mul_f32 v209, s82, v209\n"
"v_cvt_pk_bf16_f32 v208, v208, v209\n"
"v_accvgpr_write_b32 a120, v208\n"
"v_mul_f32 v210, s82, v210\n"
"v_mul_f32 v211, s82, v211\n"
"v_cvt_pk_bf16_f32 v210, v210, v211\n"
"v_accvgpr_write_b32 a121, v210\n"
"v_mul_f32 v224, s82, v224\n"
"v_mul_f32 v225, s82, v225\n"
"v_cvt_pk_bf16_f32 v224, v224, v225\n"
"v_accvgpr_write_b32 a122, v224\n"
"v_mul_f32 v226, s82, v226\n"
"v_mul_f32 v227, s82, v227\n"
"v_cvt_pk_bf16_f32 v226, v226, v227\n"
"v_accvgpr_write_b32 a123, v226\n"
"s_nop 1\n"
"v_mfma_f32_16x16x32_bf16 v[72:75], a[16:19], a[120:123], v[72:75]\n"
"ds_read_b128 a[4:7], v16 offset:49536\n"
"v_mul_f32 v240, s82, v240\n"
"v_mul_f32 v241, s82, v241\n"
"v_cvt_pk_bf16_f32 v240, v240, v241\n"
"v_accvgpr_write_b32 a124, v240\n"
"v_mul_f32 v242, s82, v242\n"
"v_mul_f32 v243, s82, v243\n"
"v_cvt_pk_bf16_f32 v242, v242, v243\n"
"v_accvgpr_write_b32 a125, v242\n"
"v_mul_f32 v252, s82, v252\n"
"v_mul_f32 v253, s82, v253\n"
"v_cvt_pk_bf16_f32 v252, v252, v253\n"
"v_accvgpr_write_b32 a126, v252\n"
"v_mul_f32 v254, s82, v254\n"
"v_mul_f32 v255, s82, v255\n"
"v_cvt_pk_bf16_f32 v254, v254, v255\n"
"v_accvgpr_write_b32 a127, v254\n"
"s_nop 1\n"
"v_mfma_f32_16x16x32_bf16 v[72:75], a[20:23], a[124:127], v[72:75]\n"
"s_sub_u32 s0, s79, s2\n"
"v_exp_f32 v64, v64\n"
"v_mfma_f32_16x16x32_bf16 v[48:51], a[24:27], a[56:59], v[48:51]\n"
"buffer_load_dwordx4 v12, s[44:47], s81 offen offset:1024 nt lds\n"
"v_mfma_f32_16x16x32_bf16 v[48:51], a[28:31], a[60:63], v[48:51]\n"
"s_sub_u32 s0, s0, s23\n"
"ds_read_b128 a[8:11], v17 offset:49536\n"
"v_mov_b32 v104, 0x3f803f80\n"
"v_mfma_f32_16x16x32_bf16 v[52:55], a[28:31], a[92:95], v[52:55]\n"
"v_exp_f32 v65, v65\n"
"v_mfma_f32_16x16x32_bf16 v[52:55], a[24:27], a[88:91], v[52:55]\n"
"s_max_i32 s0, s0, 0\n"
"ds_read_b128 a[12:15], v18 offset:49536\n"
"v_cvt_pk_bf16_f32 v64, v64, v65\n"
"v_mfma_f32_16x16x32_bf16 v[56:59], a[24:27], a[120:123], v[56:59]\n"
"v_exp_f32 v48, v48\n"
"v_mfma_f32_16x16x32_bf16 v[56:59], a[28:31], a[124:127], v[56:59]\n"
"s_add_u32 s79, s2, s0\n"
"v_exp_f32 v49, v49\n"
"v_mfma_f32_16x16x32_bf16 v[60:63], a[28:31], a[156:159], v[60:63]\n"
"s_waitcnt lgkmcnt(2)\n"
"v_cvt_pk_bf16_f32 v108, v48, v49\n"
"v_accvgpr_write_b32 a180, v64\n"
"v_mfma_f32_16x16x32_bf16 v[60:63], a[24:27], a[152:155], v[60:63]\n"
"s_lshr_b64 vcc, s[0:1], 2\n"
"v_exp_f32 v50, v50\n"
"v_mfma_f32_16x16x32_bf16 v[80:83], a[0:3], a[32:35], v[32:35]\n"
"ds_read_b128 a[16:19], v15 offset:49792\n"
"v_accvgpr_write_b32 a176, v104\n"
"v_mfma_f32_16x16x32_bf16 v[80:83], a[4:7], a[36:39], v[80:83]\n"
"s_cbranch_vccnz PC15_%=\n"
"PB15_%=:\n"
"v_accvgpr_write_b32 a177, v104\n"
"v_mfma_f32_16x16x32_bf16 v[84:87], a[4:7], a[68:71], v[36:39]\n"
"v_exp_f32 v51, v51\n"
"v_mfma_f32_16x16x32_bf16 v[84:87], a[0:3], a[64:67], v[84:87]\n"
"ds_read_b128 a[20:23], v16 offset:49792\n"
"v_cvt_pk_bf16_f32 v109, v50, v51\n"
"v_mfma_f32_16x16x32_bf16 v[88:91], a[0:3], a[96:99], v[40:43]\n"
"v_exp_f32 v52, v52\n"
"v_mfma_f32_16x16x32_bf16 v[88:91], a[4:7], a[100:103], v[88:91]\n"
"buffer_load_dwordx4 v13, s[44:47], s81 offen offset:2048 nt lds\n"
"v_mfma_f32_16x16x32_bf16 v[92:95], a[4:7], a[132:135], v[44:47]\n"
"s_waitcnt lgkmcnt(2)\n"
"v_exp_f32 v53, v53\n"
"v_mfma_f32_16x16x32_bf16 v[92:95], a[0:3], a[128:131], v[92:95]\n"
"v_exp_f32 v54, v54\n"
"v_mfma_f32_16x16x32_bf16 v[80:83], a[8:11], a[40:43], v[80:83]\n"
"ds_read_b128 a[24:27], v17 offset:49792\n"
"v_cvt_pk_bf16_f32 v112, v52, v53\n"
"v_mfma_f32_16x16x32_bf16 v[80:83], a[12:15], a[44:47], v[80:83]\n"
"v_exp_f32 v55, v55\n"
"v_mfma_f32_16x16x32_bf16 v[84:87], a[12:15], a[76:79], v[84:87]\n"
"v_exp_f32 v56, v56\n"
"v_mfma_f32_16x16x32_bf16 v[84:87], a[8:11], a[72:75], v[84:87]\n"
"ds_read_b128 a[28:31], v18 offset:49792\n"
"v_cvt_pk_bf16_f32 v113, v54, v55\n"
"v_mfma_f32_16x16x32_bf16 v[88:91], a[8:11], a[104:107], v[88:91]\n"
"v_exp_f32 v57, v57\n"
"v_mfma_f32_16x16x32_bf16 v[88:91], a[12:15], a[108:111], v[88:91]\n"
"v_exp_f32 v58, v58\n"
"v_mfma_f32_16x16x32_bf16 v[92:95], a[12:15], a[140:143], v[92:95]\n"
"s_waitcnt lgkmcnt(2)\n"
"v_cvt_pk_bf16_f32 v116, v56, v57\n"
"v_accvgpr_write_b32 a178, v104\n"
"v_mfma_f32_16x16x32_bf16 v[92:95], a[8:11], a[136:139], v[92:95]\n"
"v_exp_f32 v59, v59\n"
"v_mfma_f32_16x16x32_bf16 v[80:83], a[16:19], a[48:51], v[80:83]\n"
"ds_read_b128 a[0:3], v19 offset:49536\n"
"v_cvt_pk_bf16_f32 v117, v58, v59\n"
"v_mfma_f32_16x16x32_bf16 v[80:83], a[20:23], a[52:55], v[80:83]\n"
"v_exp_f32 v66, v66\n"
"v_mfma_f32_16x16x32_bf16 v[84:87], a[20:23], a[84:87], v[84:87]\n"
"buffer_load_dwordx4 v14, s[44:47], s81 offen offset:3072 nt lds\n"
"v_mfma_f32_16x16x32_bf16 v[84:87], a[16:19], a[80:83], v[84:87]\n"
"ds_read_b128 a[4:7], v20 offset:49536\n"
"v_accvgpr_write_b32 a179, v104\n"
"v_mfma_f32_16x16x32_bf16 v[88:91], a[16:19], a[112:115], v[88:91]\n"
"v_exp_f32 v67, v67\n"
"v_mfma_f32_16x16x32_bf16 v[88:91], a[20:23], a[116:119], v[88:91]\n"
"v_exp_f32 v68, v68\n"
"v_mfma_f32_16x16x32_bf16 v[92:95], a[20:23], a[148:151], v[92:95]\n"
"s_waitcnt lgkmcnt(2)\n"
"ds_read_b128 a[8:11], v21 offset:49536\n"
"v_cvt_pk_bf16_f32 v66, v66, v67\n"
"v_mfma_f32_16x16x32_bf16 v[92:95], a[16:19], a[144:147], v[92:95]\n"
"v_exp_f32 v69, v69\n"
"v_mfma_f32_16x16x32_bf16 v[80:83], a[24:27], a[56:59], v[80:83]\n"
"v_exp_f32 v70, v70\n"
"v_mfma_f32_16x16x32_bf16 v[80:83], a[28:31], a[60:63], v[80:83]\n"
"ds_read_b128 a[12:15], v22 offset:49536\n"
"v_accvgpr_write_b32 a181, v66\n"
"v_mfma_f32_16x16x32_bf16 v[84:87], a[28:31], a[92:95], v[84:87]\n"
"v_exp_f32 v71, v71\n"
"v_mfma_f32_16x16x32_bf16 v[84:87], a[24:27], a[88:91], v[84:87]\n"
"v_exp_f32 v72, v72\n"
"v_mfma_f32_16x16x32_bf16 v[88:91], a[24:27], a[120:123], v[88:91]\n"
"s_addk_i32 m0, 8256\n"
"ds_read_b128 a[16:19], v19 offset:49792\n"
"v_cvt_pk_bf16_f32 v68, v68, v69\n"
"v_mfma_f32_16x16x32_bf16 v[88:91], a[28:31], a[124:127], v[88:91]\n"
"s_addk_i32 s81, 16384\n"
"v_exp_f32 v73, v73\n"
"v_mfma_f32_16x16x32_bf16 v[92:95], a[28:31], a[156:159], v[92:95]\n"
"s_waitcnt lgkmcnt(2)\n"
"v_exp_f32 v74, v74\n"
"v_mfma_f32_16x16x32_bf16 v[92:95], a[24:27], a[152:155], v[92:95]\n"
"buffer_load_dwordx4 v11, s[44:47], s81 offen offset:0 nt lds\n"
"v_mfma_f32_16x16x32_bf16 v[48:51], a[0:3], a[32:35], v[32:35]\n"
"ds_read_b128 a[20:23], v20 offset:49792\n"
"v_cvt_pk_bf16_f32 v70, v70, v71\n"
"v_mfma_f32_16x16x32_bf16 v[48:51], a[4:7], a[36:39], v[48:51]\n"
"v_exp_f32 v75, v75\n"
"v_mfma_f32_16x16x32_bf16 v[52:55], a[4:7], a[68:71], v[36:39]\n"
"ds_read_b128 a[24:27], v21 offset:49792\n"
"v_accvgpr_write_b32 a184, v68\n"
"v_mfma_f32_16x16x32_bf16 v[52:55], a[0:3], a[64:67], v[52:55]\n"
"v_exp_f32 v80, v80\n"
"v_mfma_f32_16x16x32_bf16 v[56:59], a[0:3], a[96:99], v[40:43]\n"
"s_waitcnt lgkmcnt(2)\n"
"v_exp_f32 v81, v81\n"
"v_mfma_f32_16x16x32_bf16 v[56:59], a[4:7], a[100:103], v[56:59]\n"
"s_memrealtime s[2:3]\n"
"ds_read_b128 a[28:31], v22 offset:49792\n"
"v_accvgpr_write_b32 a185, v70\n"
"v_mfma_f32_16x16x32_bf16 v[48:51], a[8:11], a[40:43], v[48:51]\n"
"v_exp_f32 v82, v82\n"
"v_mfma_f32_16x16x32_bf16 v[48:51], a[12:15], a[44:47], v[48:51]\n"
"v_exp_f32 v83, v83\n"
"v_mfma_f32_16x16x32_bf16 v[52:55], a[12:15], a[76:79], v[52:55]\n"
"v_cvt_pk_bf16_f32 v124, v72, v73\n"
"v_cvt_pk_bf16_f32 v125, v74, v75\n"
"v_mfma_f32_16x16x32_bf16 v[52:55], a[8:11], a[72:75], v[52:55]\n"
"v_exp_f32 v84, v84\n"
"v_mfma_f32_16x16x32_bf16 v[56:59], a[8:11], a[104:107], v[56:59]\n"
"s_waitcnt lgkmcnt(2)\n"
"v_exp_f32 v85, v85\n"
"v_mfma_f32_16x16x32_bf16 v[56:59], a[12:15], a[108:111], v[56:59]\n"
"s_waitcnt lgkmcnt(0)\n"
"v_cvt_pk_bf16_f32 v110, v80, v81\n"
"v_cvt_pk_bf16_f32 v111, v82, v83\n"
"v_mfma_f32_16x16x32_bf16 v[48:51], a[16:19], a[48:51], v[48:51]\n"
"s_barrier\n"
"v_mfma_f32_16x16x32_bf16 v[48:51], a[20:23], a[52:55], v[48:51]\n"
"s_addk_i32 s79, 64\n"
"buffer_load_dwordx4 v12, s[44:47], s81 offen offset:1024 nt lds\n"
"v_mfma_f32_16x16x32_bf16 v[52:55], a[20:23], a[84:87], v[52:55]\n"
"ds_read_b128 a[0:3], v15 offset:57792\n"
"v_cvt_pk_bf16_f32 v114, v84, v85\n"
"v_mfma_f32_16x16x32_bf16 v[52:55], a[16:19], a[80:83], v[52:55]\n"
"s_sub_u32 s0, s79, s2\n"
"ds_read_b128 a[4:7], v16 offset:57792\n"
"v_lshl_add_u32 v103, v1, 2, s85\n"
"v_mfma_f32_16x16x32_bf16 v[56:59], a[16:19], a[112:115], v[56:59]\n"
"ds_read_b128 a[8:11], v17 offset:57792\n"
"s_mul_i32 s85, s61, -512\n"
"v_mfma_f32_16x16x32_bf16 v[56:59], a[20:23], a[116:119], v[56:59]\n"
"s_sub_u32 s0, s0, s23\n"
"v_exp_f32 v86, v86\n"
"v_mfma_f32_16x16x32_bf16 v[48:51], a[24:27], a[56:59], v[48:51]\n"
"s_add_u32 s85, s85, 256\n"
"v_exp_f32 v87, v87\n"
"v_mfma_f32_16x16x32_bf16 v[48:51], a[28:31], a[60:63], v[48:51]\n"
"s_max_i32 s0, s0, 0\n"
"ds_read_b128 a[12:15], v18 offset:57792\n"
"v_cvt_pk_bf16_f32 v115, v86, v87\n"
"v_mfma_f32_16x16x32_bf16 v[52:55], a[28:31], a[92:95], v[52:55]\n"
"v_exp_f32 v88, v88\n"
"v_mfma_f32_16x16x32_bf16 v[52:55], a[24:27], a[88:91], v[52:55]\n"
"s_add_u32 s79, s2, s0\n"
"v_exp_f32 v89, v89\n"
"v_mfma_f32_16x16x32_bf16 v[56:59], a[24:27], a[120:123], v[56:59]\n"
"s_waitcnt lgkmcnt(2)\n"
"v_cvt_pk_bf16_f32 v118, v88, v89\n"
"v_mfma_f32_16x16x32_bf16 v[56:59], a[28:31], a[124:127], v[56:59]\n"
"s_lshr_b64 vcc, s[0:1], 2\n"
"v_exp_f32 v90, v90\n"
"v_mfma_f32_16x16x32_bf16 v[64:67], a[0:3], a[32:35], v[32:35]\n"
"ds_read_b128 a[16:19], v15 offset:58048\n"
"v_add_u32 v15, s27, v15\n"
"v_mfma_f32_16x16x32_bf16 v[64:67], a[4:7], a[36:39], v[64:67]\n"
"s_cbranch_vccnz PC16_%=\n"
"PB16_%=:\n"
"v_lshrrev_b32 v27, 3, v1\n"
"v_mfma_f32_16x16x32_bf16 v[68:71], a[4:7], a[68:71], v[36:39]\n"
"buffer_load_dwordx4 v13, s[44:47], s81 offen offset:2048 nt lds\n"
"v_mfma_f32_16x16x32_bf16 v[68:71], a[0:3], a[64:67], v[68:71]\n"
"ds_read_b128 a[20:23], v16 offset:58048\n"
"v_add_u32 v16, s27, v16\n"
"v_mfma_f32_16x16x32_bf16 v[72:75], a[0:3], a[96:99], v[40:43]\n"
"v_exp_f32 v91, v91\n"
"v_mfma_f32_16x16x32_bf16 v[72:75], a[4:7], a[100:103], v[72:75]\n"
"v_exp_f32 v60, v60\n"
"v_mfma_f32_16x16x32_bf16 v[76:79], a[4:7], a[132:135], v[44:47]\n"
"s_waitcnt lgkmcnt(2)\n"
"v_cvt_pk_bf16_f32 v119, v90, v91\n"
"v_and_b32 v28, 6, v27\n"
"v_mfma_f32_16x16x32_bf16 v[76:79], a[0:3], a[128:131], v[76:79]\n"
"v_exp_f32 v61, v61\n"
"v_mfma_f32_16x16x32_bf16 v[64:67], a[8:11], a[40:43], v[64:67]\n"
"ds_read_b128 a[24:27], v17 offset:58048\n"
"v_cvt_pk_bf16_f32 v120, v60, v61\n"
"v_mfma_f32_16x16x32_bf16 v[64:67], a[12:15], a[44:47], v[64:67]\n"
"v_exp_f32 v62, v62\n"
"v_mfma_f32_16x16x32_bf16 v[68:71], a[12:15], a[76:79], v[68:71]\n"
"v_exp_f32 v63, v63\n"
"v_mfma_f32_16x16x32_bf16 v[68:71], a[8:11], a[72:75], v[68:71]\n"
"ds_read_b128 a[28:31], v18 offset:58048\n"
"v_cvt_pk_bf16_f32 v121, v62, v63\n"
"v_mfma_f32_16x16x32_bf16 v[72:75], a[8:11], a[104:107], v[72:75]\n"
"v_exp_f32 v92, v92\n"
"v_mfma_f32_16x16x32_bf16 v[72:75], a[12:15], a[108:111], v[72:75]\n"
"v_exp_f32 v93, v93\n"
"v_mfma_f32_16x16x32_bf16 v[76:79], a[12:15], a[140:143], v[76:79]\n"
"s_waitcnt lgkmcnt(2)\n"
"v_cvt_pk_bf16_f32 v122, v92, v93\n"
"v_add_u32 v17, s27, v17\n"
"v_mfma_f32_16x16x32_bf16 v[76:79], a[8:11], a[136:139], v[76:79]\n"
"buffer_load_dwordx4 v14, s[44:47], s81 offen offset:3072 nt lds\n"
"v_mfma_f32_16x16x32_bf16 v[64:67], a[16:19], a[48:51], v[64:67]\n"
"ds_read_b128 a[0:3], v19 offset:57792\n"
"v_add_u32 v18, s27, v18\n"
"v_mfma_f32_16x16x32_bf16 v[64:67], a[20:23], a[52:55], v[64:67]\n"
"v_exp_f32 v94, v94\n"
"v_mfma_f32_16x16x32_bf16 v[68:71], a[20:23], a[84:87], v[68:71]\n"
"v_exp_f32 v95, v95\n"
"v_mfma_f32_16x16x32_bf16 v[68:71], a[16:19], a[80:83], v[68:71]\n"
"ds_read_b128 a[4:7], v20 offset:57792\n"
"v_cvt_pk_bf16_f32 v123, v94, v95\n"
"v_mfma_f32_16x16x32_bf16 v[72:75], a[16:19], a[112:115], v[72:75]\n"
"v_exp_f32 v48, v48\n"
"v_mfma_f32_16x16x32_bf16 v[72:75], a[20:23], a[116:119], v[72:75]\n"
"v_exp_f32 v49, v49\n"
"v_mfma_f32_16x16x32_bf16 v[76:79], a[20:23], a[148:151], v[76:79]\n"
"s_waitcnt lgkmcnt(2)\n"
"ds_read_b128 a[8:11], v21 offset:57792\n"
"v_cvt_pk_bf16_f32 v48, v48, v49\n"
"v_mfma_f32_16x16x32_bf16 v[76:79], a[16:19], a[144:147], v[76:79]\n"
"v_exp_f32 v50, v50\n"
"v_mfma_f32_16x16x32_bf16 v[64:67], a[24:27], a[56:59], v[64:67]\n"
"v_exp_f32 v51, v51\n"
"v_mfma_f32_16x16x32_bf16 v[64:67], a[28:31], a[60:63], v[64:67]\n"
"ds_read_b128 a[12:15], v22 offset:57792\n"
"v_cvt_pk_bf16_f32 v50, v50, v51\n"
"v_mfma_f32_16x16x32_bf16 v[68:71], a[28:31], a[92:95], v[68:71]\n"
"s_addk_i32 m0, 16512\n"
"v_exp_f32 v52, v52\n"
"v_mfma_f32_16x16x32_bf16 v[68:71], a[24:27], a[88:91], v[68:71]\n"
"s_addk_i32 s81, 16384\n"
"v_exp_f32 v53, v53\n"
"v_mfma_f32_16x16x32_bf16 v[72:75], a[24:27], a[120:123], v[72:75]\n"
"ds_read_b128 a[16:19], v19 offset:58048\n"
"v_accvgpr_write_b32 a182, v48\n"
"v_mfma_f32_16x16x32_bf16 v[72:75], a[28:31], a[124:127], v[72:75]\n"
"buffer_load_dwordx4 v11, s[44:47], s81 offen offset:0 nt lds\n"
"v_mfma_f32_16x16x32_bf16 v[76:79], a[28:31], a[156:159], v[76:79]\n"
"s_waitcnt lgkmcnt(2)\n"
"v_exp_f32 v54, v54\n"
"v_mfma_f32_16x16x32_bf16 v[76:79], a[24:27], a[152:155], v[76:79]\n"
"ds_read_b128 a[20:23], v20 offset:58048\n"
"v_accvgpr_write_b32 a183, v50\n"
"v_mfma_f32_16x16x32_bf16 v[80:83], a[0:3], a[32:35], v[32:35]\n"
"v_exp_f32 v55, v55\n"
"v_mfma_f32_16x16x32_bf16 v[80:83], a[4:7], a[36:39], v[80:83]\n"
"v_exp_f32 v56, v56\n"
"v_mfma_f32_16x16x32_bf16 v[84:87], a[4:7], a[68:71], v[36:39]\n"
"v_cvt_pk_bf16_f32 v52, v52, v53\n"
"v_cvt_pk_bf16_f32 v54, v54, v55\n"
"v_mfma_f32_16x16x32_bf16 v[84:87], a[0:3], a[64:67], v[84:87]\n"
"v_exp_f32 v57, v57\n"
"v_mfma_f32_16x16x32_bf16 v[88:91], a[0:3], a[96:99], v[40:43]\n"
"s_waitcnt lgkmcnt(1)\n"
"v_exp_f32 v58, v58\n"
"v_mfma_f32_16x16x32_bf16 v[88:91], a[4:7], a[100:103], v[88:91]\n"
"s_memrealtime s[2:3]\n"
"v_accvgpr_write_b32 a186, v52\n"
"v_accvgpr_write_b32 a187, v54\n"
"v_mfma_f32_16x16x32_bf16 v[80:83], a[8:11], a[40:43], v[80:83]\n"
"ds_read_b128 a[24:27], v21 offset:58048\n"
"v_cvt_pk_bf16_f32 v126, v56, v57\n"
"v_mfma_f32_16x16x32_bf16 v[80:83], a[12:15], a[44:47], v[80:83]\n"
"v_exp_f32 v59, v59\n"
"v_mfma_f32_16x16x32_bf16 v[84:87], a[12:15], a[76:79], v[84:87]\n"
"v_exp_f32 v64, v64\n"
"v_mfma_f32_16x16x32_bf16 v[84:87], a[8:11], a[72:75], v[84:87]\n"
"ds_read_b128 a[28:31], v22 offset:58048\n"
"v_cvt_pk_bf16_f32 v127, v58, v59\n"
"v_mfma_f32_16x16x32_bf16 v[88:91], a[8:11], a[104:107], v[88:91]\n"
"s_waitcnt lgkmcnt(2)\n"
"buffer_load_dwordx4 v12, s[44:47], s81 offen offset:1024 nt lds\n"
"v_mfma_f32_16x16x32_bf16 v[88:91], a[12:15], a[108:111], v[88:91]\n"
"v_exp_f32 v65, v65\n"
"v_mfma_f32_16x16x32_bf16 v[80:83], a[16:19], a[48:51], v[80:83]\n"
"v_exp_f32 v66, v66\n"
"v_mfma_f32_16x16x32_bf16 v[80:83], a[20:23], a[52:55], v[80:83]\n"
"v_add_u32 v19, s27, v19\n"
"v_add_u32 v20, s27, v20\n"
"v_mfma_f32_16x16x32_bf16 v[84:87], a[20:23], a[84:87], v[84:87]\n"
"v_exp_f32 v67, v67\n"
"v_mfma_f32_16x16x32_bf16 v[84:87], a[16:19], a[80:83], v[84:87]\n"
"s_waitcnt vmcnt(24) lgkmcnt(0)\n"
"v_exp_f32 v68, v68\n"
"v_mfma_f32_16x16x32_bf16 v[88:91], a[16:19], a[112:115], v[88:91]\n"
"s_barrier\n"
"v_mfma_f32_16x16x32_bf16 v[88:91], a[20:23], a[116:119], v[88:91]\n"
"s_addk_i32 s79, 64\n"
"ds_read_b128 a[0:3], v15 offset:57792\n"
"v_cvt_pk_bf16_f32 v128, v64, v65\n"
"v_mfma_f32_16x16x32_bf16 a[160:163], a[176:179], v[108:111], 0\n"
"ds_read_b128 a[4:7], v16 offset:57792\n"
"v_cvt_pk_bf16_f32 v129, v66, v67\n"
"v_mfma_f32_16x16x32_bf16 a[164:167], a[176:179], v[112:115], 0\n"
"s_sub_u32 s0, s79, s2\n"
"v_exp_f32 v69, v69\n"
"v_mfma_f32_16x16x32_bf16 a[168:171], a[176:179], v[116:119], 0\n"
"ds_read_b128 a[8:11], v17 offset:57792\n"
"v_add_u32 v21, s27, v21\n"
"v_mfma_f32_16x16x32_bf16 a[172:175], a[176:179], v[120:123], 0\n"
"s_sub_u32 s0, s0, s23\n"
"v_exp_f32 v70, v70\n"
"v_mfma_f32_16x16x32_bf16 v[80:83], a[24:27], a[56:59], v[80:83]\n"
"v_exp_f32 v71, v71\n"
"v_mfma_f32_16x16x32_bf16 v[80:83], a[28:31], a[60:63], v[80:83]\n"
"s_sub_u32 s79, s77, s2\n"
"buffer_load_dwordx4 v13, s[44:47], s81 offen offset:2048 nt lds\n"
"v_mfma_f32_16x16x32_bf16 v[84:87], a[28:31], a[92:95], v[84:87]\n"
"ds_read_b128 a[12:15], v18 offset:57792\n"
"v_add_u32 v22, s27, v22\n"
"v_mfma_f32_16x16x32_bf16 v[84:87], a[24:27], a[88:91], v[84:87]\n"
"s_addk_i32 s79, 1287\n"
"v_exp_f32 v72, v72\n"
"v_mfma_f32_16x16x32_bf16 v[88:91], a[24:27], a[120:123], v[88:91]\n"
"s_waitcnt lgkmcnt(2)\n"
"v_exp_f32 v73, v73\n"
"v_mfma_f32_16x16x32_bf16 v[88:91], a[28:31], a[124:127], v[88:91]\n"
"s_max_i32 s0, s0, s79\n"
"v_cvt_pk_bf16_f32 v132, v68, v69\n"
"v_cvt_pk_bf16_f32 v133, v70, v71\n"
"v_mfma_f32_16x16x32_bf16 v[48:51], a[0:3], a[32:35], v[32:35]\n"
"s_lshl_b32 s70, s20, 2\n"
"ds_read_b128 a[16:19], v15 offset:58048\n"
"v_cvt_pk_bf16_f32 v136, v72, v73\n"
"v_mfma_f32_16x16x32_bf16 v[48:51], a[4:7], a[36:39], v[48:51]\n"
"s_min_i32 s0, s0, 40\n"
"v_exp_f32 v74, v74\n"
"v_mfma_f32_16x16x32_bf16 v[52:55], a[4:7], a[68:71], v[36:39]\n"
"s_add_u32 s70, s70, s60\n"
"v_exp_f32 v75, v75\n"
"v_mfma_f32_16x16x32_bf16 v[52:55], a[0:3], a[64:67], v[52:55]\n"
"s_max_i32 s0, s0, 0\n"
"PG5_%=:\n"
"s_cbranch_execz PG9_%=\n"
"ds_read_b128 a[20:23], v16 offset:58048\n"
"v_cvt_pk_bf16_f32 v137, v74, v75\n"
"v_mfma_f32_16x16x32_bf16 v[56:59], a[0:3], a[96:99], v[40:43]\n"
"s_mul_i32 s70, s70, 15360\n"
"v_exp_f32 v80, v80\n"
"v_mfma_f32_16x16x32_bf16 v[56:59], a[4:7], a[100:103], v[56:59]\n"
"s_add_u32 s79, s2, s0\n"
"v_exp_f32 v81, v81\n"
"v_mfma_f32_16x16x32_bf16 v[60:63], a[4:7], a[132:135], v[44:47]\n"
"s_waitcnt lgkmcnt(2)\n"
"v_subrev_u32 v15, s27, v15\n"
"v_subrev_u32 v16, s27, v16\n"
"v_mfma_f32_16x16x32_bf16 v[60:63], a[0:3], a[128:131], v[60:63]\n"
"s_lshr_b64 vcc, s[0:1], 2\n"
"v_exp_f32 v82, v82\n"
"v_mfma_f32_16x16x32_bf16 v[48:51], a[8:11], a[40:43], v[48:51]\n"
"s_add_u32 s70, s70, 1900544\n"
"ds_read_b128 a[24:27], v17 offset:58048\n"
"v_cvt_pk_bf16_f32 v80, v80, v81\n"
"v_mfma_f32_16x16x32_bf16 v[48:51], a[12:15], a[44:47], v[48:51]\n"
"s_cbranch_vccnz PC17_%=\n"
"PB17_%=:\n"
"v_accvgpr_write_b32 a188, v80\n"
"v_mfma_f32_16x16x32_bf16 v[52:55], a[12:15], a[76:79], v[52:55]\n"
"s_cmp_ge_u32 s20, 12\n"
"s_cselect_b32 s25, 0, 536870912\n"
"buffer_load_dwordx4 v14, s[44:47], s81 offen offset:3072 nt lds\n"
"v_mfma_f32_16x16x32_bf16 v[52:55], a[8:11], a[72:75], v[52:55]\n"
"s_xor_b32 s26, s25, 536870912\n"
"ds_read_b128 a[28:31], v18 offset:58048\n"
"v_subrev_u32 v17, s27, v17\n"
"v_mfma_f32_16x16x32_bf16 v[56:59], a[8:11], a[104:107], v[56:59]\n"
"v_exp_f32 v83, v83\n"
"v_mfma_f32_16x16x32_bf16 v[56:59], a[12:15], a[108:111], v[56:59]\n"
"v_exp_f32 v84, v84\n"
"v_mfma_f32_16x16x32_bf16 v[60:63], a[12:15], a[140:143], v[60:63]\n"
"s_waitcnt lgkmcnt(2)\n"
"v_cvt_pk_bf16_f32 v82, v82, v83\n"
"v_accvgpr_write_b32 a189, v82\n"
"v_mfma_f32_16x16x32_bf16 v[60:63], a[8:11], a[136:139], v[60:63]\n"
"v_exp_f32 v85, v85\n"
"v_mfma_f32_16x16x32_bf16 v[48:51], a[16:19], a[48:51], v[48:51]\n"
"ds_read_b128 a[0:3], v19 offset:57792\n"
"v_cvt_pk_bf16_f32 v84, v84, v85\n"
"v_mfma_f32_16x16x32_bf16 v[48:51], a[20:23], a[52:55], v[48:51]\n"
"v_exp_f32 v86, v86\n"
"v_mfma_f32_16x16x32_bf16 v[52:55], a[20:23], a[84:87], v[52:55]\n"
"v_exp_f32 v87, v87\n"
"v_mfma_f32_16x16x32_bf16 v[52:55], a[16:19], a[80:83], v[52:55]\n"
"s_addk_i32 m0, 8256\n"
"ds_read_b128 a[4:7], v20 offset:57792\n"
"v_cvt_pk_bf16_f32 v86, v86, v87\n"
"v_mfma_f32_16x16x32_bf16 v[56:59], a[16:19], a[112:115], v[56:59]\n"
"s_addk_i32 s81, 16384\n"
"v_exp_f32 v88, v88\n"
"v_mfma_f32_16x16x32_bf16 v[56:59], a[20:23], a[116:119], v[56:59]\n"
"v_exp_f32 v89, v89\n"
"v_mfma_f32_16x16x32_bf16 v[60:63], a[20:23], a[148:151], v[60:63]\n"
"s_waitcnt lgkmcnt(2)\n"
"buffer_load_dwordx4 v11, s[44:47], s81 offen offset:0 nt lds\n"
"v_mfma_f32_16x16x32_bf16 v[60:63], a[16:19], a[144:147], v[60:63]\n"
"ds_read_b128 a[8:11], v21 offset:57792\n"
"v_accvgpr_write_b32 a192, v84\n"
"v_mfma_f32_16x16x32_bf16 v[48:51], a[24:27], a[56:59], v[48:51]\n"
"v_exp_f32 v90, v90\n"
"v_mfma_f32_16x16x32_bf16 v[48:51], a[28:31], a[60:63], v[48:51]\n"
"ds_read_b128 a[12:15], v22 offset:57792\n"
"v_accvgpr_write_b32 a193, v86\n"
"v_mfma_f32_16x16x32_bf16 v[52:55], a[28:31], a[92:95], v[52:55]\n"
"v_exp_f32 v91, v91\n"
"v_mfma_f32_16x16x32_bf16 v[52:55], a[24:27], a[88:91], v[52:55]\n"
"v_exp_f32 v76, v76\n"
"v_mfma_f32_16x16x32_bf16 v[56:59], a[24:27], a[120:123], v[56:59]\n"
"ds_read_b128 a[16:19], v19 offset:58048\n"
"v_subrev_u32 v18, s27, v18\n"
"v_mfma_f32_16x16x32_bf16 v[56:59], a[28:31], a[124:127], v[56:59]\n"
"v_exp_f32 v48, v48\n"
"v_mfma_f32_16x16x32_bf16 v[60:63], a[28:31], a[156:159], v[60:63]\n"
"s_waitcnt lgkmcnt(2)\n"
"v_exp_f32 v49, v49\n"
"v_mfma_f32_16x16x32_bf16 v[60:63], a[24:27], a[152:155], v[60:63]\n"
"ds_read_b128 a[20:23], v20 offset:58048\n"
"v_cvt_pk_bf16_f32 v88, v88, v89\n"
"v_mfma_f32_16x16x32_bf16 v[64:67], a[0:3], a[32:35], v[32:35]\n"
"v_exp_f32 v50, v50\n"
"v_mfma_f32_16x16x32_bf16 v[64:67], a[4:7], a[36:39], v[64:67]\n"
"v_exp_f32 v51, v51\n"
"v_mfma_f32_16x16x32_bf16 v[68:71], a[4:7], a[68:71], v[36:39]\n"
"v_cvt_pk_bf16_f32 v90, v90, v91\n"
"v_accvgpr_write_b32 a196, v88\n"
"v_mfma_f32_16x16x32_bf16 v[68:71], a[0:3], a[64:67], v[68:71]\n"
"buffer_load_dwordx4 v12, s[44:47], s81 offen offset:1024 nt lds\n"
"v_mfma_f32_16x16x32_bf16 v[72:75], a[0:3], a[96:99], v[40:43]\n"
"s_waitcnt lgkmcnt(1)\n"
"v_exp_f32 v52, v52\n"
"v_mfma_f32_16x16x32_bf16 v[72:75], a[4:7], a[100:103], v[72:75]\n"
"s_memrealtime s[2:3]\n"
"ds_read_b128 a[24:27], v21 offset:58048\n"
"v_accvgpr_write_b32 a197, v90\n"
"v_mfma_f32_16x16x32_bf16 v[64:67], a[8:11], a[40:43], v[64:67]\n"
"v_exp_f32 v53, v53\n"
"v_mfma_f32_16x16x32_bf16 v[64:67], a[12:15], a[44:47], v[64:67]\n"
"v_exp_f32 v54, v54\n"
"v_mfma_f32_16x16x32_bf16 v[68:71], a[12:15], a[76:79], v[68:71]\n"
"ds_read_b128 a[28:31], v22 offset:58048\n"
"v_subrev_u32 v19, s27, v19\n"
"v_mfma_f32_16x16x32_bf16 v[68:71], a[8:11], a[72:75], v[68:71]\n"
"v_exp_f32 v55, v55\n"
"v_mfma_f32_16x16x32_bf16 v[72:75], a[8:11], a[104:107], v[72:75]\n"
"s_waitcnt lgkmcnt(2)\n"
"v_exp_f32 v56, v56\n"
"v_mfma_f32_16x16x32_bf16 v[72:75], a[12:15], a[108:111], v[72:75]\n"
"v_subrev_u32 v20, s27, v20\n"
"v_cvt_pk_bf16_f32 v130, v48, v49\n"
"v_mfma_f32_16x16x32_bf16 v[64:67], a[16:19], a[48:51], v[64:67]\n"
"v_exp_f32 v57, v57\n"
"v_mfma_f32_16x16x32_bf16 v[64:67], a[20:23], a[52:55], v[64:67]\n"
"v_exp_f32 v58, v58\n"
"v_mfma_f32_16x16x32_bf16 v[68:71], a[20:23], a[84:87], v[68:71]\n"
"s_waitcnt vmcnt(23) lgkmcnt(0)\n"
"v_cvt_pk_bf16_f32 v131, v50, v51\n"
"v_subrev_u32 v21, s27, v21\n"
"v_mfma_f32_16x16x32_bf16 v[68:71], a[16:19], a[80:83], v[68:71]\n"
"s_barrier\n"
"v_mfma_f32_16x16x32_bf16 v[72:75], a[16:19], a[112:115], v[72:75]\n"
"s_addk_i32 s79, 63\n"
"buffer_load_dwordx4 v13, s[44:47], s81 offen offset:2048 nt lds\n"
"v_mfma_f32_16x16x32_bf16 v[72:75], a[20:23], a[116:119], v[72:75]\n"
"ds_read_b128 a[0:3], v15 offset:33024\n"
"v_subrev_u32 v22, s27, v22\n"
"v_mfma_f32_16x16x32_bf16 a[160:163], a[176:179], a[180:183], a[160:163]\n"
"s_sub_u32 s0, s79, s2\n"
"ds_read_b128 a[4:7], v16 offset:33024\n"
"v_cvt_pk_bf16_f32 v134, v52, v53\n"
"v_mfma_f32_16x16x32_bf16 a[164:167], a[176:179], a[184:187], a[164:167]\n"
"ds_read_b128 a[8:11], v17 offset:33024\n"
"v_cvt_pk_bf16_f32 v135, v54, v55\n"
"v_mfma_f32_16x16x32_bf16 a[168:171], a[176:179], v[124:127], a[168:171]\n"
"s_sub_u32 s0, s0, s23\n"
"v_exp_f32 v59, v59\n"
"v_mfma_f32_16x16x32_bf16 v[64:67], a[24:27], a[56:59], v[64:67]\n"
"v_exp_f32 v77, v77\n"
"v_mfma_f32_16x16x32_bf16 v[64:67], a[28:31], a[60:63], v[64:67]\n"
"s_max_i32 s0, s0, 0\n"
"ds_read_b128 a[12:15], v18 offset:33024\n"
"v_cvt_pk_bf16_f32 v138, v56, v57\n"
"v_mfma_f32_16x16x32_bf16 v[68:71], a[28:31], a[92:95], v[68:71]\n"
"v_exp_f32 v78, v78\n"
"v_mfma_f32_16x16x32_bf16 v[68:71], a[24:27], a[88:91], v[68:71]\n"
"s_add_u32 s79, s2, s0\n"
"v_exp_f32 v79, v79\n"
"v_mfma_f32_16x16x32_bf16 v[72:75], a[24:27], a[120:123], v[72:75]\n"
"s_waitcnt lgkmcnt(2)\n"
"v_cvt_pk_bf16_f32 v139, v58, v59\n"
"v_cvt_pk_bf16_f32 v140, v76, v77\n"
"v_mfma_f32_16x16x32_bf16 v[72:75], a[28:31], a[124:127], v[72:75]\n"
"s_lshr_b64 vcc, s[0:1], 2\n"
"v_exp_f32 v60, v60\n"
"v_mfma_f32_16x16x32_bf16 v[80:83], a[0:3], a[32:35], v[32:35]\n"
"ds_read_b128 a[16:19], v15 offset:33280\n"
"v_mov_b32 v104, 2893824\n"
"v_mfma_f32_16x16x32_bf16 v[80:83], a[4:7], a[36:39], v[80:83]\n"
"s_cbranch_vccnz PC18_%=\n"
"PB18_%=:\n"
"v_cvt_pk_bf16_f32 v141, v78, v79\n"
"v_mfma_f32_16x16x32_bf16 v[84:87], a[4:7], a[68:71], v[36:39]\n"
"v_exp_f32 v61, v61\n"
"v_mfma_f32_16x16x32_bf16 v[84:87], a[0:3], a[64:67], v[84:87]\n"
"buffer_load_dwordx4 v14, s[44:47], s81 offen offset:3072 nt lds\n"
"v_mfma_f32_16x16x32_bf16 v[88:91], a[0:3], a[96:99], v[40:43]\n"
"ds_read_b128 a[20:23], v16 offset:33280\n"
"v_cvt_pk_bf16_f32 v142, v60, v61\n"
"v_mfma_f32_16x16x32_bf16 v[88:91], a[4:7], a[100:103], v[88:91]\n"
"v_exp_f32 v62, v62\n"
"v_mfma_f32_16x16x32_bf16 v[92:95], a[4:7], a[132:135], v[44:47]\n"
"s_waitcnt lgkmcnt(2)\n"
"v_exp_f32 v63, v63\n"
"v_mfma_f32_16x16x32_bf16 v[92:95], a[0:3], a[128:131], v[92:95]\n"
"v_cvt_pk_bf16_f32 v143, v62, v63\n"
"v_and_b32 v26, 7, v1\n"
"v_mfma_f32_16x16x32_bf16 v[80:83], a[8:11], a[40:43], v[80:83]\n"
"buffer_load_dwordx2 v[96:97], v104, s[52:55], 0 offen sc1\n"
"v_mfma_f32_16x16x32_bf16 v[80:83], a[12:15], a[44:47], v[80:83]\n"
"ds_read_b128 a[24:27], v17 offset:33280\n"
"v_xor_b32 v28, v28, v26\n"
"v_mfma_f32_16x16x32_bf16 v[84:87], a[12:15], a[76:79], v[84:87]\n"
"v_exp_f32 v64, v64\n"
"v_mfma_f32_16x16x32_bf16 v[84:87], a[8:11], a[72:75], v[84:87]\n"
"ds_read_b128 a[28:31], v18 offset:33280\n"
"v_lshlrev_b32 v28, 4, v28\n"
"v_mfma_f32_16x16x32_bf16 v[88:91], a[8:11], a[104:107], v[88:91]\n"
"v_exp_f32 v65, v65\n"
"v_mfma_f32_16x16x32_bf16 v[88:91], a[12:15], a[108:111], v[88:91]\n"
"s_addk_i32 m0, 8256\n"
"v_exp_f32 v66, v66\n"
"v_mfma_f32_16x16x32_bf16 v[92:95], a[12:15], a[140:143], v[92:95]\n"
"s_waitcnt lgkmcnt(2)\n"
"v_cvt_pk_bf16_f32 v64, v64, v65\n"
"v_accvgpr_write_b32 a190, v64\n"
"v_mfma_f32_16x16x32_bf16 v[92:95], a[8:11], a[136:139], v[92:95]\n"
"s_addk_i32 s81, 16384\n"
"v_exp_f32 v67, v67\n"
"v_mfma_f32_16x16x32_bf16 v[80:83], a[16:19], a[48:51], v[80:83]\n"
"buffer_load_dwordx4 v11, s[44:47], s81 offen offset:0 nt lds\n"
"v_mfma_f32_16x16x32_bf16 v[80:83], a[20:23], a[52:55], v[80:83]\n"
"ds_read_b128 a[0:3], v19 offset:33024\n"
"v_cvt_pk_bf16_f32 v66, v66, v67\n"
"v_mfma_f32_16x16x32_bf16 v[84:87], a[20:23], a[84:87], v[84:87]\n"
"v_exp_f32 v68, v68\n"
"v_mfma_f32_16x16x32_bf16 v[84:87], a[16:19], a[80:83], v[84:87]\n"
"ds_read_b128 a[4:7], v20 offset:33024\n"
"v_accvgpr_write_b32 a191, v66\n"
"v_mfma_f32_16x16x32_bf16 v[88:91], a[16:19], a[112:115], v[88:91]\n"
"v_exp_f32 v69, v69\n"
"v_mfma_f32_16x16x32_bf16 v[88:91], a[20:23], a[116:119], v[88:91]\n"
"v_exp_f32 v70, v70\n"
"v_mfma_f32_16x16x32_bf16 v[92:95], a[20:23], a[148:151], v[92:95]\n"
"s_waitcnt lgkmcnt(2)\n"
"ds_read_b128 a[8:11], v21 offset:33024\n"
"v_cvt_pk_bf16_f32 v68, v68, v69\n"
"v_mfma_f32_16x16x32_bf16 v[92:95], a[16:19], a[144:147], v[92:95]\n"
"v_exp_f32 v71, v71\n"
"v_mfma_f32_16x16x32_bf16 v[80:83], a[24:27], a[56:59], v[80:83]\n"
"v_exp_f32 v72, v72\n"
"v_mfma_f32_16x16x32_bf16 v[80:83], a[28:31], a[60:63], v[80:83]\n"
"ds_read_b128 a[12:15], v22 offset:33024\n"
"v_cvt_pk_bf16_f32 v70, v70, v71\n"
"v_mfma_f32_16x16x32_bf16 v[84:87], a[28:31], a[92:95], v[84:87]\n"
"v_exp_f32 v73, v73\n"
"v_mfma_f32_16x16x32_bf16 v[84:87], a[24:27], a[88:91], v[84:87]\n"
"v_exp_f32 v74, v74\n"
"v_mfma_f32_16x16x32_bf16 v[88:91], a[24:27], a[120:123], v[88:91]\n"
"ds_read_b128 a[16:19], v19 offset:33280\n"
"v_accvgpr_write_b32 a194, v68\n"
"v_mfma_f32_16x16x32_bf16 v[88:91], a[28:31], a[124:127], v[88:91]\n"
"buffer_load_dwordx4 v12, s[44:47], s81 offen offset:1024 nt lds\n"
"v_mfma_f32_16x16x32_bf16 v[92:95], a[28:31], a[156:159], v[92:95]\n"
"s_waitcnt lgkmcnt(2)\n"
"v_exp_f32 v75, v75\n"
"v_mfma_f32_16x16x32_bf16 v[92:95], a[24:27], a[152:155], v[92:95]\n"
"ds_read_b128 a[20:23], v20 offset:33280\n"
"v_accvgpr_write_b32 a195, v70\n"
"v_mfma_f32_16x16x32_bf16 v[48:51], a[0:3], a[32:35], v[32:35]\n"
"v_exp_f32 v80, v80\n"
"v_mfma_f32_16x16x32_bf16 v[48:51], a[4:7], a[36:39], v[48:51]\n"
"v_exp_f32 v81, v81\n"
"v_mfma_f32_16x16x32_bf16 v[52:55], a[4:7], a[68:71], v[36:39]\n"
"v_cvt_pk_bf16_f32 v72, v72, v73\n"
"v_cvt_pk_bf16_f32 v74, v74, v75\n"
"v_mfma_f32_16x16x32_bf16 v[52:55], a[0:3], a[64:67], v[52:55]\n"
"v_exp_f32 v82, v82\n"
"v_mfma_f32_16x16x32_bf16 v[56:59], a[0:3], a[96:99], v[40:43]\n"
"s_waitcnt lgkmcnt(1)\n"
"v_exp_f32 v83, v83\n"
"v_mfma_f32_16x16x32_bf16 v[56:59], a[4:7], a[100:103], v[56:59]\n"
"s_memrealtime s[2:3]\n"
"v_accvgpr_write_b32 a198, v72\n"
"v_accvgpr_write_b32 a199, v74\n"
"v_mfma_f32_16x16x32_bf16 v[48:51], a[8:11], a[40:43], v[48:51]\n"
"ds_read_b128 a[24:27], v21 offset:33280\n"
"v_cvt_pk_bf16_f32 v144, v80, v81\n"
"v_mfma_f32_16x16x32_bf16 v[48:51], a[12:15], a[44:47], v[48:51]\n"
"v_exp_f32 v84, v84\n"
"v_mfma_f32_16x16x32_bf16 v[52:55], a[12:15], a[76:79], v[52:55]\n"
"v_exp_f32 v85, v85\n"
"v_mfma_f32_16x16x32_bf16 v[52:55], a[8:11], a[72:75], v[52:55]\n"
"ds_read_b128 a[28:31], v22 offset:33280\n"
"v_cvt_pk_bf16_f32 v145, v82, v83\n"
"v_mfma_f32_16x16x32_bf16 v[56:59], a[8:11], a[104:107], v[56:59]\n"
"s_waitcnt lgkmcnt(2)\n"
"v_exp_f32 v86, v86\n"
"v_mfma_f32_16x16x32_bf16 v[56:59], a[12:15], a[108:111], v[56:59]\n"
"buffer_load_dwordx4 v13, s[44:47], s81 offen offset:2048 nt lds\n"
"v_mfma_f32_16x16x32_bf16 v[48:51], a[16:19], a[48:51], v[48:51]\n"
"v_exp_f32 v87, v87\n"
"v_mfma_f32_16x16x32_bf16 v[48:51], a[20:23], a[52:55], v[48:51]\n"
"v_exp_f32 v88, v88\n"
"v_mfma_f32_16x16x32_bf16 v[52:55], a[20:23], a[84:87], v[52:55]\n"
"v_cvt_pk_bf16_f32 v148, v84, v85\n"
"v_cvt_pk_bf16_f32 v149, v86, v87\n"
"v_mfma_f32_16x16x32_bf16 v[52:55], a[16:19], a[80:83], v[52:55]\n"
"s_waitcnt vmcnt(24) lgkmcnt(0)\n"
"v_exp_f32 v89, v89\n"
"v_mfma_f32_16x16x32_bf16 v[56:59], a[16:19], a[112:115], v[56:59]\n"
"s_barrier\n"
"v_mfma_f32_16x16x32_bf16 v[56:59], a[20:23], a[116:119], v[56:59]\n"
"s_addk_i32 s79, 64\n"
"ds_read_b128 a[0:3], v15 offset:0\n"
"v_cvt_pk_bf16_f32 v152, v88, v89\n"
"v_mfma_f32_16x16x32_bf16 a[160:163], a[176:179], v[128:131], a[160:163]\n"
"ds_read_b128 a[4:7], v16 offset:0\n"
"v_lshl_add_u32 v25, v27, 9, v28\n"
"v_mfma_f32_16x16x32_bf16 a[164:167], a[176:179], v[132:135], a[164:167]\n"
"s_sub_u32 s0, s79, s2\n"
"v_exp_f32 v90, v90\n"
"v_mfma_f32_16x16x32_bf16 a[168:171], a[176:179], v[136:139], a[168:171]\n"
"ds_read_b128 a[8:11], v17 offset:0\n"
"v_add_u32 v26, 3072, v25\n"
"v_mfma_f32_16x16x32_bf16 a[172:175], a[176:179], v[140:143], a[172:175]\n"
"s_sub_u32 s0, s0, s23\n"
"v_exp_f32 v91, v91\n"
"v_mfma_f32_16x16x32_bf16 v[48:51], a[24:27], a[56:59], v[48:51]\n"
"v_exp_f32 v92, v92\n"
"v_mfma_f32_16x16x32_bf16 v[48:51], a[28:31], a[60:63], v[48:51]\n"
"s_max_i32 s0, s0, 0\n"
"ds_read_b128 a[12:15], v18 offset:0\n"
"v_cvt_pk_bf16_f32 v153, v90, v91\n"
"v_mfma_f32_16x16x32_bf16 v[52:55], a[28:31], a[92:95], v[52:55]\n"
"buffer_load_dwordx4 v14, s[44:47], s81 offen offset:3072 nt lds\n"
"v_mfma_f32_16x16x32_bf16 v[52:55], a[24:27], a[88:91], v[52:55]\n"
"s_add_u32 s79, s2, s0\n"
"v_exp_f32 v93, v93\n"
"v_mfma_f32_16x16x32_bf16 v[56:59], a[24:27], a[120:123], v[56:59]\n"
"s_waitcnt lgkmcnt(2)\n"
"v_exp_f32 v48, v48\n"
"v_mfma_f32_16x16x32_bf16 v[56:59], a[28:31], a[124:127], v[56:59]\n"
"s_lshr_b64 vcc, s[0:1], 2\n"
"v_cvt_pk_bf16_f32 v156, v92, v93\n"
"v_add_u32 v27, 14336, v25\n"
"v_mfma_f32_16x16x32_bf16 v[64:67], a[0:3], a[32:35], v[32:35]\n"
"ds_read_b128 a[16:19], v15 offset:256\n"
"v_add_u32 v28, 17408, v25\n"
"v_mfma_f32_16x16x32_bf16 v[64:67], a[4:7], a[36:39], v[64:67]\n"
"s_cbranch_vccnz PC19_%=\n"
"PB19_%=:\n"
"v_mfma_f32_16x16x32_bf16 v[68:71], a[4:7], a[68:71], v[36:39]\n"
"v_exp_f32 v49, v49\n"
"v_mfma_f32_16x16x32_bf16 v[68:71], a[0:3], a[64:67], v[68:71]\n"
"ds_read_b128 a[20:23], v16 offset:256\n"
"v_cvt_pk_bf16_f32 v48, v48, v49\n"
"v_mfma_f32_16x16x32_bf16 v[72:75], a[0:3], a[96:99], v[40:43]\n"
"v_exp_f32 v50, v50\n"
"v_mfma_f32_16x16x32_bf16 v[72:75], a[4:7], a[100:103], v[72:75]\n"
"v_exp_f32 v51, v51\n"
"v_mfma_f32_16x16x32_bf16 v[76:79], a[4:7], a[132:135], v[44:47]\n"
"s_waitcnt lgkmcnt(2)\n"
"v_cvt_pk_bf16_f32 v50, v50, v51\n"
"v_accvgpr_write_b32 a200, v48\n"
"v_mfma_f32_16x16x32_bf16 v[76:79], a[0:3], a[128:131], v[76:79]\n"
"s_addk_i32 m0, 8256\n"
"v_exp_f32 v52, v52\n"
"v_mfma_f32_16x16x32_bf16 v[64:67], a[8:11], a[40:43], v[64:67]\n"
"s_addk_i32 s81, 16384\n"
"ds_read_b128 a[24:27], v17 offset:256\n"
"v_accvgpr_write_b32 a201, v50\n"
"v_mfma_f32_16x16x32_bf16 v[64:67], a[12:15], a[44:47], v[64:67]\n"
"buffer_load_dwordx4 v11, s[44:47], s81 offen offset:0 nt lds\n"
"v_mfma_f32_16x16x32_bf16 v[68:71], a[12:15], a[76:79], v[68:71]\n"
"v_exp_f32 v53, v53\n"
"v_mfma_f32_16x16x32_bf16 v[68:71], a[8:11], a[72:75], v[68:71]\n"
"ds_read_b128 a[28:31], v18 offset:256\n"
"v_cvt_pk_bf16_f32 v52, v52, v53\n"
"v_mfma_f32_16x16x32_bf16 v[72:75], a[8:11], a[104:107], v[72:75]\n"
"v_exp_f32 v54, v54\n"
"v_mfma_f32_16x16x32_bf16 v[72:75], a[12:15], a[108:111], v[72:75]\n"
"v_exp_f32 v55, v55\n"
"v_mfma_f32_16x16x32_bf16 v[76:79], a[12:15], a[140:143], v[76:79]\n"
"s_waitcnt lgkmcnt(2)\n"
"v_cvt_pk_bf16_f32 v54, v54, v55\n"
"v_accvgpr_write_b32 a204, v52\n"
"v_mfma_f32_16x16x32_bf16 v[76:79], a[8:11], a[136:139], v[76:79]\n"
"v_exp_f32 v56, v56\n"
"v_mfma_f32_16x16x32_bf16 v[64:67], a[16:19], a[48:51], v[64:67]\n"
"ds_read_b128 a[0:3], v19 offset:0\n"
"v_accvgpr_write_b32 a205, v54\n"
"v_mfma_f32_16x16x32_bf16 v[64:67], a[20:23], a[52:55], v[64:67]\n"
"v_exp_f32 v57, v57\n"
"v_mfma_f32_16x16x32_bf16 v[68:71], a[20:23], a[84:87], v[68:71]\n"
"v_exp_f32 v58, v58\n"
"v_mfma_f32_16x16x32_bf16 v[68:71], a[16:19], a[80:83], v[68:71]\n"
"ds_read_b128 a[4:7], v20 offset:0\n"
"v_cvt_pk_bf16_f32 v160, v56, v57\n"
"v_mfma_f32_16x16x32_bf16 v[72:75], a[16:19], a[112:115], v[72:75]\n"
"v_exp_f32 v59, v59\n"
"v_mfma_f32_16x16x32_bf16 v[72:75], a[20:23], a[116:119], v[72:75]\n"
"v_exp_f32 v94, v94\n"
"v_mfma_f32_16x16x32_bf16 v[76:79], a[20:23], a[148:151], v[76:79]\n"
"s_waitcnt lgkmcnt(2)\n"
"ds_read_b128 a[8:11], v21 offset:0\n"
"v_cvt_pk_bf16_f32 v161, v58, v59\n"
"v_mfma_f32_16x16x32_bf16 v[76:79], a[16:19], a[144:147], v[76:79]\n"
"buffer_load_dwordx4 v12, s[44:47], s81 offen offset:1024 nt lds\n"
"v_mfma_f32_16x16x32_bf16 v[64:67], a[24:27], a[56:59], v[64:67]\n"
"v_exp_f32 v95, v95\n"
"v_mfma_f32_16x16x32_bf16 v[64:67], a[28:31], a[60:63], v[64:67]\n"
"ds_read_b128 a[12:15], v22 offset:0\n"
"v_cvt_pk_bf16_f32 v157, v94, v95\n"
"v_mfma_f32_16x16x32_bf16 v[68:71], a[28:31], a[92:95], v[68:71]\n"
"v_mfma_f32_16x16x32_bf16 v[68:71], a[24:27], a[88:91], v[68:71]\n"
"v_mfma_f32_16x16x32_bf16 v[72:75], a[24:27], a[120:123], v[72:75]\n"
"ds_read_b128 a[16:19], v19 offset:256\n"
"v_mfma_f32_16x16x32_bf16 v[72:75], a[28:31], a[124:127], v[72:75]\n"
"v_mfma_f32_16x16x32_bf16 v[76:79], a[28:31], a[156:159], v[76:79]\n"
"s_waitcnt lgkmcnt(2)\n"
"v_exp_f32 v64, v64\n"
"v_mfma_f32_16x16x32_bf16 v[76:79], a[24:27], a[152:155], v[76:79]\n"
"ds_read_b128 a[20:23], v20 offset:256\n"
"v_mfma_f32_16x16x32_bf16 v[80:83], a[0:3], a[32:35], v[32:35]\n"
"v_exp_f32 v65, v65\n"
"v_mfma_f32_16x16x32_bf16 v[80:83], a[4:7], a[36:39], v[80:83]\n"
"v_exp_f32 v66, v66\n"
"v_mfma_f32_16x16x32_bf16 v[84:87], a[4:7], a[68:71], v[36:39]\n"
"v_cvt_pk_bf16_f32 v146, v64, v65\n"
"v_mfma_f32_16x16x32_bf16 v[84:87], a[0:3], a[64:67], v[84:87]\n"
"v_exp_f32 v67, v67\n"
"v_mfma_f32_16x16x32_bf16 v[88:91], a[0:3], a[96:99], v[40:43]\n"
"s_waitcnt lgkmcnt(1)\n"
"buffer_load_dwordx4 v13, s[44:47], s81 offen offset:2048 nt lds\n"
"v_mfma_f32_16x16x32_bf16 v[88:91], a[4:7], a[100:103], v[88:91]\n"
"s_memrealtime s[2:3]\n"
"ds_read_b128 a[24:27], v21 offset:256\n"
"v_cvt_pk_bf16_f32 v147, v66, v67\n"
"v_mfma_f32_16x16x32_bf16 v[80:83], a[8:11], a[40:43], v[80:83]\n"
"v_exp_f32 v68, v68\n"
"v_mfma_f32_16x16x32_bf16 v[80:83], a[12:15], a[44:47], v[80:83]\n"
"v_exp_f32 v69, v69\n"
"v_mfma_f32_16x16x32_bf16 v[84:87], a[12:15], a[76:79], v[84:87]\n"
"ds_read_b128 a[28:31], v22 offset:256\n"
"v_cvt_pk_bf16_f32 v150, v68, v69\n"
"v_mfma_f32_16x16x32_bf16 v[84:87], a[8:11], a[72:75], v[84:87]\n"
"v_exp_f32 v70, v70\n"
"v_mfma_f32_16x16x32_bf16 v[88:91], a[8:11], a[104:107], v[88:91]\n"
"s_waitcnt lgkmcnt(2)\n"
"v_exp_f32 v71, v71\n"
"v_mfma_f32_16x16x32_bf16 v[88:91], a[12:15], a[108:111], v[88:91]\n"
"v_cvt_pk_bf16_f32 v151, v70, v71\n"
"v_mfma_f32_16x16x32_bf16 v[80:83], a[16:19], a[48:51], v[80:83]\n"
"v_exp_f32 v72, v72\n"
"v_mfma_f32_16x16x32_bf16 v[80:83], a[20:23], a[52:55], v[80:83]\n"
"v_exp_f32 v73, v73\n"
"v_mfma_f32_16x16x32_bf16 v[84:87], a[20:23], a[84:87], v[84:87]\n"
"s_waitcnt vmcnt(24) lgkmcnt(0)\n"
"v_cvt_pk_bf16_f32 v154, v72, v73\n"
"v_mfma_f32_16x16x32_bf16 v[84:87], a[16:19], a[80:83], v[84:87]\n"
"s_barrier\n"
"v_mfma_f32_16x16x32_bf16 v[88:91], a[16:19], a[112:115], v[88:91]\n"
"s_addk_i32 s79, 63\n"
"ds_read_b128 a[0:3], v15 offset:8256\n"
"v_mfma_f32_16x16x32_bf16 v[88:91], a[20:23], a[116:119], v[88:91]\n"
"buffer_load_dwordx4 v14, s[44:47], s81 offen offset:3072 nt lds\n"
"v_mfma_f32_16x16x32_bf16 a[160:163], a[176:179], a[188:191], a[160:163]\n"
"s_sub_u32 s0, s79, s2\n"
"ds_read_b128 a[4:7], v16 offset:8256\n"
"v_mfma_f32_16x16x32_bf16 a[164:167], a[176:179], a[192:195], a[164:167]\n"
"ds_read_b128 a[8:11], v17 offset:8256\n"
"v_mfma_f32_16x16x32_bf16 a[168:171], a[176:179], a[196:199], a[168:171]\n"
"s_sub_u32 s0, s0, s23\n"
"v_exp_f32 v74, v74\n"
"v_mfma_f32_16x16x32_bf16 v[80:83], a[24:27], a[56:59], v[80:83]\n"
"v_exp_f32 v75, v75\n"
"v_mfma_f32_16x16x32_bf16 v[80:83], a[28:31], a[60:63], v[80:83]\n"
"s_sub_u32 s79, s77, s2\n"
"ds_read_b128 a[12:15], v18 offset:8256\n"
"v_cvt_pk_bf16_f32 v155, v74, v75\n"
"v_mfma_f32_16x16x32_bf16 v[84:87], a[28:31], a[92:95], v[84:87]\n"
"v_exp_f32 v76, v76\n"
"v_mfma_f32_16x16x32_bf16 v[84:87], a[24:27], a[88:91], v[84:87]\n"
"s_addk_i32 s79, 1488\n"
"v_exp_f32 v77, v77\n"
"v_mfma_f32_16x16x32_bf16 v[88:91], a[24:27], a[120:123], v[88:91]\n"
"s_waitcnt lgkmcnt(2)\n"
"v_cvt_pk_bf16_f32 v158, v76, v77\n"
"v_mfma_f32_16x16x32_bf16 v[88:91], a[28:31], a[124:127], v[88:91]\n"
"s_max_i32 s0, s0, s79\n"
"v_exp_f32 v78, v78\n"
"v_mfma_f32_16x16x32_bf16 v[48:51], a[0:3], a[32:35], v[32:35]\n"
"s_add_u32 m0, s63, 33024\n"
"ds_read_b128 a[16:19], v15 offset:8512\n"
"s_addk_i32 s81, 16384\n"
"v_mfma_f32_16x16x32_bf16 v[48:51], a[4:7], a[36:39], v[48:51]\n"
"s_min_i32 s0, s0, 40\n"
"v_exp_f32 v79, v79\n"
"v_mfma_f32_16x16x32_bf16 v[52:55], a[4:7], a[68:71], v[36:39]\n"
"s_lshr_b32 s90, s20, 2\n"
"v_exp_f32 v80, v80\n"
"v_mfma_f32_16x16x32_bf16 v[52:55], a[0:3], a[64:67], v[52:55]\n"
"s_max_i32 s0, s0, 0\n"
"buffer_load_dwordx4 v11, s[44:47], s81 offen offset:0 nt lds\n"
"v_mfma_f32_16x16x32_bf16 v[56:59], a[0:3], a[96:99], v[40:43]\n"
"s_and_b32 s24, s20, 3\n"
"ds_read_b128 a[20:23], v16 offset:8512\n"
"v_cvt_pk_bf16_f32 v159, v78, v79\n"
"v_mfma_f32_16x16x32_bf16 v[56:59], a[4:7], a[100:103], v[56:59]\n"
"s_add_u32 s79, s2, s0\n"
"v_exp_f32 v81, v81\n"
"v_mfma_f32_16x16x32_bf16 v[60:63], a[4:7], a[132:135], v[44:47]\n"
"s_waitcnt lgkmcnt(2)\n"
"v_exp_f32 v82, v82\n"
"v_mfma_f32_16x16x32_bf16 v[60:63], a[0:3], a[128:131], v[60:63]\n"
"s_lshr_b64 vcc, s[0:1], 2\n"
"v_cvt_pk_bf16_f32 v80, v80, v81\n"
"v_accvgpr_write_b32 a202, v80\n"
"v_mfma_f32_16x16x32_bf16 v[48:51], a[8:11], a[40:43], v[48:51]\n"
"s_lshl_b32 s24, s24, 2\n"
"ds_read_b128 a[24:27], v17 offset:8512\n"
"s_add_u32 s24, s24, s90\n"
"v_mfma_f32_16x16x32_bf16 v[48:51], a[12:15], a[44:47], v[48:51]\n"
"s_cbranch_vccnz PC20_%=\n"
"PB20_%=:\n"
"v_mfma_f32_16x16x32_bf16 v[52:55], a[12:15], a[76:79], v[52:55]\n"
"s_lshl_b32 s24, s24, 2\n"
"v_exp_f32 v83, v83\n"
"v_mfma_f32_16x16x32_bf16 v[52:55], a[8:11], a[72:75], v[52:55]\n"
"s_add_u32 s24, s24, s60\n"
"ds_read_b128 a[28:31], v18 offset:8512\n"
"v_cvt_pk_bf16_f32 v82, v82, v83\n"
"v_mfma_f32_16x16x32_bf16 v[56:59], a[8:11], a[104:107], v[56:59]\n"
"s_lshl_b32 s24, s24, 7\n"
"v_exp_f32 v84, v84\n"
"v_mfma_f32_16x16x32_bf16 v[56:59], a[12:15], a[108:111], v[56:59]\n"
"s_add_u32 s24, s24, 2885632\n"
"v_exp_f32 v85, v85\n"
"v_mfma_f32_16x16x32_bf16 v[60:63], a[12:15], a[140:143], v[60:63]\n"
"s_waitcnt lgkmcnt(2)\n"
"v_mov_b32 v106, s24\n"
"v_cmp_gt_u32 vcc, 4, v1\n"
"v_mfma_f32_16x16x32_bf16 v[60:63], a[8:11], a[136:139], v[60:63]\n"
"s_lshl_b32 s90, s90, 9\n"
"v_exp_f32 v86, v86\n"
"v_mfma_f32_16x16x32_bf16 v[48:51], a[16:19], a[48:51], v[48:51]\n"
"s_add_u32 s90, s90, 2883584\n"
"ds_read_b128 a[0:3], v19 offset:8256\n"
"v_lshl_add_u32 v107, v1, 7, s90\n"
"v_mfma_f32_16x16x32_bf16 v[48:51], a[20:23], a[52:55], v[48:51]\n"
"s_add_u32 s90, s34, 153728\n"
"buffer_load_dwordx4 v12, s[44:47], s81 offen offset:1024 nt lds\n"
"v_mfma_f32_16x16x32_bf16 v[52:55], a[20:23], a[84:87], v[52:55]\n"
"v_exp_f32 v87, v87\n"
"v_mfma_f32_16x16x32_bf16 v[52:55], a[16:19], a[80:83], v[52:55]\n"
"ds_read_b128 a[4:7], v20 offset:8256\n"
"v_cndmask_b32 v107, v106, v107, vcc\n"
"v_mfma_f32_16x16x32_bf16 v[56:59], a[16:19], a[112:115], v[56:59]\n"
"v_exp_f32 v88, v88\n"
"v_mfma_f32_16x16x32_bf16 v[56:59], a[20:23], a[116:119], v[56:59]\n"
"v_exp_f32 v89, v89\n"
"v_mfma_f32_16x16x32_bf16 v[60:63], a[20:23], a[148:151], v[60:63]\n"
"s_waitcnt lgkmcnt(2)\n"
"ds_read_b128 a[8:11], v21 offset:8256\n"
"v_accvgpr_write_b32 a203, v82\n"
"v_mfma_f32_16x16x32_bf16 v[60:63], a[16:19], a[144:147], v[60:63]\n"
"v_exp_f32 v90, v90\n"
"v_mfma_f32_16x16x32_bf16 v[48:51], a[24:27], a[56:59], v[48:51]\n"
"v_exp_f32 v91, v91\n"
"v_mfma_f32_16x16x32_bf16 v[48:51], a[28:31], a[60:63], v[48:51]\n"
"ds_read_b128 a[12:15], v22 offset:8256\n"
"v_cvt_pk_bf16_f32 v84, v84, v85\n"
"v_mfma_f32_16x16x32_bf16 v[52:55], a[28:31], a[92:95], v[52:55]\n"
"v_cvt_pk_bf16_f32 v86, v86, v87\n"
"v_accvgpr_write_b32 a206, v84\n"
"v_mfma_f32_16x16x32_bf16 v[52:55], a[24:27], a[88:91], v[52:55]\n"
"v_accvgpr_write_b32 a207, v86\n"
"v_cvt_pk_bf16_f32 v162, v88, v89\n"
"v_mfma_f32_16x16x32_bf16 v[56:59], a[24:27], a[120:123], v[56:59]\n"
"ds_read_b128 a[16:19], v19 offset:8512\n"
"v_cvt_pk_bf16_f32 v163, v90, v91\n"
"v_mfma_f32_16x16x32_bf16 v[56:59], a[28:31], a[124:127], v[56:59]\n"
"v_exp_f32 v48, v48\n"
"v_mfma_f32_16x16x32_bf16 v[60:63], a[28:31], a[156:159], v[60:63]\n"
"s_waitcnt lgkmcnt(2)\n"
"buffer_load_dwordx4 v13, s[44:47], s81 offen offset:2048 nt lds\n"
"v_mfma_f32_16x16x32_bf16 v[60:63], a[24:27], a[152:155], v[60:63]\n"
"ds_read_b128 a[20:23], v20 offset:8512\n"
"v_mov_b32 v102, s90\n"
"v_mfma_f32_16x16x32_bf16 v[64:67], a[0:3], a[32:35], v[32:35]\n"
"v_exp_f32 v49, v49\n"
"v_mfma_f32_16x16x32_bf16 v[64:67], a[4:7], a[36:39], v[64:67]\n"
"v_exp_f32 v50, v50\n"
"v_mfma_f32_16x16x32_bf16 v[68:71], a[4:7], a[68:71], v[36:39]\n"
"v_cvt_pk_bf16_f32 v164, v48, v49\n"
"v_mfma_f32_16x16x32_bf16 v[68:71], a[0:3], a[64:67], v[68:71]\n"
"v_exp_f32 v51, v51\n"
"v_mfma_f32_16x16x32_bf16 v[72:75], a[0:3], a[96:99], v[40:43]\n"
"s_waitcnt lgkmcnt(1)\n"
"v_exp_f32 v52, v52\n"
"v_mfma_f32_16x16x32_bf16 v[72:75], a[4:7], a[100:103], v[72:75]\n"
"s_memrealtime s[2:3]\n"
"v_cvt_pk_bf16_f32 v165, v50, v51\n"
"v_mfma_f32_16x16x32_bf16 v[64:67], a[8:11], a[40:43], v[64:67]\n"
"ds_read_b128 a[24:27], v21 offset:8512\n"
"v_mfma_f32_16x16x32_bf16 v[64:67], a[12:15], a[44:47], v[64:67]\n"
"v_exp_f32 v53, v53\n"
"v_mfma_f32_16x16x32_bf16 v[68:71], a[12:15], a[76:79], v[68:71]\n"
"v_exp_f32 v54, v54\n"
"v_mfma_f32_16x16x32_bf16 v[68:71], a[8:11], a[72:75], v[68:71]\n"
"ds_read_b128 a[28:31], v22 offset:8512\n"
"v_cvt_pk_bf16_f32 v168, v52, v53\n"
"v_mfma_f32_16x16x32_bf16 v[72:75], a[8:11], a[104:107], v[72:75]\n"
"s_waitcnt lgkmcnt(2)\n"
"v_exp_f32 v55, v55\n"
"v_mfma_f32_16x16x32_bf16 v[72:75], a[12:15], a[108:111], v[72:75]\n"
"buffer_load_dwordx4 v14, s[44:47], s81 offen offset:3072 nt lds\n"
"v_mfma_f32_16x16x32_bf16 v[64:67], a[16:19], a[48:51], v[64:67]\n"
"v_exp_f32 v56, v56\n"
"v_mfma_f32_16x16x32_bf16 v[64:67], a[20:23], a[52:55], v[64:67]\n"
"v_exp_f32 v57, v57\n"
"v_mfma_f32_16x16x32_bf16 v[68:71], a[20:23], a[84:87], v[68:71]\n"
"v_cvt_pk_bf16_f32 v169, v54, v55\n"
"v_cvt_pk_bf16_f32 v172, v56, v57\n"
"v_mfma_f32_16x16x32_bf16 v[68:71], a[16:19], a[80:83], v[68:71]\n"
"s_waitcnt vmcnt(25) lgkmcnt(0)\n"
"v_exp_f32 v58, v58\n"
"v_mfma_f32_16x16x32_bf16 v[72:75], a[16:19], a[112:115], v[72:75]\n"
"s_barrier\n"
"v_mfma_f32_16x16x32_bf16 v[72:75], a[20:23], a[116:119], v[72:75]\n"
"s_addk_i32 s79, 64\n"
"buffer_load_dword v30, v107, s[52:55], 0 offen sc1\n"
"v_mfma_f32_16x16x32_bf16 a[160:163], a[176:179], v[144:147], a[160:163]\n"
"ds_read_b128 a[0:3], v15 offset:16512\n"
"v_mfma_f32_16x16x32_bf16 a[164:167], a[176:179], v[148:151], a[164:167]\n"
"s_sub_u32 s0, s79, s2\n"
"ds_read_b128 a[4:7], v16 offset:16512\n"
"v_mfma_f32_16x16x32_bf16 a[168:171], a[176:179], v[152:155], a[168:171]\n"
"ds_read_b128 a[8:11], v17 offset:16512\n"
"v_mfma_f32_16x16x32_bf16 a[172:175], a[176:179], v[156:159], a[172:175]\n"
"s_sub_u32 s0, s0, s23\n"
"v_exp_f32 v59, v59\n"
"v_mfma_f32_16x16x32_bf16 v[64:67], a[24:27], a[56:59], v[64:67]\n"
"s_add_u32 m0, s63, 0\n"
"v_exp_f32 v60, v60\n"
"v_mfma_f32_16x16x32_bf16 v[64:67], a[28:31], a[60:63], v[64:67]\n"
"s_addk_i32 s81, 16384\n"
"ds_read_b128 a[12:15], v18 offset:16512\n"
"PG6_%=:\n"
"s_cbranch_execz PG10_%=\n"
"v_cvt_pk_bf16_f32 v173, v58, v59\n"
"v_mfma_f32_16x16x32_bf16 v[68:71], a[28:31], a[92:95], v[68:71]\n"
"s_max_i32 s0, s0, 0\n"
"v_exp_f32 v61, v61\n"
"v_mfma_f32_16x16x32_bf16 v[68:71], a[24:27], a[88:91], v[68:71]\n"
"buffer_load_dwordx4 v11, s[44:47], s81 offen offset:0 nt lds\n"
"v_mfma_f32_16x16x32_bf16 v[72:75], a[24:27], a[120:123], v[72:75]\n"
"s_waitcnt lgkmcnt(2)\n"
"v_exp_f32 v64, v64\n"
"v_mfma_f32_16x16x32_bf16 v[72:75], a[28:31], a[124:127], v[72:75]\n"
"s_add_u32 s79, s2, s0\n"
"v_exp_f32 v65, v65\n"
"v_mfma_f32_16x16x32_bf16 v[80:83], a[0:3], a[32:35], v[32:35]\n"
"ds_read_b128 a[16:19], v15 offset:16768\n"
"v_cvt_pk_bf16_f32 v64, v64, v65\n"
"v_mfma_f32_16x16x32_bf16 v[80:83], a[4:7], a[36:39], v[80:83]\n"
"s_lshr_b64 vcc, s[0:1], 2\n"
"v_exp_f32 v66, v66\n"
"v_mfma_f32_16x16x32_bf16 v[84:87], a[4:7], a[68:71], v[36:39]\n"
"v_exp_f32 v67, v67\n"
"v_mfma_f32_16x16x32_bf16 v[84:87], a[0:3], a[64:67], v[84:87]\n"
"s_cbranch_vccnz PC21_%=\n"
"PB21_%=:\n"
"v_cvt_pk_bf16_f32 v66, v66, v67\n"
"v_mfma_f32_16x16x32_bf16 v[88:91], a[0:3], a[96:99], v[40:43]\n"
"ds_read_b128 a[20:23], v16 offset:16768\n"
"v_accvgpr_write_b32 a208, v64\n"
"v_mfma_f32_16x16x32_bf16 v[88:91], a[4:7], a[100:103], v[88:91]\n"
"v_exp_f32 v68, v68\n"
"v_mfma_f32_16x16x32_bf16 v[92:95], a[4:7], a[132:135], v[44:47]\n"
"s_waitcnt lgkmcnt(2)\n"
"v_exp_f32 v69, v69\n"
"v_mfma_f32_16x16x32_bf16 v[92:95], a[0:3], a[128:131], v[92:95]\n"
"v_accvgpr_write_b32 a209, v66\n"
"v_cvt_pk_bf16_f32 v68, v68, v69\n"
"v_mfma_f32_16x16x32_bf16 v[80:83], a[8:11], a[40:43], v[80:83]\n"
"ds_read_b128 a[24:27], v17 offset:16768\n"
"v_accvgpr_write_b32 a212, v68\n"
"v_mfma_f32_16x16x32_bf16 v[80:83], a[12:15], a[44:47], v[80:83]\n"
"v_exp_f32 v70, v70\n"
"v_mfma_f32_16x16x32_bf16 v[84:87], a[12:15], a[76:79], v[84:87]\n"
"buffer_load_dwordx4 v12, s[44:47], s81 offen offset:1024 nt lds\n"
"v_mfma_f32_16x16x32_bf16 v[84:87], a[8:11], a[72:75], v[84:87]\n"
"ds_read_b128 a[28:31], v18 offset:16768\n"
"v_cvt_pk_bf16_f32 v176, v60, v61\n"
"v_mfma_f32_16x16x32_bf16 v[88:91], a[8:11], a[104:107], v[88:91]\n"
"v_exp_f32 v71, v71\n"
"v_mfma_f32_16x16x32_bf16 v[88:91], a[12:15], a[108:111], v[88:91]\n"
"v_exp_f32 v72, v72\n"
"v_mfma_f32_16x16x32_bf16 v[92:95], a[12:15], a[140:143], v[92:95]\n"
"s_waitcnt lgkmcnt(2)\n"
"v_cvt_pk_bf16_f32 v70, v70, v71\n"
"v_accvgpr_write_b32 a213, v70\n"
"v_mfma_f32_16x16x32_bf16 v[92:95], a[8:11], a[136:139], v[92:95]\n"
"v_exp_f32 v73, v73\n"
"v_mfma_f32_16x16x32_bf16 v[80:83], a[16:19], a[48:51], v[80:83]\n"
"ds_read_b128 a[0:3], v19 offset:16512\n"
"v_cvt_pk_bf16_f32 v72, v72, v73\n"
"v_mfma_f32_16x16x32_bf16 v[80:83], a[20:23], a[52:55], v[80:83]\n"
"v_exp_f32 v74, v74\n"
"v_mfma_f32_16x16x32_bf16 v[84:87], a[20:23], a[84:87], v[84:87]\n"
"v_exp_f32 v75, v75\n"
"v_mfma_f32_16x16x32_bf16 v[84:87], a[16:19], a[80:83], v[84:87]\n"
"ds_read_b128 a[4:7], v20 offset:16512\n"
"v_cvt_pk_bf16_f32 v74, v74, v75\n"
"v_mfma_f32_16x16x32_bf16 v[88:91], a[16:19], a[112:115], v[88:91]\n"
"v_exp_f32 v62, v62\n"
"v_mfma_f32_16x16x32_bf16 v[88:91], a[20:23], a[116:119], v[88:91]\n"
"v_exp_f32 v63, v63\n"
"v_mfma_f32_16x16x32_bf16 v[92:95], a[20:23], a[148:151], v[92:95]\n"
"s_waitcnt lgkmcnt(2)\n"
"ds_read_b128 a[8:11], v21 offset:16512\n"
"v_accvgpr_write_b32 a216, v72\n"
"v_mfma_f32_16x16x32_bf16 v[92:95], a[16:19], a[144:147], v[92:95]\n"
"buffer_load_dwordx4 v13, s[44:47], s81 offen offset:2048 nt lds\n"
"v_mfma_f32_16x16x32_bf16 v[80:83], a[24:27], a[56:59], v[80:83]\n"
"v_accvgpr_write_b32 a217, v74\n"
"v_cvt_pk_bf16_f32 v177, v62, v63\n"
"v_mfma_f32_16x16x32_bf16 v[80:83], a[28:31], a[60:63], v[80:83]\n"
"ds_read_b128 a[12:15], v22 offset:16512\n"
"v_mfma_f32_16x16x32_bf16 v[84:87], a[28:31], a[92:95], v[84:87]\n"
"v_mfma_f32_16x16x32_bf16 v[84:87], a[24:27], a[88:91], v[84:87]\n"
"v_mfma_f32_16x16x32_bf16 v[88:91], a[24:27], a[120:123], v[88:91]\n"
"ds_read_b128 a[16:19], v19 offset:16768\n"
"v_mfma_f32_16x16x32_bf16 v[88:91], a[28:31], a[124:127], v[88:91]\n"
"v_mfma_f32_16x16x32_bf16 v[92:95], a[28:31], a[156:159], v[92:95]\n"
"s_waitcnt lgkmcnt(2)\n"
"v_mfma_f32_16x16x32_bf16 v[92:95], a[24:27], a[152:155], v[92:95]\n"
"ds_read_b128 a[20:23], v20 offset:16768\n"
"v_mfma_f32_16x16x32_bf16 v[48:51], a[0:3], a[32:35], v[32:35]\n"
"v_exp_f32 v80, v80\n"
"v_mfma_f32_16x16x32_bf16 v[48:51], a[4:7], a[36:39], v[48:51]\n"
"v_exp_f32 v81, v81\n"
"v_mfma_f32_16x16x32_bf16 v[52:55], a[4:7], a[68:71], v[36:39]\n"
"v_cvt_pk_bf16_f32 v166, v80, v81\n"
"v_mfma_f32_16x16x32_bf16 v[52:55], a[0:3], a[64:67], v[52:55]\n"
"v_exp_f32 v82, v82\n"
"v_mfma_f32_16x16x32_bf16 v[56:59], a[0:3], a[96:99], v[40:43]\n"
"s_waitcnt lgkmcnt(1)\n"
"v_exp_f32 v83, v83\n"
"v_mfma_f32_16x16x32_bf16 v[56:59], a[4:7], a[100:103], v[56:59]\n"
"s_memrealtime s[2:3]\n"
"buffer_load_dwordx4 v14, s[44:47], s81 offen offset:3072 nt lds\n"
"v_mfma_f32_16x16x32_bf16 v[48:51], a[8:11], a[40:43], v[48:51]\n"
"ds_read_b128 a[24:27], v21 offset:16768\n"
"v_cvt_pk_bf16_f32 v167, v82, v83\n"
"v_mfma_f32_16x16x32_bf16 v[48:51], a[12:15], a[44:47], v[48:51]\n"
"v_exp_f32 v84, v84\n"
"v_mfma_f32_16x16x32_bf16 v[52:55], a[12:15], a[76:79], v[52:55]\n"
"ds_read_b128 a[28:31], v22 offset:16768\n"
"v_mfma_f32_16x16x32_bf16 v[52:55], a[8:11], a[72:75], v[52:55]\n"
"v_exp_f32 v85, v85\n"
"v_mfma_f32_16x16x32_bf16 v[56:59], a[8:11], a[104:107], v[56:59]\n"
"s_waitcnt lgkmcnt(2)\n"
"v_exp_f32 v86, v86\n"
"v_mfma_f32_16x16x32_bf16 v[56:59], a[12:15], a[108:111], v[56:59]\n"
"v_cvt_pk_bf16_f32 v170, v84, v85\n"
"v_mfma_f32_16x16x32_bf16 v[48:51], a[16:19], a[48:51], v[48:51]\n"
"v_exp_f32 v87, v87\n"
"v_mfma_f32_16x16x32_bf16 v[48:51], a[20:23], a[52:55], v[48:51]\n"
"v_exp_f32 v88, v88\n"
"v_mfma_f32_16x16x32_bf16 v[52:55], a[20:23], a[84:87], v[52:55]\n"
"s_waitcnt vmcnt(26) lgkmcnt(0)\n"
"v_cvt_pk_bf16_f32 v171, v86, v87\n"
"v_mfma_f32_16x16x32_bf16 v[52:55], a[16:19], a[80:83], v[52:55]\n"
"s_barrier\n"
"v_mfma_f32_16x16x32_bf16 v[56:59], a[16:19], a[112:115], v[56:59]\n"
"s_addk_i32 m0, 8256\n"
"ds_read_b128 a[0:3], v15 offset:24768\n"
"s_addk_i32 s81, 16384\n"
"v_mfma_f32_16x16x32_bf16 v[56:59], a[20:23], a[116:119], v[56:59]\n"
"s_addk_i32 s79, 64\n"
"ds_read_b128 a[4:7], v16 offset:24768\n"
"v_mfma_f32_16x16x32_bf16 a[160:163], a[176:179], a[200:203], a[160:163]\n"
"buffer_load_dwordx4 v11, s[44:47], s81 offen offset:0 nt lds\n"
"v_mfma_f32_16x16x32_bf16 a[164:167], a[176:179], a[204:207], a[164:167]\n"
"ds_read_b128 a[8:11], v17 offset:24768\n"
"s_sub_u32 s0, s79, s2\n"
"v_mfma_f32_16x16x32_bf16 a[168:171], a[176:179], v[160:163], a[168:171]\n"
"v_exp_f32 v89, v89\n"
"v_mfma_f32_16x16x32_bf16 v[48:51], a[24:27], a[56:59], v[48:51]\n"
"s_sub_u32 s0, s0, s23\n"
"v_exp_f32 v90, v90\n"
"v_mfma_f32_16x16x32_bf16 v[48:51], a[28:31], a[60:63], v[48:51]\n"
"ds_read_b128 a[12:15], v18 offset:24768\n"
"v_cvt_pk_bf16_f32 v174, v88, v89\n"
"v_mfma_f32_16x16x32_bf16 v[52:55], a[28:31], a[92:95], v[52:55]\n"
"s_max_i32 s0, s0, 0\n"
"v_exp_f32 v91, v91\n"
"v_mfma_f32_16x16x32_bf16 v[52:55], a[24:27], a[88:91], v[52:55]\n"
"v_exp_f32 v92, v92\n"
"v_mfma_f32_16x16x32_bf16 v[56:59], a[24:27], a[120:123], v[56:59]\n"
"s_waitcnt lgkmcnt(2)\n"
"v_cvt_pk_bf16_f32 v175, v90, v91\n"
"s_add_u32 s79, s2, s0\n"
"v_mfma_f32_16x16x32_bf16 v[56:59], a[28:31], a[124:127], v[56:59]\n"
"v_exp_f32 v93, v93\n"
"v_mfma_f32_16x16x32_bf16 v[64:67], a[0:3], a[32:35], v[32:35]\n"
"s_lshr_b64 vcc, s[0:1], 2\n"
"ds_read_b128 a[16:19], v15 offset:25024\n"
"v_cvt_pk_bf16_f32 v178, v92, v93\n"
"v_mfma_f32_16x16x32_bf16 v[64:67], a[4:7], a[36:39], v[64:67]\n"
"v_exp_f32 v94, v94\n"
"v_mfma_f32_16x16x32_bf16 v[68:71], a[4:7], a[68:71], v[36:39]\n"
"s_cbranch_vccnz PC22_%=\n"
"PB22_%=:\n"
"v_mfma_f32_16x16x32_bf16 v[68:71], a[0:3], a[64:67], v[68:71]\n"
"ds_read_b128 a[20:23], v16 offset:25024\n"
"v_mfma_f32_16x16x32_bf16 v[72:75], a[0:3], a[96:99], v[40:43]\n"
"buffer_load_dwordx4 v12, s[44:47], s81 offen offset:1024 nt lds\n"
"v_mfma_f32_16x16x32_bf16 v[72:75], a[4:7], a[100:103], v[72:75]\n"
"v_exp_f32 v95, v95\n"
"v_mfma_f32_16x16x32_bf16 v[76:79], a[4:7], a[132:135], v[44:47]\n"
"s_waitcnt lgkmcnt(2)\n"
"v_exp_f32 v48, v48\n"
"v_mfma_f32_16x16x32_bf16 v[76:79], a[0:3], a[128:131], v[76:79]\n"
"v_cvt_pk_bf16_f32 v179, v94, v95\n"
"v_mfma_f32_16x16x32_bf16 v[64:67], a[8:11], a[40:43], v[64:67]\n"
"ds_read_b128 a[24:27], v17 offset:25024\n"
"v_mfma_f32_16x16x32_bf16 v[64:67], a[12:15], a[44:47], v[64:67]\n"
"v_exp_f32 v49, v49\n"
"v_mfma_f32_16x16x32_bf16 v[68:71], a[12:15], a[76:79], v[68:71]\n"
"v_exp_f32 v50, v50\n"
"v_mfma_f32_16x16x32_bf16 v[68:71], a[8:11], a[72:75], v[68:71]\n"
"ds_read_b128 a[28:31], v18 offset:25024\n"
"v_cvt_pk_bf16_f32 v48, v48, v49\n"
"v_mfma_f32_16x16x32_bf16 v[72:75], a[8:11], a[104:107], v[72:75]\n"
"v_exp_f32 v51, v51\n"
"v_mfma_f32_16x16x32_bf16 v[72:75], a[12:15], a[108:111], v[72:75]\n"
"v_exp_f32 v52, v52\n"
"v_mfma_f32_16x16x32_bf16 v[76:79], a[12:15], a[140:143], v[76:79]\n"
"s_waitcnt lgkmcnt(2)\n"
"v_cvt_pk_bf16_f32 v50, v50, v51\n"
"v_accvgpr_write_b32 a210, v48\n"
"v_mfma_f32_16x16x32_bf16 v[76:79], a[8:11], a[136:139], v[76:79]\n"
"v_exp_f32 v53, v53\n"
"v_mfma_f32_16x16x32_bf16 v[64:67], a[16:19], a[48:51], v[64:67]\n"
"ds_read_b128 a[0:3], v19 offset:24768\n"
"v_accvgpr_write_b32 a211, v50\n"
"v_mfma_f32_16x16x32_bf16 v[64:67], a[20:23], a[52:55], v[64:67]\n"
"buffer_load_dwordx4 v13, s[44:47], s81 offen offset:2048 nt lds\n"
"v_mfma_f32_16x16x32_bf16 v[68:71], a[20:23], a[84:87], v[68:71]\n"
"v_exp_f32 v54, v54\n"
"v_mfma_f32_16x16x32_bf16 v[68:71], a[16:19], a[80:83], v[68:71]\n"
"ds_read_b128 a[4:7], v20 offset:24768\n"
"v_cvt_pk_bf16_f32 v52, v52, v53\n"
"v_mfma_f32_16x16x32_bf16 v[72:75], a[16:19], a[112:115], v[72:75]\n"
"v_exp_f32 v55, v55\n"
"v_mfma_f32_16x16x32_bf16 v[72:75], a[20:23], a[116:119], v[72:75]\n"
"v_exp_f32 v56, v56\n"
"v_mfma_f32_16x16x32_bf16 v[76:79], a[20:23], a[148:151], v[76:79]\n"
"s_waitcnt lgkmcnt(2)\n"
"ds_read_b128 a[8:11], v21 offset:24768\n"
"v_cvt_pk_bf16_f32 v54, v54, v55\n"
"v_mfma_f32_16x16x32_bf16 v[76:79], a[16:19], a[144:147], v[76:79]\n"
"v_exp_f32 v57, v57\n"
"v_mfma_f32_16x16x32_bf16 v[64:67], a[24:27], a[56:59], v[64:67]\n"
"v_exp_f32 v58, v58\n"
"v_mfma_f32_16x16x32_bf16 v[64:67], a[28:31], a[60:63], v[64:67]\n"
"ds_read_b128 a[12:15], v22 offset:24768\n"
"v_accvgpr_write_b32 a214, v52\n"
"v_mfma_f32_16x16x32_bf16 v[68:71], a[28:31], a[92:95], v[68:71]\n"
"v_exp_f32 v59, v59\n"
"v_mfma_f32_16x16x32_bf16 v[68:71], a[24:27], a[88:91], v[68:71]\n"
"v_accvgpr_write_b32 a215, v54\n"
"v_cvt_pk_bf16_f32 v56, v56, v57\n"
"v_mfma_f32_16x16x32_bf16 v[72:75], a[24:27], a[120:123], v[72:75]\n"
"ds_read_b128 a[16:19], v19 offset:25024\n"
"v_cvt_pk_bf16_f32 v58, v58, v59\n"
"v_mfma_f32_16x16x32_bf16 v[72:75], a[28:31], a[124:127], v[72:75]\n"
"v_exp_f32 v64, v64\n"
"v_mfma_f32_16x16x32_bf16 v[76:79], a[28:31], a[156:159], v[76:79]\n"
"s_waitcnt lgkmcnt(2)\n"
"v_exp_f32 v65, v65\n"
"v_mfma_f32_16x16x32_bf16 v[76:79], a[24:27], a[152:155], v[76:79]\n"
"buffer_load_dwordx4 v14, s[44:47], s81 offen offset:3072 nt lds\n"
"v_mfma_f32_16x16x32_bf16 v[80:83], a[0:3], a[32:35], v[32:35]\n"
"ds_read_b128 a[20:23], v20 offset:25024\n"
"v_accvgpr_write_b32 a218, v56\n"
"v_mfma_f32_16x16x32_bf16 v[80:83], a[4:7], a[36:39], v[80:83]\n"
"v_exp_f32 v66, v66\n"
"v_mfma_f32_16x16x32_bf16 v[84:87], a[4:7], a[68:71], v[36:39]\n"
"v_exp_f32 v67, v67\n"
"v_mfma_f32_16x16x32_bf16 v[84:87], a[0:3], a[64:67], v[84:87]\n"
"v_accvgpr_write_b32 a219, v58\n"
"v_cvt_pk_bf16_f32 v180, v64, v65\n"
"v_mfma_f32_16x16x32_bf16 v[88:91], a[0:3], a[96:99], v[40:43]\n"
"s_waitcnt lgkmcnt(1)\n"
"v_exp_f32 v68, v68\n"
"v_mfma_f32_16x16x32_bf16 v[88:91], a[4:7], a[100:103], v[88:91]\n"
"s_memrealtime s[2:3]\n"
"v_exp_f32 v69, v69\n"
"v_mfma_f32_16x16x32_bf16 v[80:83], a[8:11], a[40:43], v[80:83]\n"
"ds_read_b128 a[24:27], v21 offset:25024\n"
"v_cvt_pk_bf16_f32 v181, v66, v67\n"
"v_mfma_f32_16x16x32_bf16 v[80:83], a[12:15], a[44:47], v[80:83]\n"
"v_exp_f32 v70, v70\n"
"v_mfma_f32_16x16x32_bf16 v[84:87], a[12:15], a[76:79], v[84:87]\n"
"v_exp_f32 v71, v71\n"
"v_mfma_f32_16x16x32_bf16 v[84:87], a[8:11], a[72:75], v[84:87]\n"
"s_addk_i32 m0, 8256\n"
"ds_read_b128 a[28:31], v22 offset:25024\n"
"v_cvt_pk_bf16_f32 v184, v68, v69\n"
"v_mfma_f32_16x16x32_bf16 v[88:91], a[8:11], a[104:107], v[88:91]\n"
"s_waitcnt lgkmcnt(2)\n"
"v_exp_f32 v72, v72\n"
"v_mfma_f32_16x16x32_bf16 v[88:91], a[12:15], a[108:111], v[88:91]\n"
"s_add_u32 s81, s65, 0\n"
"v_exp_f32 v73, v73\n"
"v_mfma_f32_16x16x32_bf16 v[80:83], a[16:19], a[48:51], v[80:83]\n"
"buffer_load_dwordx4 v25, s[48:51], s81 offen offset:0 nt lds\n"
"v_mfma_f32_16x16x32_bf16 v[80:83], a[20:23], a[52:55], v[80:83]\n"
"v_exp_f32 v74, v74\n"
"v_mfma_f32_16x16x32_bf16 v[84:87], a[20:23], a[84:87], v[84:87]\n"
"v_exp_f32 v75, v75\n"
"v_mfma_f32_16x16x32_bf16 v[84:87], a[16:19], a[80:83], v[84:87]\n"
"s_waitcnt vmcnt(27) lgkmcnt(0)\n"
"v_cvt_pk_bf16_f32 v185, v70, v71\n"
"v_cvt_pk_bf16_f32 v188, v72, v73\n"
"v_mfma_f32_16x16x32_bf16 v[88:91], a[16:19], a[112:115], v[88:91]\n"
"s_barrier\n"
"v_mfma_f32_16x16x32_bf16 v[88:91], a[20:23], a[116:119], v[88:91]\n"
"s_addk_i32 s79, 65\n"
"ds_read_b128 a[0:3], v15 offset:41280\n"
"v_cvt_pk_bf16_f32 v189, v74, v75\n"
"v_mfma_f32_16x16x32_bf16 a[160:163], a[176:179], v[164:167], a[160:163]\n"
"ds_read_b128 a[4:7], v16 offset:41280\n"
"v_mfma_f32_16x16x32_bf16 a[164:167], a[176:179], v[168:171], a[164:167]\n"
"s_sub_u32 s0, s79, s2\n"
"v_exp_f32 v76, v76\n"
"v_mfma_f32_16x16x32_bf16 a[168:171], a[176:179], v[172:175], a[168:171]\n"
"ds_read_b128 a[8:11], v17 offset:41280\n"
"v_mfma_f32_16x16x32_bf16 a[172:175], a[176:179], v[176:179], a[172:175]\n"
"s_sub_u32 s0, s0, s23\n"
"v_exp_f32 v77, v77\n"
"v_mfma_f32_16x16x32_bf16 v[80:83], a[24:27], a[56:59], v[80:83]\n"
"v_exp_f32 v78, v78\n"
"v_mfma_f32_16x16x32_bf16 v[80:83], a[28:31], a[60:63], v[80:83]\n"
"s_sub_u32 s79, s77, s2\n"
"ds_read_b128 a[12:15], v18 offset:41280\n"
"v_cvt_pk_bf16_f32 v192, v76, v77\n"
"v_mfma_f32_16x16x32_bf16 v[84:87], a[28:31], a[92:95], v[84:87]\n"
"v_exp_f32 v79, v79\n"
"v_mfma_f32_16x16x32_bf16 v[84:87], a[24:27], a[88:91], v[84:87]\n"
"s_addk_i32 s79, 1691\n"
"buffer_load_dwordx4 v26, s[48:51], s81 offen offset:1024 nt lds\n"
"v_mfma_f32_16x16x32_bf16 v[88:91], a[24:27], a[120:123], v[88:91]\n"
"s_waitcnt lgkmcnt(2)\n"
"v_exp_f32 v80, v80\n"
"v_mfma_f32_16x16x32_bf16 v[88:91], a[28:31], a[124:127], v[88:91]\n"
"s_max_i32 s0, s0, s79\n"
"v_exp_f32 v81, v81\n"
"v_mfma_f32_16x16x32_bf16 v[48:51], a[0:3], a[32:35], v[32:35]\n"
"ds_read_b128 a[16:19], v15 offset:41536\n"
"v_cvt_pk_bf16_f32 v80, v80, v81\n"
"v_mfma_f32_16x16x32_bf16 v[48:51], a[4:7], a[36:39], v[48:51]\n"
"s_min_i32 s0, s0, 40\n"
"v_exp_f32 v82, v82\n"
"v_mfma_f32_16x16x32_bf16 v[52:55], a[4:7], a[68:71], v[36:39]\n"
"v_exp_f32 v83, v83\n"
"v_mfma_f32_16x16x32_bf16 v[52:55], a[0:3], a[64:67], v[52:55]\n"
"s_max_i32 s0, s0, 0\n"
"ds_read_b128 a[20:23], v16 offset:41536\n"
"v_cvt_pk_bf16_f32 v82, v82, v83\n"
"v_mfma_f32_16x16x32_bf16 v[56:59], a[0:3], a[96:99], v[40:43]\n"
"v_exp_f32 v84, v84\n"
"v_mfma_f32_16x16x32_bf16 v[56:59], a[4:7], a[100:103], v[56:59]\n"
"s_add_u32 s79, s2, s0\n"
"v_exp_f32 v85, v85\n"
"v_mfma_f32_16x16x32_bf16 v[60:63], a[4:7], a[132:135], v[44:47]\n"
"s_waitcnt lgkmcnt(2)\n"
"v_accvgpr_write_b32 a220, v80\n"
"v_accvgpr_write_b32 a221, v82\n"
"v_mfma_f32_16x16x32_bf16 v[60:63], a[0:3], a[128:131], v[60:63]\n"
"s_lshr_b64 vcc, s[0:1], 2\n"
"v_exp_f32 v86, v86\n"
"v_mfma_f32_16x16x32_bf16 v[48:51], a[8:11], a[40:43], v[48:51]\n"
"ds_read_b128 a[24:27], v17 offset:41536\n"
"v_cvt_pk_bf16_f32 v84, v84, v85\n"
"v_mfma_f32_16x16x32_bf16 v[48:51], a[12:15], a[44:47], v[48:51]\n"
"s_cbranch_vccnz PC23_%=\n"
"PB23_%=:\n"
"v_accvgpr_write_b32 a224, v84\n"
"v_mfma_f32_16x16x32_bf16 v[52:55], a[12:15], a[76:79], v[52:55]\n"
"v_exp_f32 v87, v87\n"
"v_mfma_f32_16x16x32_bf16 v[52:55], a[8:11], a[72:75], v[52:55]\n"
"buffer_load_dwordx4 v27, s[48:51], s81 offen offset:2048 nt lds\n"
"v_mfma_f32_16x16x32_bf16 v[56:59], a[8:11], a[104:107], v[56:59]\n"
"ds_read_b128 a[28:31], v18 offset:41536\n"
"v_cvt_pk_bf16_f32 v86, v86, v87\n"
"v_mfma_f32_16x16x32_bf16 v[56:59], a[12:15], a[108:111], v[56:59]\n"
"v_exp_f32 v88, v88\n"
"v_mfma_f32_16x16x32_bf16 v[60:63], a[12:15], a[140:143], v[60:63]\n"
"s_waitcnt lgkmcnt(2)\n"
"v_exp_f32 v89, v89\n"
"v_mfma_f32_16x16x32_bf16 v[60:63], a[8:11], a[136:139], v[60:63]\n"
"v_accvgpr_write_b32 a225, v86\n"
"v_cvt_pk_bf16_f32 v196, v88, v89\n"
"v_mfma_f32_16x16x32_bf16 v[48:51], a[16:19], a[48:51], v[48:51]\n"
"ds_read_b128 a[0:3], v19 offset:41280\n"
"v_cvt_pk_bf16_f32 v193, v78, v79\n"
"v_mfma_f32_16x16x32_bf16 v[48:51], a[20:23], a[52:55], v[48:51]\n"
"v_exp_f32 v90, v90\n"
"v_mfma_f32_16x16x32_bf16 v[52:55], a[20:23], a[84:87], v[52:55]\n"
"v_exp_f32 v91, v91\n"
"v_mfma_f32_16x16x32_bf16 v[52:55], a[16:19], a[80:83], v[52:55]\n"
"ds_read_b128 a[4:7], v20 offset:41280\n"
"v_cvt_pk_bf16_f32 v197, v90, v91\n"
"v_mfma_f32_16x16x32_bf16 v[56:59], a[16:19], a[112:115], v[56:59]\n"
"v_mfma_f32_16x16x32_bf16 v[56:59], a[20:23], a[116:119], v[56:59]\n"
"v_mfma_f32_16x16x32_bf16 v[60:63], a[20:23], a[148:151], v[60:63]\n"
"s_waitcnt lgkmcnt(2)\n"
"ds_read_b128 a[8:11], v21 offset:41280\n"
"v_mfma_f32_16x16x32_bf16 v[60:63], a[16:19], a[144:147], v[60:63]\n"
"v_mfma_f32_16x16x32_bf16 v[48:51], a[24:27], a[56:59], v[48:51]\n"
"buffer_load_dwordx4 v28, s[48:51], s81 offen offset:3072 nt lds\n"
"v_mfma_f32_16x16x32_bf16 v[48:51], a[28:31], a[60:63], v[48:51]\n"
"ds_read_b128 a[12:15], v22 offset:41280\n"
"v_mfma_f32_16x16x32_bf16 v[52:55], a[28:31], a[92:95], v[52:55]\n"
"v_mfma_f32_16x16x32_bf16 v[52:55], a[24:27], a[88:91], v[52:55]\n"
"v_mfma_f32_16x16x32_bf16 v[56:59], a[24:27], a[120:123], v[56:59]\n"
"ds_read_b128 a[16:19], v19 offset:41536\n"
"v_mfma_f32_16x16x32_bf16 v[56:59], a[28:31], a[124:127], v[56:59]\n"
"v_mfma_f32_16x16x32_bf16 v[60:63], a[28:31], a[156:159], v[60:63]\n"
"s_waitcnt lgkmcnt(2)\n"
"v_mfma_f32_16x16x32_bf16 v[60:63], a[24:27], a[152:155], v[60:63]\n"
"ds_read_b128 a[20:23], v20 offset:41536\n"
"v_mfma_f32_16x16x32_bf16 v[64:67], a[0:3], a[32:35], v[32:35]\n"
"v_exp_f32 v48, v48\n"
"v_mfma_f32_16x16x32_bf16 v[64:67], a[4:7], a[36:39], v[64:67]\n"
"v_exp_f32 v49, v49\n"
"v_mfma_f32_16x16x32_bf16 v[68:71], a[4:7], a[68:71], v[36:39]\n"
"s_addk_i32 m0, 8256\n"
"v_cvt_pk_bf16_f32 v182, v48, v49\n"
"s_add_u32 s81, s65, 65536\n"
"v_mfma_f32_16x16x32_bf16 v[68:71], a[0:3], a[64:67], v[68:71]\n"
"v_exp_f32 v50, v50\n"
"v_mfma_f32_16x16x32_bf16 v[72:75], a[0:3], a[96:99], v[40:43]\n"
"s_waitcnt lgkmcnt(1)\n"
"v_exp_f32 v51, v51\n"
"v_mfma_f32_16x16x32_bf16 v[72:75], a[4:7], a[100:103], v[72:75]\n"
"s_memrealtime s[2:3]\n"
"buffer_load_dwordx4 v25, s[48:51], s81 offen offset:0 nt lds\n"
"v_mfma_f32_16x16x32_bf16 v[64:67], a[8:11], a[40:43], v[64:67]\n"
"ds_read_b128 a[24:27], v21 offset:41536\n"
"v_cvt_pk_bf16_f32 v183, v50, v51\n"
"v_mfma_f32_16x16x32_bf16 v[64:67], a[12:15], a[44:47], v[64:67]\n"
"v_exp_f32 v52, v52\n"
"v_mfma_f32_16x16x32_bf16 v[68:71], a[12:15], a[76:79], v[68:71]\n"
"ds_read_b128 a[28:31], v22 offset:41536\n"
"v_mfma_f32_16x16x32_bf16 v[68:71], a[8:11], a[72:75], v[68:71]\n"
"v_exp_f32 v53, v53\n"
"v_mfma_f32_16x16x32_bf16 v[72:75], a[8:11], a[104:107], v[72:75]\n"
"s_waitcnt lgkmcnt(2)\n"
"v_exp_f32 v54, v54\n"
"v_mfma_f32_16x16x32_bf16 v[72:75], a[12:15], a[108:111], v[72:75]\n"
"v_cvt_pk_bf16_f32 v186, v52, v53\n"
"v_mfma_f32_16x16x32_bf16 v[64:67], a[16:19], a[48:51], v[64:67]\n"
"v_exp_f32 v55, v55\n"
"v_mfma_f32_16x16x32_bf16 v[64:67], a[20:23], a[52:55], v[64:67]\n"
"v_exp_f32 v56, v56\n"
"v_mfma_f32_16x16x32_bf16 v[68:71], a[20:23], a[84:87], v[68:71]\n"
"s_waitcnt vmcnt(27) lgkmcnt(0)\n"
"v_cvt_pk_bf16_f32 v187, v54, v55\n"
"v_mfma_f32_16x16x32_bf16 v[68:71], a[16:19], a[80:83], v[68:71]\n"
"s_barrier\n"
"v_mfma_f32_16x16x32_bf16 v[72:75], a[16:19], a[112:115], v[72:75]\n"
"s_addk_i32 s79, 64\n"
"ds_read_b128 a[0:3], v15 offset:49536\n"
"v_mfma_f32_16x16x32_bf16 v[72:75], a[20:23], a[116:119], v[72:75]\n"
"ds_read_b128 a[4:7], v16 offset:49536\n"
"v_mfma_f32_16x16x32_bf16 a[160:163], a[176:179], a[208:211], a[160:163]\n"
"s_sub_u32 s0, s79, s2\n"
"v_exp_f32 v57, v57\n"
"v_mfma_f32_16x16x32_bf16 a[164:167], a[176:179], a[212:215], a[164:167]\n"
"buffer_load_dwordx4 v26, s[48:51], s81 offen offset:1024 nt lds\n"
"v_mfma_f32_16x16x32_bf16 a[168:171], a[176:179], a[216:219], a[168:171]\n"
"s_sub_u32 s0, s0, s23\n"
"ds_read_b128 a[8:11], v17 offset:49536\n"
"v_cvt_pk_bf16_f32 v190, v56, v57\n"
"v_mfma_f32_16x16x32_bf16 v[64:67], a[24:27], a[56:59], v[64:67]\n"
"v_exp_f32 v58, v58\n"
"v_mfma_f32_16x16x32_bf16 v[64:67], a[28:31], a[60:63], v[64:67]\n"
"s_max_i32 s0, s0, 0\n"
"ds_read_b128 a[12:15], v18 offset:49536\n"
"v_mfma_f32_16x16x32_bf16 v[68:71], a[28:31], a[92:95], v[68:71]\n"
"v_exp_f32 v59, v59\n"
"v_mfma_f32_16x16x32_bf16 v[68:71], a[24:27], a[88:91], v[68:71]\n"
"s_add_u32 s79, s2, s0\n"
"v_exp_f32 v60, v60\n"
"v_mfma_f32_16x16x32_bf16 v[72:75], a[24:27], a[120:123], v[72:75]\n"
"s_waitcnt lgkmcnt(2)\n"
"v_cvt_pk_bf16_f32 v191, v58, v59\n"
"v_mfma_f32_16x16x32_bf16 v[72:75], a[28:31], a[124:127], v[72:75]\n"
"s_lshr_b64 vcc, s[0:1], 2\n"
"v_exp_f32 v61, v61\n"
"v_mfma_f32_16x16x32_bf16 v[80:83], a[0:3], a[32:35], v[32:35]\n"
"ds_read_b128 a[16:19], v15 offset:49792\n"
"v_cvt_pk_bf16_f32 v194, v60, v61\n"
"v_mfma_f32_16x16x32_bf16 v[80:83], a[4:7], a[36:39], v[80:83]\n"
"s_cbranch_vccnz PC24_%=\n"
"PB24_%=:\n"
"v_xor_b32 v6, 0, v29\n"
"v_mfma_f32_16x16x32_bf16 v[84:87], a[4:7], a[68:71], v[36:39]\n"
"v_exp_f32 v62, v62\n"
"v_mfma_f32_16x16x32_bf16 v[84:87], a[0:3], a[64:67], v[84:87]\n"
"ds_read_b128 a[20:23], v16 offset:49792\n"
"v_add_u32 v2, s83, v6\n"
"v_mfma_f32_16x16x32_bf16 v[88:91], a[0:3], a[96:99], v[40:43]\n"
"v_exp_f32 v63, v63\n"
"v_mfma_f32_16x16x32_bf16 v[88:91], a[4:7], a[100:103], v[88:91]\n"
"buffer_load_dwordx4 v27, s[48:51], s81 offen offset:2048 nt lds\n"
"v_mfma_f32_16x16x32_bf16 v[92:95], a[4:7], a[132:135], v[44:47]\n"
"s_waitcnt lgkmcnt(2)\n"
"v_exp_f32 v64, v64\n"
"v_mfma_f32_16x16x32_bf16 v[92:95], a[0:3], a[128:131], v[92:95]\n"
"v_exp_f32 v65, v65\n"
"v_mfma_f32_16x16x32_bf16 v[80:83], a[8:11], a[40:43], v[80:83]\n"
"ds_read_b128 a[24:27], v17 offset:49792\n"
"v_cvt_pk_bf16_f32 v195, v62, v63\n"
"v_mfma_f32_16x16x32_bf16 v[80:83], a[12:15], a[44:47], v[80:83]\n"
"v_exp_f32 v66, v66\n"
"v_mfma_f32_16x16x32_bf16 v[84:87], a[12:15], a[76:79], v[84:87]\n"
"v_exp_f32 v67, v67\n"
"v_mfma_f32_16x16x32_bf16 v[84:87], a[8:11], a[72:75], v[84:87]\n"
"ds_read_b128 a[28:31], v18 offset:49792\n"
"v_cvt_pk_bf16_f32 v64, v64, v65\n"
"v_mfma_f32_16x16x32_bf16 v[88:91], a[8:11], a[104:107], v[88:91]\n"
"v_exp_f32 v68, v68\n"
"v_mfma_f32_16x16x32_bf16 v[88:91], a[12:15], a[108:111], v[88:91]\n"
"v_exp_f32 v69, v69\n"
"v_mfma_f32_16x16x32_bf16 v[92:95], a[12:15], a[140:143], v[92:95]\n"
"s_waitcnt lgkmcnt(2)\n"
"v_cvt_pk_bf16_f32 v66, v66, v67\n"
"v_accvgpr_write_b32 a222, v64\n"
"v_mfma_f32_16x16x32_bf16 v[92:95], a[8:11], a[136:139], v[92:95]\n"
"v_exp_f32 v70, v70\n"
"v_mfma_f32_16x16x32_bf16 v[80:83], a[16:19], a[48:51], v[80:83]\n"
"ds_read_b128 a[0:3], v19 offset:49536\n"
"v_accvgpr_write_b32 a223, v66\n"
"v_mfma_f32_16x16x32_bf16 v[80:83], a[20:23], a[52:55], v[80:83]\n"
"v_exp_f32 v71, v71\n"
"v_mfma_f32_16x16x32_bf16 v[84:87], a[20:23], a[84:87], v[84:87]\n"
"buffer_load_dwordx4 v28, s[48:51], s81 offen offset:3072 nt lds\n"
"v_mfma_f32_16x16x32_bf16 v[84:87], a[16:19], a[80:83], v[84:87]\n"
"ds_read_b128 a[4:7], v20 offset:49536\n"
"v_cvt_pk_bf16_f32 v68, v68, v69\n"
"v_mfma_f32_16x16x32_bf16 v[88:91], a[16:19], a[112:115], v[88:91]\n"
"v_exp_f32 v72, v72\n"
"v_mfma_f32_16x16x32_bf16 v[88:91], a[20:23], a[116:119], v[88:91]\n"
"v_exp_f32 v73, v73\n"
"v_mfma_f32_16x16x32_bf16 v[92:95], a[20:23], a[148:151], v[92:95]\n"
"s_waitcnt lgkmcnt(2)\n"
"ds_read_b128 a[8:11], v21 offset:49536\n"
"v_cvt_pk_bf16_f32 v70, v70, v71\n"
"v_mfma_f32_16x16x32_bf16 v[92:95], a[16:19], a[144:147], v[92:95]\n"
"v_exp_f32 v74, v74\n"
"v_mfma_f32_16x16x32_bf16 v[80:83], a[24:27], a[56:59], v[80:83]\n"
"v_exp_f32 v75, v75\n"
"v_mfma_f32_16x16x32_bf16 v[80:83], a[28:31], a[60:63], v[80:83]\n"
"ds_read_b128 a[12:15], v22 offset:49536\n"
"v_accvgpr_write_b32 a226, v68\n"
"v_mfma_f32_16x16x32_bf16 v[84:87], a[28:31], a[92:95], v[84:87]\n"
"v_accvgpr_write_b32 a227, v70\n"
"v_cvt_pk_bf16_f32 v198, v72, v73\n"
"v_mfma_f32_16x16x32_bf16 v[84:87], a[24:27], a[88:91], v[84:87]\n"
"v_cvt_pk_bf16_f32 v199, v74, v75\n"
"v_add_u32 v6, s84, v6\n"
"v_mfma_f32_16x16x32_bf16 v[88:91], a[24:27], a[120:123], v[88:91]\n"
"s_addk_i32 m0, 16512\n"
"ds_read_b128 a[16:19], v19 offset:49792\n"
"v_xor_b32 v7, 32, v29\n"
"v_mfma_f32_16x16x32_bf16 v[88:91], a[28:31], a[124:127], v[88:91]\n"
"s_add_u32 s81, s65, 131072\n"
"v_exp_f32 v80, v80\n"
"v_mfma_f32_16x16x32_bf16 v[92:95], a[28:31], a[156:159], v[92:95]\n"
"s_waitcnt lgkmcnt(2)\n"
"v_exp_f32 v81, v81\n"
"v_mfma_f32_16x16x32_bf16 v[92:95], a[24:27], a[152:155], v[92:95]\n"
"buffer_load_dwordx4 v25, s[48:51], s81 offen offset:0 nt lds\n"
"v_mfma_f32_16x16x32_bf16 v[48:51], a[0:3], a[32:35], v[32:35]\n"
"ds_read_b128 a[20:23], v20 offset:49792\n"
"v_cvt_pk_bf16_f32 v200, v80, v81\n"
"v_mfma_f32_16x16x32_bf16 v[48:51], a[4:7], a[36:39], v[48:51]\n"
"v_exp_f32 v82, v82\n"
"v_mfma_f32_16x16x32_bf16 v[52:55], a[4:7], a[68:71], v[36:39]\n"
"v_exp_f32 v83, v83\n"
"v_mfma_f32_16x16x32_bf16 v[52:55], a[0:3], a[64:67], v[52:55]\n"
"v_cvt_pk_bf16_f32 v201, v82, v83\n"
"v_add_u32 v3, s83, v7\n"
"v_mfma_f32_16x16x32_bf16 v[56:59], a[0:3], a[96:99], v[40:43]\n"
"s_waitcnt lgkmcnt(1)\n"
"v_exp_f32 v84, v84\n"
"v_mfma_f32_16x16x32_bf16 v[56:59], a[4:7], a[100:103], v[56:59]\n"
"s_memrealtime s[2:3]\n"
"v_exp_f32 v85, v85\n"
"v_mfma_f32_16x16x32_bf16 v[48:51], a[8:11], a[40:43], v[48:51]\n"
"ds_read_b128 a[24:27], v21 offset:49792\n"
"v_cvt_pk_bf16_f32 v204, v84, v85\n"
"v_mfma_f32_16x16x32_bf16 v[48:51], a[12:15], a[44:47], v[48:51]\n"
"v_exp_f32 v86, v86\n"
"v_mfma_f32_16x16x32_bf16 v[52:55], a[12:15], a[76:79], v[52:55]\n"
"v_exp_f32 v87, v87\n"
"v_mfma_f32_16x16x32_bf16 v[52:55], a[8:11], a[72:75], v[52:55]\n"
"ds_read_b128 a[28:31], v22 offset:49792\n"
"v_cvt_pk_bf16_f32 v205, v86, v87\n"
"v_mfma_f32_16x16x32_bf16 v[56:59], a[8:11], a[104:107], v[56:59]\n"
"s_waitcnt lgkmcnt(2)\n"
"v_exp_f32 v88, v88\n"
"v_mfma_f32_16x16x32_bf16 v[56:59], a[12:15], a[108:111], v[56:59]\n"
"v_exp_f32 v89, v89\n"
"v_mfma_f32_16x16x32_bf16 v[48:51], a[16:19], a[48:51], v[48:51]\n"
"v_cvt_pk_bf16_f32 v208, v88, v89\n"
"v_add_u32 v7, s84, v7\n"
"v_mfma_f32_16x16x32_bf16 v[48:51], a[20:23], a[52:55], v[48:51]\n"
"buffer_load_dwordx4 v26, s[48:51], s81 offen offset:1024 nt lds\n"
"v_mfma_f32_16x16x32_bf16 v[52:55], a[20:23], a[84:87], v[52:55]\n"
"v_exp_f32 v90, v90\n"
"v_mfma_f32_16x16x32_bf16 v[52:55], a[16:19], a[80:83], v[52:55]\n"
"s_waitcnt vmcnt(27) lgkmcnt(0)\n"
"v_exp_f32 v91, v91\n"
"v_mfma_f32_16x16x32_bf16 v[56:59], a[16:19], a[112:115], v[56:59]\n"
"s_barrier\n"
"v_mfma_f32_16x16x32_bf16 v[56:59], a[20:23], a[116:119], v[56:59]\n"
"s_addk_i32 s79, 65\n"
"ds_read_b128 a[0:3], v15 offset:57792\n"
"v_cvt_pk_bf16_f32 v209, v90, v91\n"
"v_mfma_f32_16x16x32_bf16 a[160:163], a[176:179], v[180:183], a[160:163]\n"
"ds_read_b128 a[4:7], v16 offset:57792\n"
"v_xor_b32 v8, 64, v29\n"
"v_mfma_f32_16x16x32_bf16 a[164:167], a[176:179], v[184:187], a[164:167]\n"
"s_sub_u32 s0, s79, s2\n"
"v_exp_f32 v92, v92\n"
"v_mfma_f32_16x16x32_bf16 a[168:171], a[176:179], v[188:191], a[168:171]\n"
"ds_read_b128 a[8:11], v17 offset:57792\n"
"v_add_u32 v4, s83, v8\n"
"v_mfma_f32_16x16x32_bf16 a[172:175], a[176:179], v[192:195], a[172:175]\n"
"s_sub_u32 s0, s0, s23\n"
"v_exp_f32 v93, v93\n"
"v_mfma_f32_16x16x32_bf16 v[48:51], a[24:27], a[56:59], v[48:51]\n"
"v_exp_f32 v94, v94\n"
"v_mfma_f32_16x16x32_bf16 v[48:51], a[28:31], a[60:63], v[48:51]\n"
"s_max_i32 s0, s0, 0\n"
"ds_read_b128 a[12:15], v18 offset:57792\n"
"v_cvt_pk_bf16_f32 v212, v92, v93\n"
"v_mfma_f32_16x16x32_bf16 v[52:55], a[28:31], a[92:95], v[52:55]\n"
"v_exp_f32 v95, v95\n"
"v_mfma_f32_16x16x32_bf16 v[52:55], a[24:27], a[88:91], v[52:55]\n"
"s_add_u32 s79, s2, s0\n"
"v_cvt_pk_bf16_f32 v213, v94, v95\n"
"v_add_u32 v8, s84, v8\n"
"v_mfma_f32_16x16x32_bf16 v[56:59], a[24:27], a[120:123], v[56:59]\n"
"s_waitcnt lgkmcnt(2)\n"
"buffer_load_dwordx4 v27, s[48:51], s81 offen offset:2048 nt lds\n"
"v_mfma_f32_16x16x32_bf16 v[56:59], a[28:31], a[124:127], v[56:59]\n"
"s_lshr_b64 vcc, s[0:1], 2\n"
"v_exp_f32 v48, v48\n"
"PG7_%=:\n"
"s_cbranch_execz PG11_%=\n"
"v_mfma_f32_16x16x32_bf16 v[64:67], a[0:3], a[32:35], v[32:35]\n"
"ds_read_b128 a[16:19], v15 offset:58048\n"
"v_add_u32 v15, s27, v15\n"
"v_mfma_f32_16x16x32_bf16 v[64:67], a[4:7], a[36:39], v[64:67]\n"
"s_cbranch_vccnz PC25_%=\n"
"PB25_%=:\n"
"v_xor_b32 v9, 96, v29\n"
"v_mfma_f32_16x16x32_bf16 v[68:71], a[4:7], a[68:71], v[36:39]\n"
"v_exp_f32 v49, v49\n"
"v_mfma_f32_16x16x32_bf16 v[68:71], a[0:3], a[64:67], v[68:71]\n"
"ds_read_b128 a[20:23], v16 offset:58048\n"
"v_add_u32 v16, s27, v16\n"
"v_mfma_f32_16x16x32_bf16 v[72:75], a[0:3], a[96:99], v[40:43]\n"
"v_exp_f32 v50, v50\n"
"v_mfma_f32_16x16x32_bf16 v[72:75], a[4:7], a[100:103], v[72:75]\n"
"v_exp_f32 v51, v51\n"
"v_mfma_f32_16x16x32_bf16 v[76:79], a[4:7], a[132:135], v[44:47]\n"
"s_waitcnt lgkmcnt(2)\n"
"v_cvt_pk_bf16_f32 v48, v48, v49\n"
"v_cvt_pk_bf16_f32 v50, v50, v51\n"
"v_mfma_f32_16x16x32_bf16 v[76:79], a[0:3], a[128:131], v[76:79]\n"
"v_exp_f32 v52, v52\n"
"v_mfma_f32_16x16x32_bf16 v[64:67], a[8:11], a[40:43], v[64:67]\n"
"ds_read_b128 a[24:27], v17 offset:58048\n"
"v_accvgpr_write_b32 a228, v48\n"
"v_mfma_f32_16x16x32_bf16 v[64:67], a[12:15], a[44:47], v[64:67]\n"
"v_exp_f32 v53, v53\n"
"v_mfma_f32_16x16x32_bf16 v[68:71], a[12:15], a[76:79], v[68:71]\n"
"v_exp_f32 v54, v54\n"
"v_mfma_f32_16x16x32_bf16 v[68:71], a[8:11], a[72:75], v[68:71]\n"
"buffer_load_dwordx4 v28, s[48:51], s81 offen offset:3072 nt lds\n"
"v_mfma_f32_16x16x32_bf16 v[72:75], a[8:11], a[104:107], v[72:75]\n"
"ds_read_b128 a[28:31], v18 offset:58048\n"
"v_accvgpr_write_b32 a229, v50\n"
"v_mfma_f32_16x16x32_bf16 v[72:75], a[12:15], a[108:111], v[72:75]\n"
"v_exp_f32 v55, v55\n"
"v_mfma_f32_16x16x32_bf16 v[76:79], a[12:15], a[140:143], v[76:79]\n"
"s_waitcnt lgkmcnt(2)\n"
"v_exp_f32 v56, v56\n"
"v_mfma_f32_16x16x32_bf16 v[76:79], a[8:11], a[136:139], v[76:79]\n"
"v_cvt_pk_bf16_f32 v52, v52, v53\n"
"v_cvt_pk_bf16_f32 v54, v54, v55\n"
"v_mfma_f32_16x16x32_bf16 v[64:67], a[16:19], a[48:51], v[64:67]\n"
"ds_read_b128 a[0:3], v19 offset:57792\n"
"v_accvgpr_write_b32 a232, v52\n"
"v_mfma_f32_16x16x32_bf16 v[64:67], a[20:23], a[52:55], v[64:67]\n"
"v_exp_f32 v57, v57\n"
"v_mfma_f32_16x16x32_bf16 v[68:71], a[20:23], a[84:87], v[68:71]\n"
"v_exp_f32 v58, v58\n"
"v_mfma_f32_16x16x32_bf16 v[68:71], a[16:19], a[80:83], v[68:71]\n"
"ds_read_b128 a[4:7], v20 offset:57792\n"
"v_accvgpr_write_b32 a233, v54\n"
"v_mfma_f32_16x16x32_bf16 v[72:75], a[16:19], a[112:115], v[72:75]\n"
"v_exp_f32 v59, v59\n"
"v_mfma_f32_16x16x32_bf16 v[72:75], a[20:23], a[116:119], v[72:75]\n"
"v_add_u32 v17, s27, v17\n"
"v_add_u32 v18, s27, v18\n"
"v_mfma_f32_16x16x32_bf16 v[76:79], a[20:23], a[148:151], v[76:79]\n"
"s_waitcnt lgkmcnt(2)\n"
"ds_read_b128 a[8:11], v21 offset:57792\n"
"v_cvt_pk_bf16_f32 v56, v56, v57\n"
"v_mfma_f32_16x16x32_bf16 v[76:79], a[16:19], a[144:147], v[76:79]\n"
"s_addk_i32 m0, 8256\n"
"v_cvt_pk_bf16_f32 v58, v58, v59\n"
"v_accvgpr_write_b32 a236, v56\n"
"v_mfma_f32_16x16x32_bf16 v[64:67], a[24:27], a[56:59], v[64:67]\n"
"s_add_u32 s81, s65, 196608\n"
"v_accvgpr_write_b32 a237, v58\n"
"v_add_u32 v5, s83, v9\n"
"v_mfma_f32_16x16x32_bf16 v[64:67], a[28:31], a[60:63], v[64:67]\n"
"buffer_load_dwordx4 v25, s[48:51], s81 offen offset:0 nt lds\n"
"v_mfma_f32_16x16x32_bf16 v[68:71], a[28:31], a[92:95], v[68:71]\n"
"ds_read_b128 a[12:15], v22 offset:57792\n"
"v_add_u32 v9, s84, v9\n"
"v_mfma_f32_16x16x32_bf16 v[68:71], a[24:27], a[88:91], v[68:71]\n"
"v_mfma_f32_16x16x32_bf16 v[72:75], a[24:27], a[120:123], v[72:75]\n"
"ds_read_b128 a[16:19], v19 offset:58048\n"
"v_add_u32 v19, s27, v19\n"
"v_mfma_f32_16x16x32_bf16 v[72:75], a[28:31], a[124:127], v[72:75]\n"
"v_exp_f32 v64, v64\n"
"v_mfma_f32_16x16x32_bf16 v[76:79], a[28:31], a[156:159], v[76:79]\n"
"s_waitcnt lgkmcnt(2)\n"
"v_exp_f32 v65, v65\n"
"v_mfma_f32_16x16x32_bf16 v[76:79], a[24:27], a[152:155], v[76:79]\n"
"ds_read_b128 a[20:23], v20 offset:58048\n"
"v_add_u32 v20, s27, v20\n"
"v_mfma_f32_16x16x32_bf16 v[80:83], a[0:3], a[32:35], v[32:35]\n"
"v_exp_f32 v66, v66\n"
"v_mfma_f32_16x16x32_bf16 v[80:83], a[4:7], a[36:39], v[80:83]\n"
"v_exp_f32 v67, v67\n"
"v_mfma_f32_16x16x32_bf16 v[84:87], a[4:7], a[68:71], v[36:39]\n"
"v_cvt_pk_bf16_f32 v202, v64, v65\n"
"v_cvt_pk_bf16_f32 v203, v66, v67\n"
"v_mfma_f32_16x16x32_bf16 v[84:87], a[0:3], a[64:67], v[84:87]\n"
"v_exp_f32 v68, v68\n"
"v_mfma_f32_16x16x32_bf16 v[88:91], a[0:3], a[96:99], v[40:43]\n"
"s_waitcnt lgkmcnt(1)\n"
"v_exp_f32 v69, v69\n"
"v_mfma_f32_16x16x32_bf16 v[88:91], a[4:7], a[100:103], v[88:91]\n"
"s_memrealtime s[2:3]\n"
"ds_read_b128 a[24:27], v21 offset:58048\n"
"v_add_u32 v21, s27, v21\n"
"v_mfma_f32_16x16x32_bf16 v[80:83], a[8:11], a[40:43], v[80:83]\n"
"buffer_load_dwordx4 v26, s[48:51], s81 offen offset:1024 nt lds\n"
"v_mfma_f32_16x16x32_bf16 v[80:83], a[12:15], a[44:47], v[80:83]\n"
"v_exp_f32 v70, v70\n"
"v_mfma_f32_16x16x32_bf16 v[84:87], a[12:15], a[76:79], v[84:87]\n"
"ds_read_b128 a[28:31], v22 offset:58048\n"
"v_add_u32 v22, s27, v22\n"
"v_mfma_f32_16x16x32_bf16 v[84:87], a[8:11], a[72:75], v[84:87]\n"
"v_exp_f32 v71, v71\n"
"v_mfma_f32_16x16x32_bf16 v[88:91], a[8:11], a[104:107], v[88:91]\n"
"s_waitcnt lgkmcnt(2)\n"
"v_exp_f32 v72, v72\n"
"v_mfma_f32_16x16x32_bf16 v[88:91], a[12:15], a[108:111], v[88:91]\n"
"v_cvt_pk_bf16_f32 v206, v68, v69\n"
"v_cvt_pk_bf16_f32 v207, v70, v71\n"
"v_mfma_f32_16x16x32_bf16 v[80:83], a[16:19], a[48:51], v[80:83]\n"
"v_exp_f32 v73, v73\n"
"v_mfma_f32_16x16x32_bf16 v[80:83], a[20:23], a[52:55], v[80:83]\n"
"v_exp_f32 v74, v74\n"
"v_mfma_f32_16x16x32_bf16 v[84:87], a[20:23], a[84:87], v[84:87]\n"
"s_waitcnt vmcnt(27) lgkmcnt(0)\n"
"v_cvt_pk_bf16_f32 v210, v72, v73\n"
"v_mfma_f32_16x16x32_bf16 v[84:87], a[16:19], a[80:83], v[84:87]\n"
"s_barrier\n"
"v_mfma_f32_16x16x32_bf16 v[88:91], a[16:19], a[112:115], v[88:91]\n"
"s_addk_i32 s79, 64\n"
"ds_read_b128 a[0:3], v15 offset:57792\n"
"v_mfma_f32_16x16x32_bf16 v[88:91], a[20:23], a[116:119], v[88:91]\n"
"ds_read_b128 a[4:7], v16 offset:57792\n"
"v_mfma_f32_16x16x32_bf16 a[160:163], a[176:179], a[220:223], a[160:163]\n"
"s_sub_u32 s0, s79, s2\n"
"v_exp_f32 v75, v75\n"
"v_mfma_f32_16x16x32_bf16 a[164:167], a[176:179], a[224:227], a[164:167]\n"
"buffer_load_dwordx4 v27, s[48:51], s81 offen offset:2048 nt lds\n"
"v_mfma_f32_16x16x32_bf16 a[168:171], a[176:179], v[196:199], a[168:171]\n"
"s_sub_u32 s0, s0, s23\n"
"ds_read_b128 a[8:11], v17 offset:57792\n"
"v_cvt_pk_bf16_f32 v211, v74, v75\n"
"v_mfma_f32_16x16x32_bf16 v[80:83], a[24:27], a[56:59], v[80:83]\n"
"v_exp_f32 v76, v76\n"
"v_mfma_f32_16x16x32_bf16 v[80:83], a[28:31], a[60:63], v[80:83]\n"
"s_sub_u32 s79, s77, s2\n"
"ds_read_b128 a[12:15], v18 offset:57792\n"
"v_mfma_f32_16x16x32_bf16 v[84:87], a[28:31], a[92:95], v[84:87]\n"
"v_exp_f32 v77, v77\n"
"v_mfma_f32_16x16x32_bf16 v[84:87], a[24:27], a[88:91], v[84:87]\n"
"s_addk_i32 s79, 1895\n"
"v_exp_f32 v78, v78\n"
"v_mfma_f32_16x16x32_bf16 v[88:91], a[24:27], a[120:123], v[88:91]\n"
"s_waitcnt lgkmcnt(2)\n"
"v_cvt_pk_bf16_f32 v214, v76, v77\n"
"v_mfma_f32_16x16x32_bf16 v[88:91], a[28:31], a[124:127], v[88:91]\n"
"s_max_i32 s0, s0, s79\n"
"v_exp_f32 v79, v79\n"
"v_mfma_f32_16x16x32_bf16 v[48:51], a[0:3], a[32:35], v[32:35]\n"
"ds_read_b128 a[16:19], v15 offset:58048\n"
"v_cvt_pk_bf16_f32 v215, v78, v79\n"
"v_mfma_f32_16x16x32_bf16 v[48:51], a[4:7], a[36:39], v[48:51]\n"
"s_min_i32 s0, s0, 40\n"
"v_exp_f32 v80, v80\n"
"v_mfma_f32_16x16x32_bf16 v[52:55], a[4:7], a[68:71], v[36:39]\n"
"v_exp_f32 v81, v81\n"
"v_mfma_f32_16x16x32_bf16 v[52:55], a[0:3], a[64:67], v[52:55]\n"
"s_max_i32 s0, s0, 0\n"
"ds_read_b128 a[20:23], v16 offset:58048\n"
"v_subrev_u32 v15, s27, v15\n"
"v_mfma_f32_16x16x32_bf16 v[56:59], a[0:3], a[96:99], v[40:43]\n"
"v_exp_f32 v82, v82\n"
"v_mfma_f32_16x16x32_bf16 v[56:59], a[4:7], a[100:103], v[56:59]\n"
"s_add_u32 s79, s2, s0\n"
"v_exp_f32 v83, v83\n"
"v_mfma_f32_16x16x32_bf16 v[60:63], a[4:7], a[132:135], v[44:47]\n"
"s_waitcnt lgkmcnt(2)\n"
"buffer_load_dwordx4 v28, s[48:51], s81 offen offset:3072 nt lds\n"
"v_mfma_f32_16x16x32_bf16 v[60:63], a[0:3], a[128:131], v[60:63]\n"
"s_lshr_b64 vcc, s[0:1], 2\n"
"v_exp_f32 v84, v84\n"
"v_mfma_f32_16x16x32_bf16 v[48:51], a[8:11], a[40:43], v[48:51]\n"
"ds_read_b128 a[24:27], v17 offset:58048\n"
"v_subrev_u32 v16, s27, v16\n"
"v_mfma_f32_16x16x32_bf16 v[48:51], a[12:15], a[44:47], v[48:51]\n"
"s_cbranch_vccnz PC26_%=\n"
"PB26_%=:\n"
"v_cvt_pk_bf16_f32 v80, v80, v81\n"
"v_mfma_f32_16x16x32_bf16 v[52:55], a[12:15], a[76:79], v[52:55]\n"
"v_exp_f32 v85, v85\n"
"v_mfma_f32_16x16x32_bf16 v[52:55], a[8:11], a[72:75], v[52:55]\n"
"ds_read_b128 a[28:31], v18 offset:58048\n"
"v_cvt_pk_bf16_f32 v82, v82, v83\n"
"v_mfma_f32_16x16x32_bf16 v[56:59], a[8:11], a[104:107], v[56:59]\n"
"v_exp_f32 v86, v86\n"
"v_mfma_f32_16x16x32_bf16 v[56:59], a[12:15], a[108:111], v[56:59]\n"
"v_exp_f32 v87, v87\n"
"v_mfma_f32_16x16x32_bf16 v[60:63], a[12:15], a[140:143], v[60:63]\n"
"s_waitcnt lgkmcnt(2)\n"
"v_accvgpr_write_b32 a230, v80\n"
"v_accvgpr_write_b32 a231, v82\n"
"v_mfma_f32_16x16x32_bf16 v[60:63], a[8:11], a[136:139], v[60:63]\n"
"v_exp_f32 v88, v88\n"
"v_mfma_f32_16x16x32_bf16 v[48:51], a[16:19], a[48:51], v[48:51]\n"
"s_addk_i32 m0, 8256\n"
"ds_read_b128 a[0:3], v19 offset:57792\n"
"v_cvt_pk_bf16_f32 v84, v84, v85\n"
"v_mfma_f32_16x16x32_bf16 v[48:51], a[20:23], a[52:55], v[48:51]\n"
"s_add_u32 s81, s65, 128\n"
"v_exp_f32 v89, v89\n"
"v_mfma_f32_16x16x32_bf16 v[52:55], a[20:23], a[84:87], v[52:55]\n"
"v_exp_f32 v90, v90\n"
"v_mfma_f32_16x16x32_bf16 v[52:55], a[16:19], a[80:83], v[52:55]\n"
"buffer_load_dwordx4 v25, s[48:51], s81 offen offset:0 nt lds\n"
"v_mfma_f32_16x16x32_bf16 v[56:59], a[16:19], a[112:115], v[56:59]\n"
"ds_read_b128 a[4:7], v20 offset:57792\n"
"v_cvt_pk_bf16_f32 v86, v86, v87\n"
"v_mfma_f32_16x16x32_bf16 v[56:59], a[20:23], a[116:119], v[56:59]\n"
"v_exp_f32 v91, v91\n"
"v_mfma_f32_16x16x32_bf16 v[60:63], a[20:23], a[148:151], v[60:63]\n"
"s_waitcnt lgkmcnt(2)\n"
"ds_read_b128 a[8:11], v21 offset:57792\n"
"v_accvgpr_write_b32 a234, v84\n"
"v_mfma_f32_16x16x32_bf16 v[60:63], a[16:19], a[144:147], v[60:63]\n"
"v_accvgpr_write_b32 a235, v86\n"
"v_subrev_u32 v17, s27, v17\n"
"v_mfma_f32_16x16x32_bf16 v[48:51], a[24:27], a[56:59], v[48:51]\n"
"v_subrev_u32 v18, s27, v18\n"
"v_cvt_pk_bf16_f32 v88, v88, v89\n"
"v_mfma_f32_16x16x32_bf16 v[48:51], a[28:31], a[60:63], v[48:51]\n"
"ds_read_b128 a[12:15], v22 offset:57792\n"
"v_cvt_pk_bf16_f32 v90, v90, v91\n"
"v_mfma_f32_16x16x32_bf16 v[52:55], a[28:31], a[92:95], v[52:55]\n"
"v_accvgpr_write_b32 a238, v88\n"
"v_accvgpr_write_b32 a239, v90\n"
"v_mfma_f32_16x16x32_bf16 v[52:55], a[24:27], a[88:91], v[52:55]\n"
"v_readfirstlane_b32 s88, v96\n"
"v_readfirstlane_b32 s89, v97\n"
"v_mfma_f32_16x16x32_bf16 v[56:59], a[24:27], a[120:123], v[56:59]\n"
"s_add_u32 s88, s88, 0x7f4a7c15\n"
"ds_read_b128 a[16:19], v19 offset:58048\n"
"v_subrev_u32 v19, s27, v19\n"
"v_mfma_f32_16x16x32_bf16 v[56:59], a[28:31], a[124:127], v[56:59]\n"
"s_add_u32 s89, s89, 0x9e3779b9\n"
"v_exp_f32 v48, v48\n"
"v_mfma_f32_16x16x32_bf16 v[60:63], a[28:31], a[156:159], v[60:63]\n"
"s_waitcnt lgkmcnt(2)\n"
"v_exp_f32 v49, v49\n"
"v_mfma_f32_16x16x32_bf16 v[60:63], a[24:27], a[152:155], v[60:63]\n"
"ds_read_b128 a[20:23], v20 offset:58048\n"
"v_subrev_u32 v20, s27, v20\n"
"v_mfma_f32_16x16x32_bf16 v[64:67], a[0:3], a[32:35], v[32:35]\n"
"buffer_load_dwordx4 v26, s[48:51], s81 offen offset:1024 nt lds\n"
"v_mfma_f32_16x16x32_bf16 v[64:67], a[4:7], a[36:39], v[64:67]\n"
"v_exp_f32 v50, v50\n"
"v_mfma_f32_16x16x32_bf16 v[68:71], a[4:7], a[68:71], v[36:39]\n"
"v_exp_f32 v51, v51\n"
"v_mfma_f32_16x16x32_bf16 v[68:71], a[0:3], a[64:67], v[68:71]\n"
"v_cvt_pk_bf16_f32 v216, v48, v49\n"
"v_cvt_pk_bf16_f32 v217, v50, v51\n"
"v_mfma_f32_16x16x32_bf16 v[72:75], a[0:3], a[96:99], v[40:43]\n"
"s_waitcnt lgkmcnt(1)\n"
"v_exp_f32 v52, v52\n"
"v_mfma_f32_16x16x32_bf16 v[72:75], a[4:7], a[100:103], v[72:75]\n"
"s_memrealtime s[2:3]\n"
"v_exp_f32 v53, v53\n"
"v_mfma_f32_16x16x32_bf16 v[64:67], a[8:11], a[40:43], v[64:67]\n"
"ds_read_b128 a[24:27], v21 offset:58048\n"
"v_subrev_u32 v21, s27, v21\n"
"v_mfma_f32_16x16x32_bf16 v[64:67], a[12:15], a[44:47], v[64:67]\n"
"v_exp_f32 v54, v54\n"
"v_mfma_f32_16x16x32_bf16 v[68:71], a[12:15], a[76:79], v[68:71]\n"
"v_exp_f32 v55, v55\n"
"v_mfma_f32_16x16x32_bf16 v[68:71], a[8:11], a[72:75], v[68:71]\n"
"ds_read_b128 a[28:31], v22 offset:58048\n"
"v_subrev_u32 v22, s27, v22\n"
"v_mfma_f32_16x16x32_bf16 v[72:75], a[8:11], a[104:107], v[72:75]\n"
"s_waitcnt lgkmcnt(2)\n"
"v_exp_f32 v56, v56\n"
"v_mfma_f32_16x16x32_bf16 v[72:75], a[12:15], a[108:111], v[72:75]\n"
"v_exp_f32 v57, v57\n"
"v_mfma_f32_16x16x32_bf16 v[64:67], a[16:19], a[48:51], v[64:67]\n"
"v_cvt_pk_bf16_f32 v220, v52, v53\n"
"v_cvt_pk_bf16_f32 v221, v54, v55\n"
"v_mfma_f32_16x16x32_bf16 v[64:67], a[20:23], a[52:55], v[64:67]\n"
"buffer_load_dwordx4 v27, s[48:51], s81 offen offset:2048 nt lds\n"
"v_mfma_f32_16x16x32_bf16 v[68:71], a[20:23], a[84:87], v[68:71]\n"
"v_exp_f32 v58, v58\n"
"v_mfma_f32_16x16x32_bf16 v[68:71], a[16:19], a[80:83], v[68:71]\n"
"s_waitcnt vmcnt(28) lgkmcnt(0)\n"
"v_exp_f32 v59, v59\n"
"v_mfma_f32_16x16x32_bf16 v[72:75], a[16:19], a[112:115], v[72:75]\n"
"s_barrier\n"
"v_mfma_f32_16x16x32_bf16 v[72:75], a[20:23], a[116:119], v[72:75]\n"
"s_addk_i32 s79, 65\n"
"ds_read_b128 a[0:3], v15 offset:33024\n"
"v_cvt_pk_bf16_f32 v224, v56, v57\n"
"v_mfma_f32_16x16x32_bf16 a[160:163], a[176:179], v[200:203], a[160:163]\n"
"ds_read_b128 a[4:7], v16 offset:33024\n"
"v_cvt_pk_bf16_f32 v225, v58, v59\n"
"v_mfma_f32_16x16x32_bf16 a[164:167], a[176:179], v[204:207], a[164:167]\n"
"s_sub_u32 s0, s79, s2\n"
"v_exp_f32 v60, v60\n"
"v_mfma_f32_16x16x32_bf16 a[168:171], a[176:179], v[208:211], a[168:171]\n"
"ds_read_b128 a[8:11], v17 offset:33024\n"
"v_mov_b32 v31, s89\n"
"v_mfma_f32_16x16x32_bf16 a[172:175], a[176:179], v[212:215], a[172:175]\n"
"s_sub_u32 s0, s0, s23\n"
"v_exp_f32 v61, v61\n"
"v_mfma_f32_16x16x32_bf16 v[64:67], a[24:27], a[56:59], v[64:67]\n"
"v_exp_f32 v62, v62\n"
"v_mfma_f32_16x16x32_bf16 v[64:67], a[28:31], a[60:63], v[64:67]\n"
"s_max_i32 s0, s0, 0\n"
"ds_read_b128 a[12:15], v18 offset:33024\n"
"v_cvt_pk_bf16_f32 v228, v60, v61\n"
"v_mfma_f32_16x16x32_bf16 v[68:71], a[28:31], a[92:95], v[68:71]\n"
"v_exp_f32 v63, v63\n"
"v_mfma_f32_16x16x32_bf16 v[68:71], a[24:27], a[88:91], v[68:71]\n"
"s_add_u32 s79, s2, s0\n"
"v_cvt_pk_bf16_f32 v229, v62, v63\n"
"v_mfma_f32_16x16x32_bf16 v[72:75], a[24:27], a[120:123], v[72:75]\n"
"s_waitcnt lgkmcnt(2)\n"
"v_exp_f32 v64, v64\n"
"v_mfma_f32_16x16x32_bf16 v[72:75], a[28:31], a[124:127], v[72:75]\n"
"s_lshr_b64 vcc, s[0:1], 2\n"
"buffer_load_dwordx4 v28, s[48:51], s81 offen offset:3072 nt lds\n"
"v_mfma_f32_16x16x32_bf16 v[80:83], a[0:3], a[32:35], v[32:35]\n"
"ds_read_b128 a[16:19], v15 offset:33280\n"
"v_mfma_f32_16x16x32_bf16 v[80:83], a[4:7], a[36:39], v[80:83]\n"
"s_cbranch_vccnz PC27_%=\n"
"PB27_%=:\n"
"v_mfma_f32_16x16x32_bf16 v[84:87], a[4:7], a[68:71], v[36:39]\n"
"v_exp_f32 v65, v65\n"
"v_mfma_f32_16x16x32_bf16 v[84:87], a[0:3], a[64:67], v[84:87]\n"
"ds_read_b128 a[20:23], v16 offset:33280\n"
"v_cvt_pk_bf16_f32 v64, v64, v65\n"
"v_mfma_f32_16x16x32_bf16 v[88:91], a[0:3], a[96:99], v[40:43]\n"
"v_exp_f32 v66, v66\n"
"v_mfma_f32_16x16x32_bf16 v[88:91], a[4:7], a[100:103], v[88:91]\n"
"v_exp_f32 v67, v67\n"
"v_mfma_f32_16x16x32_bf16 v[92:95], a[4:7], a[132:135], v[44:47]\n"
"s_waitcnt lgkmcnt(2)\n"
"v_cvt_pk_bf16_f32 v66, v66, v67\n"
"v_accvgpr_write_b32 a240, v64\n"
"v_mfma_f32_16x16x32_bf16 v[92:95], a[0:3], a[128:131], v[92:95]\n"
"v_exp_f32 v68, v68\n"
"v_mfma_f32_16x16x32_bf16 v[80:83], a[8:11], a[40:43], v[80:83]\n"
"ds_read_b128 a[24:27], v17 offset:33280\n"
"v_accvgpr_write_b32 a241, v66\n"
"v_mfma_f32_16x16x32_bf16 v[80:83], a[12:15], a[44:47], v[80:83]\n"
"s_addk_i32 m0, 8256\n"
"v_exp_f32 v69, v69\n"
"v_mfma_f32_16x16x32_bf16 v[84:87], a[12:15], a[76:79], v[84:87]\n"
"s_add_u32 s81, s65, 65664\n"
"v_exp_f32 v70, v70\n"
"v_mfma_f32_16x16x32_bf16 v[84:87], a[8:11], a[72:75], v[84:87]\n"
"ds_read_b128 a[28:31], v18 offset:33280\n"
"v_cvt_pk_bf16_f32 v68, v68, v69\n"
"v_mfma_f32_16x16x32_bf16 v[88:91], a[8:11], a[104:107], v[88:91]\n"
"buffer_load_dwordx4 v25, s[48:51], s81 offen offset:0 nt lds\n"
"v_mfma_f32_16x16x32_bf16 v[88:91], a[12:15], a[108:111], v[88:91]\n"
"v_exp_f32 v71, v71\n"
"v_mfma_f32_16x16x32_bf16 v[92:95], a[12:15], a[140:143], v[92:95]\n"
"s_waitcnt lgkmcnt(2)\n"
"v_exp_f32 v72, v72\n"
"v_mfma_f32_16x16x32_bf16 v[92:95], a[8:11], a[136:139], v[92:95]\n"
"v_cvt_pk_bf16_f32 v70, v70, v71\n"
"v_accvgpr_write_b32 a244, v68\n"
"v_mfma_f32_16x16x32_bf16 v[80:83], a[16:19], a[48:51], v[80:83]\n"
"ds_read_b128 a[0:3], v19 offset:33024\n"
"v_accvgpr_write_b32 a245, v70\n"
"v_mfma_f32_16x16x32_bf16 v[80:83], a[20:23], a[52:55], v[80:83]\n"
"v_exp_f32 v73, v73\n"
"v_mfma_f32_16x16x32_bf16 v[84:87], a[20:23], a[84:87], v[84:87]\n"
"v_exp_f32 v74, v74\n"
"v_mfma_f32_16x16x32_bf16 v[84:87], a[16:19], a[80:83], v[84:87]\n"
"ds_read_b128 a[4:7], v20 offset:33024\n"
"v_cvt_pk_bf16_f32 v232, v72, v73\n"
"v_mfma_f32_16x16x32_bf16 v[88:91], a[16:19], a[112:115], v[88:91]\n"
"v_exp_f32 v75, v75\n"
"v_mfma_f32_16x16x32_bf16 v[88:91], a[20:23], a[116:119], v[88:91]\n"
"v_cvt_pk_bf16_f32 v233, v74, v75\n"
"v_mfma_f32_16x16x32_bf16 v[92:95], a[20:23], a[148:151], v[92:95]\n"
"s_waitcnt lgkmcnt(2)\n"
"ds_read_b128 a[8:11], v21 offset:33024\n"
"v_mfma_f32_16x16x32_bf16 v[92:95], a[16:19], a[144:147], v[92:95]\n"
"v_mfma_f32_16x16x32_bf16 v[80:83], a[24:27], a[56:59], v[80:83]\n"
"v_mfma_f32_16x16x32_bf16 v[80:83], a[28:31], a[60:63], v[80:83]\n"
"buffer_load_dwordx4 v26, s[48:51], s81 offen offset:1024 nt lds\n"
"v_mfma_f32_16x16x32_bf16 v[84:87], a[28:31], a[92:95], v[84:87]\n"
"ds_read_b128 a[12:15], v22 offset:33024\n"
"v_mfma_f32_16x16x32_bf16 v[84:87], a[24:27], a[88:91], v[84:87]\n"
"v_mfma_f32_16x16x32_bf16 v[88:91], a[24:27], a[120:123], v[88:91]\n"
"ds_read_b128 a[16:19], v19 offset:33280\n"
"v_mfma_f32_16x16x32_bf16 v[88:91], a[28:31], a[124:127], v[88:91]\n"
"v_mfma_f32_16x16x32_bf16 v[92:95], a[28:31], a[156:159], v[92:95]\n"
"s_waitcnt lgkmcnt(2)\n"
"v_exp_f32 v80, v80\n"
"v_mfma_f32_16x16x32_bf16 v[92:95], a[24:27], a[152:155], v[92:95]\n"
"ds_read_b128 a[20:23], v20 offset:33280\n"
"v_mfma_f32_16x16x32_bf16 v[48:51], a[0:3], a[32:35], v[32:35]\n"
"v_exp_f32 v81, v81\n"
"v_mfma_f32_16x16x32_bf16 v[48:51], a[4:7], a[36:39], v[48:51]\n"
"v_exp_f32 v82, v82\n"
"v_mfma_f32_16x16x32_bf16 v[52:55], a[4:7], a[68:71], v[36:39]\n"
"v_cvt_pk_bf16_f32 v218, v80, v81\n"
"v_mfma_f32_16x16x32_bf16 v[52:55], a[0:3], a[64:67], v[52:55]\n"
"v_exp_f32 v83, v83\n"
"v_mfma_f32_16x16x32_bf16 v[56:59], a[0:3], a[96:99], v[40:43]\n"
"s_waitcnt lgkmcnt(1)\n"
"v_exp_f32 v84, v84\n"
"v_mfma_f32_16x16x32_bf16 v[56:59], a[4:7], a[100:103], v[56:59]\n"
"s_memrealtime s[2:3]\n"
"ds_read_b128 a[24:27], v21 offset:33280\n"
"v_cvt_pk_bf16_f32 v219, v82, v83\n"
"v_mfma_f32_16x16x32_bf16 v[48:51], a[8:11], a[40:43], v[48:51]\n"
"v_exp_f32 v85, v85\n"
"v_mfma_f32_16x16x32_bf16 v[48:51], a[12:15], a[44:47], v[48:51]\n"
"buffer_load_dwordx4 v27, s[48:51], s81 offen offset:2048 nt lds\n"
"v_mfma_f32_16x16x32_bf16 v[52:55], a[12:15], a[76:79], v[52:55]\n"
"ds_read_b128 a[28:31], v22 offset:33280\n"
"v_cvt_pk_bf16_f32 v222, v84, v85\n"
"v_mfma_f32_16x16x32_bf16 v[52:55], a[8:11], a[72:75], v[52:55]\n"
"v_exp_f32 v86, v86\n"
"v_mfma_f32_16x16x32_bf16 v[56:59], a[8:11], a[104:107], v[56:59]\n"
"s_waitcnt lgkmcnt(2)\n"
"v_exp_f32 v87, v87\n"
"v_mfma_f32_16x16x32_bf16 v[56:59], a[12:15], a[108:111], v[56:59]\n"
"v_cvt_pk_bf16_f32 v223, v86, v87\n"
"v_mfma_f32_16x16x32_bf16 v[48:51], a[16:19], a[48:51], v[48:51]\n"
"v_exp_f32 v88, v88\n"
"v_mfma_f32_16x16x32_bf16 v[48:51], a[20:23], a[52:55], v[48:51]\n"
"v_exp_f32 v89, v89\n"
"v_mfma_f32_16x16x32_bf16 v[52:55], a[20:23], a[84:87], v[52:55]\n"
"s_waitcnt vmcnt(27) lgkmcnt(0)\n"
"v_cvt_pk_bf16_f32 v226, v88, v89\n"
"v_mfma_f32_16x16x32_bf16 v[52:55], a[16:19], a[80:83], v[52:55]\n"
"s_barrier\n"
"v_mfma_f32_16x16x32_bf16 v[56:59], a[16:19], a[112:115], v[56:59]\n"
"s_addk_i32 s79, 64\n"
"ds_read_b128 a[0:3], v15 offset:0\n"
"v_mfma_f32_16x16x32_bf16 v[56:59], a[20:23], a[116:119], v[56:59]\n"
"ds_read_b128 a[4:7], v16 offset:0\n"
"v_mfma_f32_16x16x32_bf16 a[160:163], a[176:179], a[228:231], a[160:163]\n"
"s_sub_u32 s0, s79, s2\n"
"v_exp_f32 v90, v90\n"
"v_mfma_f32_16x16x32_bf16 a[164:167], a[176:179], a[232:235], a[164:167]\n"
"ds_read_b128 a[8:11], v17 offset:0\n"
"v_mfma_f32_16x16x32_bf16 a[168:171], a[176:179], a[236:239], a[168:171]\n"
"s_sub_u32 s0, s0, s23\n"
"buffer_load_dwordx4 v28, s[48:51], s81 offen offset:3072 nt lds\n"
"v_mfma_f32_16x16x32_bf16 v[48:51], a[24:27], a[56:59], v[48:51]\n"
"v_exp_f32 v91, v91\n"
"v_mfma_f32_16x16x32_bf16 v[48:51], a[28:31], a[60:63], v[48:51]\n"
"s_max_i32 s0, s0, 0\n"
"ds_read_b128 a[12:15], v18 offset:0\n"
"v_cvt_pk_bf16_f32 v227, v90, v91\n"
"v_mfma_f32_16x16x32_bf16 v[52:55], a[28:31], a[92:95], v[52:55]\n"
"v_exp_f32 v92, v92\n"
"v_mfma_f32_16x16x32_bf16 v[52:55], a[24:27], a[88:91], v[52:55]\n"
"s_add_u32 s79, s2, s0\n"
"v_exp_f32 v93, v93\n"
"v_mfma_f32_16x16x32_bf16 v[56:59], a[24:27], a[120:123], v[56:59]\n"
"s_waitcnt lgkmcnt(2)\n"
"v_cvt_pk_bf16_f32 v230, v92, v93\n"
"v_mfma_f32_16x16x32_bf16 v[56:59], a[28:31], a[124:127], v[56:59]\n"
"s_lshr_b64 vcc, s[0:1], 2\n"
"v_exp_f32 v94, v94\n"
"v_mfma_f32_16x16x32_bf16 v[64:67], a[0:3], a[32:35], v[32:35]\n"
"ds_read_b128 a[16:19], v15 offset:256\n"
"v_mfma_f32_16x16x32_bf16 v[64:67], a[4:7], a[36:39], v[64:67]\n"
"s_cbranch_vccnz PC28_%=\n"
"PB28_%=:\n"
"v_mfma_f32_16x16x32_bf16 v[68:71], a[4:7], a[68:71], v[36:39]\n"
"v_exp_f32 v95, v95\n"
"v_mfma_f32_16x16x32_bf16 v[68:71], a[0:3], a[64:67], v[68:71]\n"
"s_add_u32 m0, s63, 33024\n"
"ds_read_b128 a[20:23], v16 offset:256\n"
"v_cvt_pk_bf16_f32 v231, v94, v95\n"
"v_mfma_f32_16x16x32_bf16 v[72:75], a[0:3], a[96:99], v[40:43]\n"
"s_add_u32 s81, s65, 131200\n"
"v_exp_f32 v48, v48\n"
"v_mfma_f32_16x16x32_bf16 v[72:75], a[4:7], a[100:103], v[72:75]\n"
"v_exp_f32 v49, v49\n"
"v_mfma_f32_16x16x32_bf16 v[76:79], a[4:7], a[132:135], v[44:47]\n"
"s_waitcnt lgkmcnt(2)\n"
"buffer_load_dwordx4 v25, s[48:51], s81 offen offset:0 nt lds\n"
"v_mfma_f32_16x16x32_bf16 v[76:79], a[0:3], a[128:131], v[76:79]\n"
"v_exp_f32 v50, v50\n"
"v_mfma_f32_16x16x32_bf16 v[64:67], a[8:11], a[40:43], v[64:67]\n"
"ds_read_b128 a[24:27], v17 offset:256\n"
"v_cvt_pk_bf16_f32 v48, v48, v49\n"
"v_mfma_f32_16x16x32_bf16 v[64:67], a[12:15], a[44:47], v[64:67]\n"
"v_exp_f32 v51, v51\n"
"v_mfma_f32_16x16x32_bf16 v[68:71], a[12:15], a[76:79], v[68:71]\n"
"v_exp_f32 v52, v52\n"
"v_mfma_f32_16x16x32_bf16 v[68:71], a[8:11], a[72:75], v[68:71]\n"
"ds_read_b128 a[28:31], v18 offset:256\n"
"v_cvt_pk_bf16_f32 v50, v50, v51\n"
"v_mfma_f32_16x16x32_bf16 v[72:75], a[8:11], a[104:107], v[72:75]\n"
"v_exp_f32 v53, v53\n"
"v_mfma_f32_16x16x32_bf16 v[72:75], a[12:15], a[108:111], v[72:75]\n"
"v_exp_f32 v54, v54\n"
"v_mfma_f32_16x16x32_bf16 v[76:79], a[12:15], a[140:143], v[76:79]\n"
"s_waitcnt lgkmcnt(2)\n"
"v_accvgpr_write_b32 a242, v48\n"
"v_accvgpr_write_b32 a243, v50\n"
"v_mfma_f32_16x16x32_bf16 v[76:79], a[8:11], a[136:139], v[76:79]\n"
"v_exp_f32 v55, v55\n"
"v_mfma_f32_16x16x32_bf16 v[64:67], a[16:19], a[48:51], v[64:67]\n"
"ds_read_b128 a[0:3], v19 offset:0\n"
"v_cvt_pk_bf16_f32 v52, v52, v53\n"
"v_mfma_f32_16x16x32_bf16 v[64:67], a[20:23], a[52:55], v[64:67]\n"
"v_exp_f32 v56, v56\n"
"v_mfma_f32_16x16x32_bf16 v[68:71], a[20:23], a[84:87], v[68:71]\n"
"v_exp_f32 v57, v57\n"
"v_mfma_f32_16x16x32_bf16 v[68:71], a[16:19], a[80:83], v[68:71]\n"
"ds_read_b128 a[4:7], v20 offset:0\n"
"v_cvt_pk_bf16_f32 v54, v54, v55\n"
"v_mfma_f32_16x16x32_bf16 v[72:75], a[16:19], a[112:115], v[72:75]\n"
"buffer_load_dwordx4 v26, s[48:51], s81 offen offset:1024 nt lds\n"
"v_mfma_f32_16x16x32_bf16 v[72:75], a[20:23], a[116:119], v[72:75]\n"
"v_exp_f32 v58, v58\n"
"v_mfma_f32_16x16x32_bf16 v[76:79], a[20:23], a[148:151], v[76:79]\n"
"s_waitcnt lgkmcnt(2)\n"
"ds_read_b128 a[8:11], v21 offset:0\n"
"v_accvgpr_write_b32 a246, v52\n"
"v_mfma_f32_16x16x32_bf16 v[76:79], a[16:19], a[144:147], v[76:79]\n"
"v_exp_f32 v59, v59\n"
"v_mfma_f32_16x16x32_bf16 v[64:67], a[24:27], a[56:59], v[64:67]\n"
"v_accvgpr_write_b32 a247, v54\n"
"v_cvt_pk_bf16_f32 v234, v56, v57\n"
"v_mfma_f32_16x16x32_bf16 v[64:67], a[28:31], a[60:63], v[64:67]\n"
"ds_read_b128 a[12:15], v22 offset:0\n"
"v_cvt_pk_bf16_f32 v235, v58, v59\n"
"v_mfma_f32_16x16x32_bf16 v[68:71], a[28:31], a[92:95], v[68:71]\n"
"v_mfma_f32_16x16x32_bf16 v[68:71], a[24:27], a[88:91], v[68:71]\n"
"v_mfma_f32_16x16x32_bf16 v[72:75], a[24:27], a[120:123], v[72:75]\n"
"ds_read_b128 a[16:19], v19 offset:256\n"
"v_mfma_f32_16x16x32_bf16 v[72:75], a[28:31], a[124:127], v[72:75]\n"
"v_mfma_f32_16x16x32_bf16 v[76:79], a[28:31], a[156:159], v[76:79]\n"
"s_waitcnt lgkmcnt(2)\n"
"v_exp_f32 v64, v64\n"
"v_mfma_f32_16x16x32_bf16 v[76:79], a[24:27], a[152:155], v[76:79]\n"
"ds_read_b128 a[20:23], v20 offset:256\n"
"v_mfma_f32_16x16x32_bf16 v[80:83], a[0:3], a[32:35], v[32:35]\n"
"v_exp_f32 v65, v65\n"
"v_mfma_f32_16x16x32_bf16 v[80:83], a[4:7], a[36:39], v[80:83]\n"
"buffer_load_dwordx4 v27, s[48:51], s81 offen offset:2048 nt lds\n"
"v_mfma_f32_16x16x32_bf16 v[84:87], a[4:7], a[68:71], v[36:39]\n"
"v_exp_f32 v66, v66\n"
"v_mfma_f32_16x16x32_bf16 v[84:87], a[0:3], a[64:67], v[84:87]\n"
"v_exp_f32 v67, v67\n"
"v_mfma_f32_16x16x32_bf16 v[88:91], a[0:3], a[96:99], v[40:43]\n"
"s_waitcnt lgkmcnt(1)\n"
"v_cvt_pk_bf16_f32 v236, v64, v65\n"
"v_cvt_pk_bf16_f32 v237, v66, v67\n"
"v_mfma_f32_16x16x32_bf16 v[88:91], a[4:7], a[100:103], v[88:91]\n"
"s_memrealtime s[2:3]\n"
"v_exp_f32 v68, v68\n"
"v_mfma_f32_16x16x32_bf16 v[80:83], a[8:11], a[40:43], v[80:83]\n"
"ds_read_b128 a[24:27], v21 offset:256\n"
"v_mfma_f32_16x16x32_bf16 v[80:83], a[12:15], a[44:47], v[80:83]\n"
"v_exp_f32 v69, v69\n"
"v_mfma_f32_16x16x32_bf16 v[84:87], a[12:15], a[76:79], v[84:87]\n"
"v_exp_f32 v70, v70\n"
"v_mfma_f32_16x16x32_bf16 v[84:87], a[8:11], a[72:75], v[84:87]\n"
"ds_read_b128 a[28:31], v22 offset:256\n"
"v_cvt_pk_bf16_f32 v240, v68, v69\n"
"v_mfma_f32_16x16x32_bf16 v[88:91], a[8:11], a[104:107], v[88:91]\n"
"s_waitcnt lgkmcnt(2)\n"
"v_exp_f32 v71, v71\n"
"v_mfma_f32_16x16x32_bf16 v[88:91], a[12:15], a[108:111], v[88:91]\n"
"v_exp_f32 v72, v72\n"
"v_mfma_f32_16x16x32_bf16 v[80:83], a[16:19], a[48:51], v[80:83]\n"
"v_cvt_pk_bf16_f32 v241, v70, v71\n"
"v_mfma_f32_16x16x32_bf16 v[80:83], a[20:23], a[52:55], v[80:83]\n"
"v_exp_f32 v73, v73\n"
"v_mfma_f32_16x16x32_bf16 v[84:87], a[20:23], a[84:87], v[84:87]\n"
"buffer_load_dwordx4 v28, s[48:51], s81 offen offset:3072 nt lds\n"
"v_mfma_f32_16x16x32_bf16 v[84:87], a[16:19], a[80:83], v[84:87]\n"
"s_waitcnt vmcnt(28) lgkmcnt(0)\n"
"v_exp_f32 v74, v74\n"
"v_mfma_f32_16x16x32_bf16 v[88:91], a[16:19], a[112:115], v[88:91]\n"
"s_barrier\n"
"v_mfma_f32_16x16x32_bf16 v[88:91], a[20:23], a[116:119], v[88:91]\n"
"s_addk_i32 s79, 65\n"
"ds_read_b128 a[0:3], v15 offset:8256\n"
"v_cvt_pk_bf16_f32 v244, v72, v73\n"
"v_mfma_f32_16x16x32_bf16 a[160:163], a[176:179], v[216:219], a[160:163]\n"
"ds_read_b128 a[4:7], v16 offset:8256\n"
"v_mfma_f32_16x16x32_bf16 a[164:167], a[176:179], v[220:223], a[164:167]\n"
"s_sub_u32 s0, s79, s2\n"
"v_exp_f32 v75, v75\n"
"v_mfma_f32_16x16x32_bf16 a[168:171], a[176:179], v[224:227], a[168:171]\n"
"ds_read_b128 a[8:11], v17 offset:8256\n"
"v_cvt_pk_bf16_f32 v245, v74, v75\n"
"v_mfma_f32_16x16x32_bf16 a[172:175], a[176:179], v[228:231], a[172:175]\n"
"s_sub_u32 s0, s0, s23\n"
"v_exp_f32 v76, v76\n"
"v_mfma_f32_16x16x32_bf16 v[80:83], a[24:27], a[56:59], v[80:83]\n"
"v_exp_f32 v77, v77\n"
"v_mfma_f32_16x16x32_bf16 v[80:83], a[28:31], a[60:63], v[80:83]\n"
"s_sub_u32 s79, s77, s2\n"
"ds_read_b128 a[12:15], v18 offset:8256\n"
"v_cvt_pk_bf16_f32 v248, v76, v77\n"
"v_mfma_f32_16x16x32_bf16 v[84:87], a[28:31], a[92:95], v[84:87]\n"
"s_add_u32 m0, s63, 0\n"
"v_exp_f32 v78, v78\n"
"v_mfma_f32_16x16x32_bf16 v[84:87], a[24:27], a[88:91], v[84:87]\n"
"s_add_u32 s81, s65, 196736\n"
"v_exp_f32 v80, v80\n"
"v_mfma_f32_16x16x32_bf16 v[88:91], a[24:27], a[120:123], v[88:91]\n"
"s_waitcnt lgkmcnt(2)\n"
"s_addk_i32 s79, 2099\n"
"v_mfma_f32_16x16x32_bf16 v[88:91], a[28:31], a[124:127], v[88:91]\n"
"buffer_load_dwordx4 v25, s[48:51], s81 offen offset:0 nt lds\n"
"v_mfma_f32_16x16x32_bf16 v[48:51], a[0:3], a[32:35], v[32:35]\n"
"s_max_i32 s0, s0, s79\n"
"PG8_%=:\n"
"s_cbranch_execz PG12_%=\n"
"ds_read_b128 a[16:19], v15 offset:8512\n"
"v_mfma_f32_16x16x32_bf16 v[48:51], a[4:7], a[36:39], v[48:51]\n"
"v_exp_f32 v81, v81\n"
"v_mfma_f32_16x16x32_bf16 v[52:55], a[4:7], a[68:71], v[36:39]\n"
"s_min_i32 s0, s0, 40\n"
"v_exp_f32 v82, v82\n"
"v_mfma_f32_16x16x32_bf16 v[52:55], a[0:3], a[64:67], v[52:55]\n"
"ds_read_b128 a[20:23], v16 offset:8512\n"
"v_cvt_pk_bf16_f32 v80, v80, v81\n"
"v_mfma_f32_16x16x32_bf16 v[56:59], a[0:3], a[96:99], v[40:43]\n"
"s_max_i32 s0, s0, 0\n"
"v_exp_f32 v83, v83\n"
"v_mfma_f32_16x16x32_bf16 v[56:59], a[4:7], a[100:103], v[56:59]\n"
"v_exp_f32 v84, v84\n"
"v_mfma_f32_16x16x32_bf16 v[60:63], a[4:7], a[132:135], v[44:47]\n"
"s_waitcnt lgkmcnt(2)\n"
"v_cvt_pk_bf16_f32 v82, v82, v83\n"
"v_accvgpr_write_b32 a248, v80\n"
"v_mfma_f32_16x16x32_bf16 v[60:63], a[0:3], a[128:131], v[60:63]\n"
"s_add_u32 s79, s2, s0\n"
"v_exp_f32 v85, v85\n"
"v_mfma_f32_16x16x32_bf16 v[48:51], a[8:11], a[40:43], v[48:51]\n"
"ds_read_b128 a[24:27], v17 offset:8512\n"
"v_accvgpr_write_b32 a249, v82\n"
"v_mfma_f32_16x16x32_bf16 v[48:51], a[12:15], a[44:47], v[48:51]\n"
"s_lshr_b64 vcc, s[0:1], 2\n"
"v_exp_f32 v86, v86\n"
"v_mfma_f32_16x16x32_bf16 v[52:55], a[12:15], a[76:79], v[52:55]\n"
"v_exp_f32 v87, v87\n"
"v_mfma_f32_16x16x32_bf16 v[52:55], a[8:11], a[72:75], v[52:55]\n"
"s_cbranch_vccnz PC29_%=\n"
"PB29_%=:\n"
"v_cvt_pk_bf16_f32 v84, v84, v85\n"
"v_mfma_f32_16x16x32_bf16 v[56:59], a[8:11], a[104:107], v[56:59]\n"
"ds_read_b128 a[28:31], v18 offset:8512\n"
"v_cvt_pk_bf16_f32 v86, v86, v87\n"
"v_mfma_f32_16x16x32_bf16 v[56:59], a[12:15], a[108:111], v[56:59]\n"
"buffer_load_dwordx4 v26, s[48:51], s81 offen offset:1024 nt lds\n"
"v_mfma_f32_16x16x32_bf16 v[60:63], a[12:15], a[140:143], v[60:63]\n"
"s_waitcnt lgkmcnt(2)\n"
"v_exp_f32 v88, v88\n"
"v_mfma_f32_16x16x32_bf16 v[60:63], a[8:11], a[136:139], v[60:63]\n"
"v_exp_f32 v89, v89\n"
"v_mfma_f32_16x16x32_bf16 v[48:51], a[16:19], a[48:51], v[48:51]\n"
"ds_read_b128 a[0:3], v19 offset:8256\n"
"v_accvgpr_write_b32 a252, v84\n"
"v_mfma_f32_16x16x32_bf16 v[48:51], a[20:23], a[52:55], v[48:51]\n"
"v_exp_f32 v90, v90\n"
"v_mfma_f32_16x16x32_bf16 v[52:55], a[20:23], a[84:87], v[52:55]\n"
"v_exp_f32 v91, v91\n"
"v_mfma_f32_16x16x32_bf16 v[52:55], a[16:19], a[80:83], v[52:55]\n"
"ds_read_b128 a[4:7], v20 offset:8256\n"
"v_accvgpr_write_b32 a253, v86\n"
"v_mfma_f32_16x16x32_bf16 v[56:59], a[16:19], a[112:115], v[56:59]\n"
"v_exp_f32 v79, v79\n"
"v_mfma_f32_16x16x32_bf16 v[56:59], a[20:23], a[116:119], v[56:59]\n"
"v_cvt_pk_bf16_f32 v252, v88, v89\n"
"v_cvt_pk_bf16_f32 v253, v90, v91\n"
"v_mfma_f32_16x16x32_bf16 v[60:63], a[20:23], a[148:151], v[60:63]\n"
"s_waitcnt lgkmcnt(2)\n"
"ds_read_b128 a[8:11], v21 offset:8256\n"
"v_cvt_pk_bf16_f32 v249, v78, v79\n"
"v_mfma_f32_16x16x32_bf16 v[60:63], a[16:19], a[144:147], v[60:63]\n"
"v_mfma_f32_16x16x32_bf16 v[48:51], a[24:27], a[56:59], v[48:51]\n"
"v_mfma_f32_16x16x32_bf16 v[48:51], a[28:31], a[60:63], v[48:51]\n"
"ds_read_b128 a[12:15], v22 offset:8256\n"
"v_mfma_f32_16x16x32_bf16 v[52:55], a[28:31], a[92:95], v[52:55]\n"
"buffer_load_dwordx4 v27, s[48:51], s81 offen offset:2048 nt lds\n"
"v_mfma_f32_16x16x32_bf16 v[52:55], a[24:27], a[88:91], v[52:55]\n"
"v_mfma_f32_16x16x32_bf16 v[56:59], a[24:27], a[120:123], v[56:59]\n"
"ds_read_b128 a[16:19], v19 offset:8512\n"
"v_mfma_f32_16x16x32_bf16 v[56:59], a[28:31], a[124:127], v[56:59]\n"
"v_mfma_f32_16x16x32_bf16 v[60:63], a[28:31], a[156:159], v[60:63]\n"
"s_waitcnt lgkmcnt(2)\n"
"v_exp_f32 v48, v48\n"
"v_mfma_f32_16x16x32_bf16 v[60:63], a[24:27], a[152:155], v[60:63]\n"
"ds_read_b128 a[20:23], v20 offset:8512\n"
"v_mfma_f32_16x16x32_bf16 v[64:67], a[0:3], a[32:35], v[32:35]\n"
"v_exp_f32 v49, v49\n"
"v_mfma_f32_16x16x32_bf16 v[64:67], a[4:7], a[36:39], v[64:67]\n"
"v_exp_f32 v50, v50\n"
"v_mfma_f32_16x16x32_bf16 v[68:71], a[4:7], a[68:71], v[36:39]\n"
"v_cvt_pk_bf16_f32 v238, v48, v49\n"
"v_mfma_f32_16x16x32_bf16 v[68:71], a[0:3], a[64:67], v[68:71]\n"
"v_exp_f32 v51, v51\n"
"v_mfma_f32_16x16x32_bf16 v[72:75], a[0:3], a[96:99], v[40:43]\n"
"s_waitcnt lgkmcnt(1)\n"
"v_exp_f32 v52, v52\n"
"v_mfma_f32_16x16x32_bf16 v[72:75], a[4:7], a[100:103], v[72:75]\n"
"s_memrealtime s[2:3]\n"
"ds_read_b128 a[24:27], v21 offset:8512\n"
"v_cvt_pk_bf16_f32 v239, v50, v51\n"
"v_mfma_f32_16x16x32_bf16 v[64:67], a[8:11], a[40:43], v[64:67]\n"
"v_exp_f32 v53, v53\n"
"v_mfma_f32_16x16x32_bf16 v[64:67], a[12:15], a[44:47], v[64:67]\n"
"buffer_load_dwordx4 v28, s[48:51], s81 offen offset:3072 nt lds\n"
"v_mfma_f32_16x16x32_bf16 v[68:71], a[12:15], a[76:79], v[68:71]\n"
"ds_read_b128 a[28:31], v22 offset:8512\n"
"v_cvt_pk_bf16_f32 v242, v52, v53\n"
"v_mfma_f32_16x16x32_bf16 v[68:71], a[8:11], a[72:75], v[68:71]\n"
"v_exp_f32 v54, v54\n"
"v_mfma_f32_16x16x32_bf16 v[72:75], a[8:11], a[104:107], v[72:75]\n"
"s_waitcnt lgkmcnt(2)\n"
"v_exp_f32 v55, v55\n"
"v_mfma_f32_16x16x32_bf16 v[72:75], a[12:15], a[108:111], v[72:75]\n"
"v_cvt_pk_bf16_f32 v243, v54, v55\n"
"v_mfma_f32_16x16x32_bf16 v[64:67], a[16:19], a[48:51], v[64:67]\n"
"v_exp_f32 v60, v60\n"
"v_mfma_f32_16x16x32_bf16 v[64:67], a[20:23], a[52:55], v[64:67]\n"
"v_exp_f32 v61, v61\n"
"v_mfma_f32_16x16x32_bf16 v[68:71], a[20:23], a[84:87], v[68:71]\n"
"s_waitcnt vmcnt(28) lgkmcnt(0)\n"
"v_cvt_pk_bf16_f32 v250, v60, v61\n"
"v_mfma_f32_16x16x32_bf16 v[68:71], a[16:19], a[80:83], v[68:71]\n"
"s_barrier\n"
"v_mfma_f32_16x16x32_bf16 v[72:75], a[16:19], a[112:115], v[72:75]\n"
"s_addk_i32 s79, 67\n"
"ds_read_b64_tr_b16 a[0:1], v2 offset:16512\n"
"v_mfma_f32_16x16x32_bf16 v[72:75], a[20:23], a[116:119], v[72:75]\n"
"ds_read_b64_tr_b16 a[2:3], v2 offset:18560\n"
"v_mfma_f32_16x16x32_bf16 a[160:163], a[176:179], a[240:243], a[160:163]\n"
"s_addk_i32 m0, 8256\n"
"ds_read_b64_tr_b16 a[4:5], v6 offset:16512\n"
"s_add_u32 s81, s65, 256\n"
"v_mfma_f32_16x16x32_bf16 a[164:167], a[176:179], a[244:247], a[164:167]\n"
"ds_read_b64_tr_b16 a[6:7], v6 offset:18560\n"
"s_sub_u32 s0, s79, s2\n"
"v_mfma_f32_16x16x32_bf16 a[168:171], a[176:179], v[232:235], a[168:171]\n"
"ds_read_b64_tr_b16 a[8:9], v2 offset:20608\n"
"v_mfma_f32_16x16x32_bf16 v[64:67], a[24:27], a[56:59], v[64:67]\n"
"s_sub_u32 s0, s0, s23\n"
"buffer_load_dwordx4 v25, s[48:51], s81 offen offset:0 nt lds\n"
"v_mfma_f32_16x16x32_bf16 v[64:67], a[28:31], a[60:63], v[64:67]\n"
"ds_read_b64_tr_b16 a[10:11], v2 offset:22656\n"
"v_mfma_f32_16x16x32_bf16 v[68:71], a[28:31], a[92:95], v[68:71]\n"
"s_max_i32 s0, s0, 0\n"
"ds_read_b64_tr_b16 a[12:13], v6 offset:20608\n"
"v_mfma_f32_16x16x32_bf16 v[68:71], a[24:27], a[88:91], v[68:71]\n"
"ds_read_b64_tr_b16 a[14:15], v6 offset:22656\n"
"v_mfma_f32_16x16x32_bf16 v[72:75], a[24:27], a[120:123], v[72:75]\n"
"s_waitcnt lgkmcnt(4)\n"
"ds_read_b64_tr_b16 a[16:17], v3 offset:16512\n"
"s_add_u32 s79, s2, s0\n"
"v_mfma_f32_16x16x32_bf16 v[72:75], a[28:31], a[124:127], v[72:75]\n"
"ds_read_b64_tr_b16 a[18:19], v3 offset:18560\n"
"v_mfma_f32_16x16x32_bf16 v[32:35], a[0:3], v[108:111], 0\n"
"s_lshr_b64 vcc, s[0:1], 2\n"
"ds_read_b64_tr_b16 a[20:21], v7 offset:16512\n"
"v_mfma_f32_16x16x32_bf16 v[32:35], a[4:7], a[180:183], v[32:35]\n"
"ds_read_b64_tr_b16 a[22:23], v7 offset:18560\n"
"v_mfma_f32_16x16x32_bf16 v[36:39], a[4:7], a[184:187], 0\n"
"s_cbranch_vccnz PC30_%=\n"
"PB30_%=:\n"
"v_mfma_f32_16x16x32_bf16 v[36:39], a[0:3], v[112:115], v[36:39]\n"
"ds_read_b64_tr_b16 a[24:25], v3 offset:20608\n"
"v_mfma_f32_16x16x32_bf16 v[44:47], a[0:3], v[120:123], 0\n"
"ds_read_b64_tr_b16 a[26:27], v3 offset:22656\n"
"v_mfma_f32_16x16x32_bf16 v[40:43], a[0:3], v[116:119], 0\n"
"s_waitcnt lgkmcnt(5)\n"
"ds_read_b64_tr_b16 a[28:29], v7 offset:20608\n"
"v_mfma_f32_16x16x32_bf16 v[40:43], a[4:7], v[124:127], v[40:43]\n"
"ds_read_b64_tr_b16 a[30:31], v7 offset:22656\n"
"v_mfma_f32_16x16x32_bf16 v[32:35], a[8:11], v[128:131], v[32:35]\n"
"buffer_load_dwordx4 v26, s[48:51], s81 offen offset:1024 nt lds\n"
"v_mfma_f32_16x16x32_bf16 v[32:35], a[12:15], a[188:191], v[32:35]\n"
"ds_read_b64_tr_b16 a[0:1], v4 offset:16512\n"
"v_mfma_f32_16x16x32_bf16 v[36:39], a[12:15], a[192:195], v[36:39]\n"
"ds_read_b64_tr_b16 a[2:3], v4 offset:18560\n"
"v_mfma_f32_16x16x32_bf16 v[36:39], a[8:11], v[132:135], v[36:39]\n"
"ds_read_b64_tr_b16 a[4:5], v8 offset:16512\n"
"v_mfma_f32_16x16x32_bf16 v[44:47], a[8:11], v[140:143], v[44:47]\n"
"ds_read_b64_tr_b16 a[6:7], v8 offset:18560\n"
"v_mfma_f32_16x16x32_bf16 v[40:43], a[8:11], v[136:139], v[40:43]\n"
"s_waitcnt lgkmcnt(5)\n"
"ds_read_b64_tr_b16 a[8:9], v4 offset:20608\n"
"v_mfma_f32_16x16x32_bf16 v[40:43], a[12:15], a[196:199], v[40:43]\n"
"ds_read_b64_tr_b16 a[10:11], v4 offset:22656\n"
"v_mfma_f32_16x16x32_bf16 v[80:83], a[16:19], v[108:111], 0\n"
"ds_read_b64_tr_b16 a[12:13], v8 offset:20608\n"
"v_mfma_f32_16x16x32_bf16 v[80:83], a[20:23], a[180:183], v[80:83]\n"
"ds_read_b64_tr_b16 a[14:15], v8 offset:22656\n"
"v_mfma_f32_16x16x32_bf16 v[84:87], a[20:23], a[184:187], 0\n"
"v_exp_f32 v62, v62\n"
"v_mfma_f32_16x16x32_bf16 v[84:87], a[16:19], v[112:115], v[84:87]\n"
"v_exp_f32 v63, v63\n"
"v_mfma_f32_16x16x32_bf16 v[92:95], a[16:19], v[120:123], 0\n"
"v_cvt_pk_bf16_f32 v251, v62, v63\n"
"v_mfma_f32_16x16x32_bf16 v[88:91], a[16:19], v[116:119], 0\n"
"s_waitcnt lgkmcnt(3)\n"
"v_exp_f32 v56, v56\n"
"v_mfma_f32_16x16x32_bf16 v[88:91], a[20:23], v[124:127], v[88:91]\n"
"buffer_load_dwordx4 v27, s[48:51], s81 offen offset:2048 nt lds\n"
"v_mfma_f32_16x16x32_bf16 v[80:83], a[24:27], v[128:131], v[80:83]\n"
"v_exp_f32 v57, v57\n"
"v_mfma_f32_16x16x32_bf16 v[80:83], a[28:31], a[188:191], v[80:83]\n"
"v_exp_f32 v58, v58\n"
"v_mfma_f32_16x16x32_bf16 v[84:87], a[28:31], a[192:195], v[84:87]\n"
"ds_read_b64_tr_b16 a[16:17], v5 offset:16512\n"
"v_cvt_pk_bf16_f32 v246, v56, v57\n"
"v_mfma_f32_16x16x32_bf16 v[84:87], a[24:27], v[132:135], v[84:87]\n"
"v_exp_f32 v59, v59\n"
"v_mfma_f32_16x16x32_bf16 v[92:95], a[24:27], v[140:143], v[92:95]\n"
"ds_read_b64_tr_b16 a[18:19], v5 offset:18560\n"
"v_cvt_pk_bf16_f32 v247, v58, v59\n"
"v_mfma_f32_16x16x32_bf16 v[88:91], a[24:27], v[136:139], v[88:91]\n"
"ds_read_b64_tr_b16 a[20:21], v9 offset:16512\n"
"v_mfma_f32_16x16x32_bf16 v[88:91], a[28:31], a[196:199], v[88:91]\n"
"ds_read_b64_tr_b16 a[22:23], v9 offset:18560\n"
"v_mfma_f32_16x16x32_bf16 v[48:51], a[0:3], v[108:111], 0\n"
"v_exp_f32 v64, v64\n"
"v_mfma_f32_16x16x32_bf16 v[48:51], a[4:7], a[180:183], v[48:51]\n"
"v_exp_f32 v65, v65\n"
"v_mfma_f32_16x16x32_bf16 v[52:55], a[4:7], a[184:187], 0\n"
"ds_read_b64_tr_b16 a[24:25], v5 offset:20608\n"
"v_cvt_pk_bf16_f32 v64, v64, v65\n"
"v_mfma_f32_16x16x32_bf16 v[52:55], a[0:3], v[112:115], v[52:55]\n"
"v_exp_f32 v66, v66\n"
"v_mfma_f32_16x16x32_bf16 v[60:63], a[0:3], v[120:123], 0\n"
"v_exp_f32 v67, v67\n"
"v_mfma_f32_16x16x32_bf16 v[56:59], a[0:3], v[116:119], 0\n"
"s_waitcnt lgkmcnt(2)\n"
"ds_read_b64_tr_b16 a[26:27], v5 offset:22656\n"
"v_cvt_pk_bf16_f32 v66, v66, v67\n"
"v_mfma_f32_16x16x32_bf16 v[56:59], a[4:7], v[124:127], v[56:59]\n"
"buffer_load_dwordx4 v28, s[48:51], s81 offen offset:3072 nt lds\n"
"v_mfma_f32_16x16x32_bf16 v[48:51], a[8:11], v[128:131], v[48:51]\n"
"ds_read_b64_tr_b16 a[28:29], v9 offset:20608\n"
"v_accvgpr_write_b32 a250, v64\n"
"v_mfma_f32_16x16x32_bf16 v[48:51], a[12:15], a[188:191], v[48:51]\n"
"ds_read_b64_tr_b16 a[30:31], v9 offset:22656\n"
"v_accvgpr_write_b32 a251, v66\n"
"v_mfma_f32_16x16x32_bf16 v[52:55], a[12:15], a[192:195], v[52:55]\n"
"v_exp_f32 v68, v68\n"
"v_mfma_f32_16x16x32_bf16 v[52:55], a[8:11], v[132:135], v[52:55]\n"
"v_exp_f32 v69, v69\n"
"v_mfma_f32_16x16x32_bf16 v[60:63], a[8:11], v[140:143], v[60:63]\n"
"v_cvt_pk_bf16_f32 v68, v68, v69\n"
"v_accvgpr_write_b32 a254, v68\n"
"v_mfma_f32_16x16x32_bf16 v[56:59], a[8:11], v[136:139], v[56:59]\n"
"s_waitcnt lgkmcnt(2)\n"
"v_exp_f32 v70, v70\n"
"v_mfma_f32_16x16x32_bf16 v[56:59], a[12:15], a[196:199], v[56:59]\n"
"v_exp_f32 v71, v71\n"
"v_mfma_f32_16x16x32_bf16 v[64:67], a[16:19], v[108:111], 0\n"
"s_memrealtime s[2:3]\n"
"v_cvt_pk_bf16_f32 v70, v70, v71\n"
"v_accvgpr_write_b32 a255, v70\n"
"v_mfma_f32_16x16x32_bf16 v[64:67], a[20:23], a[180:183], v[64:67]\n"
"v_exp_f32 v72, v72\n"
"v_mfma_f32_16x16x32_bf16 v[68:71], a[20:23], a[184:187], 0\n"
"v_exp_f32 v73, v73\n"
"v_mfma_f32_16x16x32_bf16 v[68:71], a[16:19], v[112:115], v[68:71]\n"
"v_cvt_pk_bf16_f32 v254, v72, v73\n"
"v_mfma_f32_16x16x32_bf16 v[76:79], a[16:19], v[120:123], 0\n"
"v_exp_f32 v74, v74\n"
"v_exp_f32 v75, v75\n"
"s_nop 0\n"
"v_cvt_pk_bf16_f32 v255, v74, v75\n"
"v_mfma_f32_16x16x32_bf16 v[72:75], a[16:19], v[116:119], 0\n"
"s_waitcnt lgkmcnt(0)\n"
"v_mfma_f32_16x16x32_bf16 v[72:75], a[20:23], v[124:127], v[72:75]\n"
"v_mfma_f32_16x16x32_bf16 v[64:67], a[24:27], v[128:131], v[64:67]\n"
"v_mfma_f32_16x16x32_bf16 v[64:67], a[28:31], a[188:191], v[64:67]\n"
"s_waitcnt vmcnt(28) lgkmcnt(0)\n"
"v_mfma_f32_16x16x32_bf16 v[68:71], a[28:31], a[192:195], v[68:71]\n"
"s_barrier\n"
"v_mfma_f32_16x16x32_bf16 v[68:71], a[24:27], v[132:135], v[68:71]\n"
"s_addk_i32 m0, 8256\n"
"ds_read_b64_tr_b16 a[0:1], v2 offset:24768\n"
"s_add_u32 s81, s65, 65792\n"
"v_mfma_f32_16x16x32_bf16 v[76:79], a[24:27], v[140:143], v[76:79]\n"
"s_addk_i32 s79, 71\n"
"ds_read_b64_tr_b16 a[2:3], v2 offset:26816\n"
"v_mfma_f32_16x16x32_bf16 v[72:75], a[24:27], v[136:139], v[72:75]\n"
"ds_read_b64_tr_b16 a[4:5], v6 offset:24768\n"
"v_mfma_f32_16x16x32_bf16 v[72:75], a[28:31], a[196:199], v[72:75]\n"
"s_sub_u32 s0, s79, s2\n"
"ds_read_b64_tr_b16 a[6:7], v6 offset:26816\n"
"v_mfma_f32_16x16x32_bf16 a[160:163], a[176:179], v[236:239], a[160:163]\n"
"buffer_load_dwordx4 v25, s[48:51], s81 offen offset:0 nt lds\n"
"v_mfma_f32_16x16x32_bf16 a[164:167], a[176:179], v[240:243], a[164:167]\n"
"s_sub_u32 s0, s0, s23\n"
"ds_read_b64_tr_b16 a[8:9], v2 offset:28864\n"
"v_mfma_f32_16x16x32_bf16 a[168:171], a[176:179], v[244:247], a[168:171]\n"
"ds_read_b64_tr_b16 a[10:11], v2 offset:30912\n"
"v_mfma_f32_16x16x32_bf16 a[172:175], a[176:179], v[248:251], a[172:175]\n"
"s_max_i32 s0, s0, 0\n"
"ds_read_b64_tr_b16 a[12:13], v6 offset:28864\n"
"v_mfma_f32_16x16x32_bf16 a[160:163], a[176:179], a[248:251], a[160:163]\n"
"ds_read_b64_tr_b16 a[14:15], v6 offset:30912\n"
"v_mfma_f32_16x16x32_bf16 a[164:167], a[176:179], a[252:255], a[164:167]\n"
"s_waitcnt lgkmcnt(4)\n"
"ds_read_b64_tr_b16 a[16:17], v3 offset:24768\n"
"s_add_u32 s79, s2, s0\n"
"v_mfma_f32_16x16x32_bf16 a[168:171], a[176:179], v[252:255], a[168:171]\n"
"ds_read_b64_tr_b16 a[18:19], v3 offset:26816\n"
"v_accvgpr_read_b32 v99, a172\n"
"v_mfma_f32_16x16x32_bf16 v[32:35], a[0:3], v[144:147], v[32:35]\n"
"ds_read_b64_tr_b16 a[20:21], v7 offset:24768\n"
"v_accvgpr_read_b32 v96, a160\n"
"v_mfma_f32_16x16x32_bf16 v[32:35], a[4:7], a[200:203], v[32:35]\n"
"ds_read_b64_tr_b16 a[22:23], v7 offset:26816\n"
"v_accvgpr_read_b32 v97, a164\n"
"v_mfma_f32_16x16x32_bf16 v[36:39], a[4:7], a[204:207], v[36:39]\n"
"ds_read_b64_tr_b16 a[24:25], v3 offset:28864\n"
"v_accvgpr_read_b32 v98, a168\n"
"v_mfma_f32_16x16x32_bf16 v[36:39], a[0:3], v[148:151], v[36:39]\n"
"ds_read_b64_tr_b16 a[26:27], v3 offset:30912\n"
"v_min3_f32 v100, v96, v97, v98\n"
"v_mfma_f32_16x16x32_bf16 v[44:47], a[0:3], v[156:159], v[44:47]\n"
"ds_read_b64_tr_b16 a[28:29], v7 offset:28864\n"
"v_min_f32 v100, v100, v99\n"
"v_mfma_f32_16x16x32_bf16 v[40:43], a[0:3], v[152:155], v[40:43]\n"
"s_waitcnt lgkmcnt(6)\n"
"buffer_load_dwordx4 v26, s[48:51], s81 offen offset:1024 nt lds\n"
"v_mfma_f32_16x16x32_bf16 v[40:43], a[4:7], v[160:163], v[40:43]\n"
"ds_read_b64_tr_b16 a[30:31], v7 offset:30912\n"
"v_max3_f32 v101, v96, v97, v98\n"
"v_mfma_f32_16x16x32_bf16 v[32:35], a[8:11], v[164:167], v[32:35]\n"
"ds_read_b64_tr_b16 a[0:1], v4 offset:24768\n"
"v_cmp_gt_f32 vcc, s86, v100\n"
"v_mfma_f32_16x16x32_bf16 v[32:35], a[12:15], a[208:211], v[32:35]\n"
"ds_read_b64_tr_b16 a[2:3], v4 offset:26816\n"
"v_max_f32 v101, v101, v99\n"
"v_mfma_f32_16x16x32_bf16 v[36:39], a[12:15], a[212:215], v[36:39]\n"
"ds_read_b64_tr_b16 a[4:5], v8 offset:24768\n"
"v_cndmask_b32 v100, 4, 0, vcc\n"
"v_mfma_f32_16x16x32_bf16 v[36:39], a[8:11], v[168:171], v[36:39]\n"
"ds_read_b64_tr_b16 a[6:7], v8 offset:26816\n"
"v_cmp_lt_f32 vcc, s87, v101\n"
"v_mfma_f32_16x16x32_bf16 v[44:47], a[8:11], v[176:179], v[44:47]\n"
"ds_write_b32 v103, v99\n"
"v_mfma_f32_16x16x32_bf16 v[40:43], a[8:11], v[172:175], v[40:43]\n"
"s_waitcnt lgkmcnt(6)\n"
"ds_read_b64_tr_b16 a[8:9], v4 offset:28864\n"
"v_cndmask_b32 v100, v100, 0, vcc\n"
"v_mfma_f32_16x16x32_bf16 v[40:43], a[12:15], a[216:219], v[40:43]\n"
"s_lshr_b64 vcc, s[0:1], 2\n"
"ds_read_b64_tr_b16 a[10:11], v4 offset:30912\n"
"v_add_u32 v100, s90, v100\n"
"v_mfma_f32_16x16x32_bf16 v[80:83], a[16:19], v[144:147], v[80:83]\n"
"ds_read_b64_tr_b16 a[12:13], v8 offset:28864\n"
"v_add_u32 v103, s85, v103\n"
"v_mfma_f32_16x16x32_bf16 v[80:83], a[20:23], a[200:203], v[80:83]\n"
"s_cbranch_vccnz PC31_%=\n"
"PB31_%=:\n"
"v_mfma_f32_16x16x32_bf16 v[84:87], a[20:23], a[204:207], v[84:87]\n"
"ds_read_b64_tr_b16 a[14:15], v8 offset:30912\n"
"v_mfma_f32_16x16x32_bf16 v[84:87], a[16:19], v[148:151], v[84:87]\n"
"ds_write_b32 v100, v102\n"
"v_mfma_f32_16x16x32_bf16 v[92:95], a[16:19], v[156:159], v[92:95]\n"
"buffer_load_dwordx4 v27, s[48:51], s81 offen offset:2048 nt lds\n"
"v_mfma_f32_16x16x32_bf16 v[88:91], a[16:19], v[152:155], v[88:91]\n"
"s_waitcnt lgkmcnt(4)\n"
"ds_read_b64_tr_b16 a[16:17], v5 offset:24768\n"
"v_mfma_f32_16x16x32_bf16 v[88:91], a[20:23], v[160:163], v[88:91]\n"
"ds_read_b64_tr_b16 a[18:19], v5 offset:26816\n"
"v_mfma_f32_16x16x32_bf16 v[80:83], a[24:27], v[164:167], v[80:83]\n"
"ds_read_b64_tr_b16 a[20:21], v9 offset:24768\n"
"v_mfma_f32_16x16x32_bf16 v[80:83], a[28:31], a[208:211], v[80:83]\n"
"ds_read_b64_tr_b16 a[22:23], v9 offset:26816\n"
"v_mfma_f32_16x16x32_bf16 v[84:87], a[28:31], a[212:215], v[84:87]\n"
"v_mfma_f32_16x16x32_bf16 v[84:87], a[24:27], v[168:171], v[84:87]\n"
"v_mfma_f32_16x16x32_bf16 v[92:95], a[24:27], v[176:179], v[92:95]\n"
"v_mfma_f32_16x16x32_bf16 v[88:91], a[24:27], v[172:175], v[88:91]\n"
"ds_read_b64_tr_b16 a[24:25], v5 offset:28864\n"
"v_mfma_f32_16x16x32_bf16 v[88:91], a[28:31], a[216:219], v[88:91]\n"
"ds_read_b64_tr_b16 a[26:27], v5 offset:30912\n"
"v_mfma_f32_16x16x32_bf16 v[48:51], a[0:3], v[144:147], v[48:51]\n"
"ds_read_b64_tr_b16 a[28:29], v9 offset:28864\n"
"v_mfma_f32_16x16x32_bf16 v[48:51], a[4:7], a[200:203], v[48:51]\n"
"ds_read_b64_tr_b16 a[30:31], v9 offset:30912\n"
"v_mfma_f32_16x16x32_bf16 v[52:55], a[4:7], a[204:207], v[52:55]\n"
"v_mfma_f32_16x16x32_bf16 v[52:55], a[0:3], v[148:151], v[52:55]\n"
"buffer_load_dwordx4 v28, s[48:51], s81 offen offset:3072 nt lds\n"
"v_mfma_f32_16x16x32_bf16 v[60:63], a[0:3], v[156:159], v[60:63]\n"
"v_mfma_f32_16x16x32_bf16 v[56:59], a[0:3], v[152:155], v[56:59]\n"
"s_waitcnt lgkmcnt(3)\n"
"v_mfma_f32_16x16x32_bf16 v[56:59], a[4:7], v[160:163], v[56:59]\n"
"v_mfma_f32_16x16x32_bf16 v[48:51], a[8:11], v[164:167], v[48:51]\n"
"s_add_u32 s30, s65, 196992\n"
"v_mfma_f32_16x16x32_bf16 v[48:51], a[12:15], a[208:211], v[48:51]\n"
"s_memrealtime s[2:3]\n"
"v_mfma_f32_16x16x32_bf16 v[52:55], a[12:15], a[212:215], v[52:55]\n"
"v_mfma_f32_16x16x32_bf16 v[52:55], a[8:11], v[168:171], v[52:55]\n"
"buffer_load_dwordx4 a[160:163], v25, s[48:51], s30 offen offset:0 nt\n"
"v_mfma_f32_16x16x32_bf16 v[60:63], a[8:11], v[176:179], v[60:63]\n"
"v_mfma_f32_16x16x32_bf16 v[56:59], a[8:11], v[172:175], v[56:59]\n"
"v_mfma_f32_16x16x32_bf16 v[56:59], a[12:15], a[216:219], v[56:59]\n"
"v_mfma_f32_16x16x32_bf16 v[64:67], a[16:19], v[144:147], v[64:67]\n"
"v_mfma_f32_16x16x32_bf16 v[64:67], a[20:23], a[200:203], v[64:67]\n"
"s_waitcnt vmcnt(29) lgkmcnt(0)\n"
"v_mfma_f32_16x16x32_bf16 v[68:71], a[20:23], a[204:207], v[68:71]\n"
"s_barrier\n"
"v_mfma_f32_16x16x32_bf16 v[68:71], a[16:19], v[148:151], v[68:71]\n"
"s_addk_i32 m0, 8256\n"
"ds_read_b64_tr_b16 a[0:1], v2 offset:41280\n"
"s_add_u32 s81, s65, 131328\n"
"v_mfma_f32_16x16x32_bf16 v[76:79], a[16:19], v[156:159], v[76:79]\n"
"s_addk_i32 s79, 63\n"
"ds_read_b64_tr_b16 a[2:3], v2 offset:43328\n"
"v_mfma_f32_16x16x32_bf16 v[72:75], a[16:19], v[152:155], v[72:75]\n"
"ds_read_b64_tr_b16 a[4:5], v6 offset:41280\n"
"v_mfma_f32_16x16x32_bf16 v[72:75], a[20:23], v[160:163], v[72:75]\n"
"s_sub_u32 s0, s79, s2\n"
"ds_read_b64_tr_b16 a[6:7], v6 offset:43328\n"
"v_mfma_f32_16x16x32_bf16 v[64:67], a[24:27], v[164:167], v[64:67]\n"
"buffer_load_dwordx4 v25, s[48:51], s81 offen offset:0 nt lds\n"
"v_mfma_f32_16x16x32_bf16 v[64:67], a[28:31], a[208:211], v[64:67]\n"
"s_sub_u32 s0, s0, s23\n"
"ds_read_b64_tr_b16 a[8:9], v2 offset:45376\n"
"v_mfma_f32_16x16x32_bf16 v[68:71], a[28:31], a[212:215], v[68:71]\n"
"ds_read_b64_tr_b16 a[10:11], v2 offset:47424\n"
"v_mfma_f32_16x16x32_bf16 v[68:71], a[24:27], v[168:171], v[68:71]\n"
"s_sub_u32 s79, s77, s2\n"
"ds_read_b64_tr_b16 a[12:13], v6 offset:45376\n"
"v_mfma_f32_16x16x32_bf16 v[76:79], a[24:27], v[176:179], v[76:79]\n"
"ds_read_b64_tr_b16 a[14:15], v6 offset:47424\n"
"v_mfma_f32_16x16x32_bf16 v[72:75], a[24:27], v[172:175], v[72:75]\n"
"s_waitcnt lgkmcnt(4)\n"
"ds_read_b64_tr_b16 a[16:17], v3 offset:41280\n"
"s_addk_i32 s79, 2311\n"
"v_mfma_f32_16x16x32_bf16 v[72:75], a[28:31], a[216:219], v[72:75]\n"
"ds_read_b64_tr_b16 a[18:19], v3 offset:43328\n"
"v_mfma_f32_16x16x32_bf16 v[32:35], a[0:3], v[180:183], v[32:35]\n"
"s_max_i32 s0, s0, s79\n"
"ds_read_b64_tr_b16 a[20:21], v7 offset:41280\n"
"v_mfma_f32_16x16x32_bf16 v[32:35], a[4:7], a[220:223], v[32:35]\n"
"ds_read_b64_tr_b16 a[22:23], v7 offset:43328\n"
"v_mfma_f32_16x16x32_bf16 v[36:39], a[4:7], a[224:227], v[36:39]\n"
"s_min_i32 s0, s0, 40\n"
"ds_read_b64_tr_b16 a[24:25], v3 offset:45376\n"
"v_mfma_f32_16x16x32_bf16 v[36:39], a[0:3], v[184:187], v[36:39]\n"
"ds_read_b64_tr_b16 a[26:27], v3 offset:47424\n"
"v_mfma_f32_16x16x32_bf16 v[44:47], a[0:3], v[192:195], v[44:47]\n"
"s_max_i32 s0, s0, 0\n"
"ds_read_b64_tr_b16 a[28:29], v7 offset:45376\n"
"v_mfma_f32_16x16x32_bf16 v[40:43], a[0:3], v[188:191], v[40:43]\n"
"s_waitcnt lgkmcnt(6)\n"
"buffer_load_dwordx4 v26, s[48:51], s81 offen offset:1024 nt lds\n"
"v_mfma_f32_16x16x32_bf16 v[40:43], a[4:7], v[196:199], v[40:43]\n"
"s_add_u32 s79, s2, s0\n"
"ds_read_b64_tr_b16 a[30:31], v7 offset:47424\n"
"v_mfma_f32_16x16x32_bf16 v[32:35], a[8:11], v[200:203], v[32:35]\n"
"ds_read_b64_tr_b16 a[0:1], v4 offset:41280\n"
"v_mfma_f32_16x16x32_bf16 v[32:35], a[12:15], a[228:231], v[32:35]\n"
"s_lshr_b64 vcc, s[0:1], 2\n"
"ds_read_b64_tr_b16 a[2:3], v4 offset:43328\n"
"v_mfma_f32_16x16x32_bf16 v[36:39], a[12:15], a[232:235], v[36:39]\n"
"ds_read_b64_tr_b16 a[4:5], v8 offset:41280\n"
"v_mfma_f32_16x16x32_bf16 v[36:39], a[8:11], v[204:207], v[36:39]\n"
"s_cbranch_vccnz PC32_%=\n"
"PB32_%=:\n"
"v_mfma_f32_16x16x32_bf16 v[44:47], a[8:11], v[212:215], v[44:47]\n"
"ds_read_b64_tr_b16 a[6:7], v8 offset:43328\n"
"v_mfma_f32_16x16x32_bf16 v[40:43], a[8:11], v[208:211], v[40:43]\n"
"s_waitcnt lgkmcnt(5)\n"
"ds_read_b64_tr_b16 a[8:9], v4 offset:45376\n"
"v_mfma_f32_16x16x32_bf16 v[40:43], a[12:15], a[236:239], v[40:43]\n"
"ds_read_b64_tr_b16 a[10:11], v4 offset:47424\n"
"v_mfma_f32_16x16x32_bf16 v[80:83], a[16:19], v[180:183], v[80:83]\n"
"ds_read_b64_tr_b16 a[12:13], v8 offset:45376\n"
"v_mfma_f32_16x16x32_bf16 v[80:83], a[20:23], a[220:223], v[80:83]\n"
"ds_read_b64_tr_b16 a[14:15], v8 offset:47424\n"
"v_mfma_f32_16x16x32_bf16 v[84:87], a[20:23], a[224:227], v[84:87]\n"
"ds_read_b32 v100, v103\n"
"v_mfma_f32_16x16x32_bf16 v[84:87], a[16:19], v[184:187], v[84:87]\n"
"ds_read_b32 v101, v102\n"
"v_mfma_f32_16x16x32_bf16 v[92:95], a[16:19], v[192:195], v[92:95]\n"
"buffer_load_dwordx4 v27, s[48:51], s81 offen offset:2048 nt lds\n"
"v_mfma_f32_16x16x32_bf16 v[88:91], a[16:19], v[188:191], v[88:91]\n"
"s_waitcnt lgkmcnt(5)\n"
"ds_read_b64_tr_b16 a[16:17], v5 offset:41280\n"
"v_mfma_f32_16x16x32_bf16 v[88:91], a[20:23], v[196:199], v[88:91]\n"
"ds_read_b64_tr_b16 a[18:19], v5 offset:43328\n"
"v_mfma_f32_16x16x32_bf16 v[80:83], a[24:27], v[200:203], v[80:83]\n"
"ds_read_b64_tr_b16 a[20:21], v9 offset:41280\n"
"v_mfma_f32_16x16x32_bf16 v[80:83], a[28:31], a[228:231], v[80:83]\n"
"s_waitcnt lgkmcnt(3)\n"
"ds_read_b64_tr_b16 a[22:23], v9 offset:43328\n"
"v_cmp_ne_u32 vcc, 0, v101\n"
"v_mfma_f32_16x16x32_bf16 v[84:87], a[28:31], a[232:235], v[84:87]\n"
"s_cbranch_vccnz FALLBACK%=\n"
"v_rcp_f32 v15, v96\n"
"v_mfma_f32_16x16x32_bf16 v[84:87], a[24:27], v[204:207], v[84:87]\n"
"s_lshr_b32 s91, s20, 2\n"
"v_rcp_f32 v16, v97\n"
"v_mfma_f32_16x16x32_bf16 v[92:95], a[24:27], v[212:215], v[92:95]\n"
"s_lshl_b32 s91, s91, 1\n"
"v_add_f32 v99, v99, v100\n"
"v_add_u32 v30, 0, v1\n"
"v_mfma_f32_16x16x32_bf16 v[88:91], a[24:27], v[208:211], v[88:91]\n"
"ds_read_b64_tr_b16 a[24:25], v5 offset:45376\n"
"v_mul_u32_u24 v11, 4682, v30\n"
"v_mfma_f32_16x16x32_bf16 v[88:91], a[28:31], a[236:239], v[88:91]\n"
"s_add_u32 s91, s91, 2\n"
"ds_read_b64_tr_b16 a[26:27], v5 offset:47424\n"
"v_lshrrev_b32 v11, 16, v11\n"
"v_mfma_f32_16x16x32_bf16 v[48:51], a[0:3], v[180:183], v[48:51]\n"
"s_and_b32 s91, s91, 7\n"
"ds_read_b64_tr_b16 a[28:29], v9 offset:45376\n"
"v_mad_i32_i24 v30, v11, -14, v30\n"
"v_mfma_f32_16x16x32_bf16 v[48:51], a[4:7], a[220:223], v[48:51]\n"
"s_and_b32 s75, s20, 1\n"
"ds_read_b64_tr_b16 a[30:31], v9 offset:47424\n"
"v_mul_u32_u24 v11, 896, v11\n"
"v_mfma_f32_16x16x32_bf16 v[52:55], a[4:7], a[224:227], v[52:55]\n"
"s_add_u32 s75, s75, s91\n"
"v_rcp_f32 v17, v98\n"
"v_mfma_f32_16x16x32_bf16 v[52:55], a[0:3], v[184:187], v[52:55]\n"
"s_xor_b32 s56, s20, 0\n"
"buffer_load_dwordx4 v28, s[48:51], s81 offen offset:3072 nt lds\n"
"v_mfma_f32_16x16x32_bf16 v[60:63], a[0:3], v[192:195], v[60:63]\n"
"s_and_b32 s66, s56, 3\n"
"v_rcp_f32 v18, v99\n"
"v_mfma_f32_16x16x32_bf16 v[56:59], a[0:3], v[188:191], v[56:59]\n"
"v_lshl_add_u32 v30, s75, 1, v30\n"
"v_add_u32 v10, -14, v30\n"
"v_mfma_f32_16x16x32_bf16 v[56:59], a[4:7], v[196:199], v[56:59]\n"
"s_lshl_b32 s66, s66, 4\n"
"v_min_u32 v30, v30, v10\n"
"v_lshl_add_u32 v11, v30, 4, v11\n"
"v_mfma_f32_16x16x32_bf16 v[48:51], a[8:11], v[200:203], v[48:51]\n"
"s_add_u32 s66, s66, s60\n"
"v_add_u32 v30, 64, v1\n"
"v_mul_u32_u24 v12, 4682, v30\n"
"v_mfma_f32_16x16x32_bf16 v[48:51], a[12:15], a[228:231], v[48:51]\n"
"s_memrealtime s[2:3]\n"
"v_lshrrev_b32 v12, 16, v12\n"
"v_mad_i32_i24 v30, v12, -14, v30\n"
"v_mfma_f32_16x16x32_bf16 v[52:55], a[12:15], a[232:235], v[52:55]\n"
"s_mul_i32 s66, s66, 14336\n"
"v_lshl_add_u32 v30, s75, 1, v30\n"
"v_add_u32 v10, -14, v30\n"
"v_mfma_f32_16x16x32_bf16 v[52:55], a[8:11], v[204:207], v[52:55]\n"
"s_mov_b64 s[36:37], s[12:13]\n"
"v_mul_u32_u24 v12, 896, v12\n"
"v_min_u32 v30, v30, v10\n"
"v_mfma_f32_16x16x32_bf16 v[60:63], a[8:11], v[212:215], v[60:63]\n"
"s_and_b32 s37, s37, 0xffff\n"
"v_lshl_add_u32 v12, v30, 4, v12\n"
"v_add_u32 v12, -1024, v12\n"
"v_mfma_f32_16x16x32_bf16 v[56:59], a[8:11], v[208:211], v[56:59]\n"
"s_waitcnt lgkmcnt(4)\n"
"v_add_u32 v30, 128, v1\n"
"v_mul_u32_u24 v13, 4682, v30\n"
"v_mfma_f32_16x16x32_bf16 v[56:59], a[12:15], a[236:239], v[56:59]\n"
"s_mov_b32 s38, 916832\n"
"v_lshrrev_b32 v13, 16, v13\n"
"v_mad_i32_i24 v30, v13, -14, v30\n"
"v_mfma_f32_16x16x32_bf16 v[64:67], a[16:19], v[180:183], v[64:67]\n"
"s_mov_b32 s39, 0x20000\n"
"v_lshl_add_u32 v30, s75, 1, v30\n"
"v_add_u32 v10, -14, v30\n"
"v_mfma_f32_16x16x32_bf16 v[64:67], a[20:23], a[220:223], v[64:67]\n"
"s_waitcnt vmcnt(29) lgkmcnt(0)\n"
"v_mul_u32_u24 v13, 896, v13\n"
"v_min_u32 v30, v30, v10\n"
"v_mfma_f32_16x16x32_bf16 v[68:71], a[20:23], a[224:227], v[68:71]\n"
"s_barrier\n"
"v_mfma_f32_16x16x32_bf16 v[68:71], a[16:19], v[184:187], v[68:71]\n"
"s_addk_i32 m0, 16512\n"
"ds_read_b64_tr_b16 a[0:1], v2 offset:49536\n"
"v_lshl_add_u32 v13, v30, 4, v13\n"
"v_mfma_f32_16x16x32_bf16 v[76:79], a[16:19], v[192:195], v[76:79]\n"
"s_add_u32 s81, s65, 196864\n"
"ds_read_b64_tr_b16 a[2:3], v2 offset:51584\n"
"v_add_u32 v13, -2048, v13\n"
"v_mfma_f32_16x16x32_bf16 v[72:75], a[16:19], v[188:191], v[72:75]\n"
"ds_read_b64_tr_b16 a[4:5], v6 offset:49536\n"
"v_add_u32 v30, 192, v1\n"
"v_mfma_f32_16x16x32_bf16 v[72:75], a[20:23], v[196:199], v[72:75]\n"
"s_addk_i32 s79, 63\n"
"ds_read_b64_tr_b16 a[6:7], v6 offset:51584\n"
"v_mul_u32_u24 v14, 4682, v30\n"
"v_mfma_f32_16x16x32_bf16 v[64:67], a[24:27], v[200:203], v[64:67]\n"
"s_mul_i32 s85, s60, 1024\n"
"buffer_load_dwordx4 v25, s[48:51], s81 offen offset:0 nt lds\n"
"v_mfma_f32_16x16x32_bf16 v[64:67], a[28:31], a[228:231], v[64:67]\n"
"s_sub_u32 s0, s79, s2\n"
"ds_read_b64_tr_b16 a[8:9], v2 offset:53632\n"
"v_lshrrev_b32 v14, 16, v14\n"
"v_mfma_f32_16x16x32_bf16 v[68:71], a[28:31], a[232:235], v[68:71]\n"
"s_mul_i32 s56, s56, 118784\n"
"ds_read_b64_tr_b16 a[10:11], v2 offset:55680\n"
"v_mad_i32_i24 v30, v14, -14, v30\n"
"v_mfma_f32_16x16x32_bf16 v[68:71], a[24:27], v[204:207], v[68:71]\n"
"s_sub_u32 s0, s0, s23\n"
"ds_read_b64_tr_b16 a[12:13], v6 offset:53632\n"
"PG9_%=:\n"
"s_cbranch_execz PG13_%=\n"
"v_lshl_add_u32 v30, s75, 1, v30\n"
"v_mfma_f32_16x16x32_bf16 v[76:79], a[24:27], v[212:215], v[76:79]\n"
"s_xor_b32 s57, s20, 1\n"
"ds_read_b64_tr_b16 a[14:15], v6 offset:55680\n"
"v_lshl_add_u32 v19, v1, 4, s85\n"
"v_mfma_f32_16x16x32_bf16 v[72:75], a[24:27], v[208:211], v[72:75]\n"
"s_waitcnt lgkmcnt(4)\n"
"ds_read_b64_tr_b16 a[16:17], v3 offset:49536\n"
"v_add_u32 v10, -14, v30\n"
"v_mfma_f32_16x16x32_bf16 v[72:75], a[28:31], a[236:239], v[72:75]\n"
"s_max_i32 s0, s0, 0\n"
"ds_read_b64_tr_b16 a[18:19], v3 offset:51584\n"
"v_mul_u32_u24 v14, 896, v14\n"
"v_mfma_f32_16x16x32_bf16 v[32:35], a[0:3], v[216:219], v[32:35]\n"
"s_and_b32 s67, s57, 3\n"
"ds_read_b64_tr_b16 a[20:21], v7 offset:49536\n"
"v_min_u32 v30, v30, v10\n"
"v_mfma_f32_16x16x32_bf16 v[32:35], a[4:7], a[240:243], v[32:35]\n"
"s_add_u32 s79, s2, s0\n"
"buffer_load_dwordx4 a[164:167], v26, s[48:51], s30 offen offset:1024 nt\n"
"v_mfma_f32_16x16x32_bf16 v[36:39], a[4:7], a[244:247], v[36:39]\n"
"s_lshl_b32 s67, s67, 4\n"
"ds_read_b64_tr_b16 a[22:23], v7 offset:51584\n"
"v_lshl_add_u32 v14, v30, 4, v14\n"
"v_mfma_f32_16x16x32_bf16 v[36:39], a[0:3], v[220:223], v[36:39]\n"
"s_lshr_b64 vcc, s[0:1], 2\n"
"ds_read_b64_tr_b16 a[24:25], v3 offset:53632\n"
"v_add_u32 v14, -3072, v14\n"
"v_mfma_f32_16x16x32_bf16 v[44:47], a[0:3], v[228:231], v[44:47]\n"
"s_add_u32 s67, s67, s60\n"
"ds_read_b64_tr_b16 a[26:27], v3 offset:55680\n"
"v_lshrrev_b32 v30, 5, v1\n"
"v_mfma_f32_16x16x32_bf16 v[40:43], a[0:3], v[224:227], v[40:43]\n"
"s_waitcnt lgkmcnt(5)\n"
"buffer_load_dwordx4 v26, s[48:51], s81 offen offset:1024 nt lds\n"
"v_mfma_f32_16x16x32_bf16 v[40:43], a[4:7], v[232:235], v[40:43]\n"
"s_cbranch_vccnz PC33_%=\n"
"PB33_%=:\n"
"v_lshl_add_u32 v14, v30, 30, v14\n"
"v_mfma_f32_16x16x32_bf16 v[32:35], a[8:11], v[236:239], v[32:35]\n"
"s_mul_i32 s67, s67, 14336\n"
"ds_read_b64_tr_b16 a[28:29], v7 offset:53632\n"
"v_lshrrev_b32 v30, 2, v1\n"
"v_mfma_f32_16x16x32_bf16 v[32:35], a[12:15], a[248:251], v[32:35]\n"
"s_lshr_b32 s95, s20, 2\n"
"ds_read_b64_tr_b16 a[30:31], v7 offset:55680\n"
"v_and_b32 v10, 3, v1\n"
"v_mfma_f32_16x16x32_bf16 v[36:39], a[12:15], a[252:255], v[36:39]\n"
"s_lshl_b32 s95, s95, 9\n"
"ds_read_b64_tr_b16 a[0:1], v4 offset:49536\n"
"v_mul_u32_u24 v30, 224, v30\n"
"v_mfma_f32_16x16x32_bf16 v[36:39], a[8:11], v[240:243], v[36:39]\n"
"s_add_u32 s95, s95, 2883584\n"
"ds_read_b64_tr_b16 a[2:3], v4 offset:51584\n"
"v_mul_f32 v32, v15, v32\n"
"v_mfma_f32_16x16x32_bf16 v[44:47], a[8:11], v[248:251], v[44:47]\n"
"s_and_b32 s85, s20, 3\n"
"ds_read_b64_tr_b16 a[4:5], v8 offset:49536\n"
"v_mul_f32 v33, v15, v33\n"
"v_mfma_f32_16x16x32_bf16 v[40:43], a[8:11], v[244:247], v[40:43]\n"
"s_waitcnt lgkmcnt(5)\n"
"ds_read_b64_tr_b16 a[6:7], v8 offset:51584\n"
"v_mul_f32 v34, v15, v34\n"
"v_mfma_f32_16x16x32_bf16 v[40:43], a[12:15], v[252:255], v[40:43]\n"
"s_lshl_b32 s85, s85, 3\n"
"ds_read_b64_tr_b16 a[8:9], v4 offset:53632\n"
"v_mul_f32 v35, v15, v35\n"
"v_mfma_f32_16x16x32_bf16 v[80:83], a[16:19], v[216:219], v[80:83]\n"
"s_add_u32 s85, s85, s95\n"
"ds_read_b64_tr_b16 a[10:11], v4 offset:55680\n"
"v_cvt_pk_bf16_f32 v32, v32, v33\n"
"v_mfma_f32_16x16x32_bf16 v[80:83], a[20:23], a[240:243], v[80:83]\n"
"s_xor_b32 s58, s20, 2\n"
"ds_read_b64_tr_b16 a[12:13], v8 offset:53632\n"
"v_cvt_pk_bf16_f32 v34, v34, v35\n"
"v_mfma_f32_16x16x32_bf16 v[84:87], a[20:23], a[244:247], v[84:87]\n"
"s_and_b32 s68, s58, 3\n"
"ds_read_b64_tr_b16 a[14:15], v8 offset:55680\n"
"v_accvgpr_write_b32 a32, v32\n"
"v_mfma_f32_16x16x32_bf16 v[84:87], a[16:19], v[220:223], v[84:87]\n"
"s_lshl_b32 s68, s68, 4\n"
"v_accvgpr_write_b32 a33, v34\n"
"v_mul_f32 v36, v16, v36\n"
"v_mfma_f32_16x16x32_bf16 v[92:95], a[16:19], v[228:231], v[92:95]\n"
"s_add_u32 s68, s68, s60\n"
"buffer_load_dwordx4 v27, s[48:51], s81 offen offset:2048 nt lds\n"
"v_mfma_f32_16x16x32_bf16 v[88:91], a[16:19], v[224:227], v[88:91]\n"
"s_waitcnt lgkmcnt(4)\n"
"ds_read_b64_tr_b16 a[16:17], v5 offset:49536\n"
"v_mul_f32 v37, v16, v37\n"
"v_mfma_f32_16x16x32_bf16 v[88:91], a[20:23], v[232:235], v[88:91]\n"
"s_mul_i32 s68, s68, 14336\n"
"ds_read_b64_tr_b16 a[18:19], v5 offset:51584\n"
"v_mul_f32 v38, v16, v38\n"
"v_mfma_f32_16x16x32_bf16 v[80:83], a[24:27], v[236:239], v[80:83]\n"
"s_xor_b32 s59, s20, 3\n"
"ds_read_b64_tr_b16 a[20:21], v9 offset:49536\n"
"v_mul_f32 v39, v16, v39\n"
"v_mfma_f32_16x16x32_bf16 v[80:83], a[28:31], a[248:251], v[80:83]\n"
"s_and_b32 s69, s59, 3\n"
"ds_read_b64_tr_b16 a[22:23], v9 offset:51584\n"
"v_cvt_pk_bf16_f32 v36, v36, v37\n"
"v_mfma_f32_16x16x32_bf16 v[84:87], a[28:31], a[252:255], v[84:87]\n"
"s_lshl_b32 s69, s69, 4\n"
"v_cvt_pk_bf16_f32 v38, v38, v39\n"
"v_accvgpr_write_b32 a40, v36\n"
"v_mfma_f32_16x16x32_bf16 v[84:87], a[24:27], v[240:243], v[84:87]\n"
"s_add_u32 s69, s69, s60\n"
"v_accvgpr_write_b32 a41, v38\n"
"v_mul_f32 v40, v17, v40\n"
"v_mfma_f32_16x16x32_bf16 v[92:95], a[24:27], v[248:251], v[92:95]\n"
"s_mul_i32 s69, s69, 14336\n"
"v_mul_f32 v41, v17, v41\n"
"v_mul_f32 v42, v17, v42\n"
"v_mfma_f32_16x16x32_bf16 v[88:91], a[24:27], v[244:247], v[88:91]\n"
"ds_read_b64_tr_b16 a[24:25], v5 offset:53632\n"
"v_mul_f32 v43, v17, v43\n"
"v_mfma_f32_16x16x32_bf16 v[88:91], a[28:31], v[252:255], v[88:91]\n"
"s_mul_i32 s57, s57, 118784\n"
"ds_read_b64_tr_b16 a[26:27], v5 offset:55680\n"
"v_mul_f32 v96, v18, v44\n"
"v_mfma_f32_16x16x32_bf16 v[48:51], a[0:3], v[216:219], v[48:51]\n"
"s_mul_i32 s58, s58, 118784\n"
"ds_read_b64_tr_b16 a[28:29], v9 offset:53632\n"
"v_mul_f32 v97, v18, v45\n"
"v_mfma_f32_16x16x32_bf16 v[48:51], a[4:7], a[240:243], v[48:51]\n"
"s_mul_i32 s59, s59, 118784\n"
"ds_read_b64_tr_b16 a[30:31], v9 offset:55680\n"
"v_mul_f32 v98, v18, v46\n"
"v_mfma_f32_16x16x32_bf16 v[52:55], a[4:7], a[244:247], v[52:55]\n"
"s_add_u32 s82, s91, -2\n"
"v_mul_f32 v99, v18, v47\n"
"v_cvt_pk_bf16_f32 v40, v40, v41\n"
"v_mfma_f32_16x16x32_bf16 v[52:55], a[0:3], v[220:223], v[52:55]\n"
"buffer_load_dwordx4 v28, s[48:51], s81 offen offset:3072 nt lds\n"
"v_mfma_f32_16x16x32_bf16 v[60:63], a[0:3], v[228:231], v[60:63]\n"
"v_cvt_pk_bf16_f32 v42, v42, v43\n"
"v_accvgpr_write_b32 a48, v40\n"
"v_mfma_f32_16x16x32_bf16 v[56:59], a[0:3], v[224:227], v[56:59]\n"
"s_waitcnt lgkmcnt(3)\n"
"v_accvgpr_write_b32 a49, v42\n"
"v_mul_f32 v80, v15, v80\n"
"v_mfma_f32_16x16x32_bf16 v[56:59], a[4:7], v[232:235], v[56:59]\n"
"v_mul_f32 v81, v15, v81\n"
"v_mul_f32 v82, v15, v82\n"
"v_mfma_f32_16x16x32_bf16 v[48:51], a[8:11], v[236:239], v[48:51]\n"
"v_mul_f32 v83, v15, v83\n"
"v_cvt_pk_bf16_f32 v80, v80, v81\n"
"v_mfma_f32_16x16x32_bf16 v[48:51], a[12:15], a[248:251], v[48:51]\n"
"s_memrealtime s[2:3]\n"
"v_cvt_pk_bf16_f32 v82, v82, v83\n"
"v_accvgpr_write_b32 a34, v80\n"
"v_mfma_f32_16x16x32_bf16 v[52:55], a[12:15], a[252:255], v[52:55]\n"
"v_accvgpr_write_b32 a35, v82\n"
"v_mul_f32 v84, v16, v84\n"
"v_mfma_f32_16x16x32_bf16 v[52:55], a[8:11], v[240:243], v[52:55]\n"
"v_mul_f32 v85, v16, v85\n"
"v_mul_f32 v86, v16, v86\n"
"v_mfma_f32_16x16x32_bf16 v[60:63], a[8:11], v[248:251], v[60:63]\n"
"v_mul_f32 v87, v16, v87\n"
"v_cvt_pk_bf16_f32 v84, v84, v85\n"
"v_mfma_f32_16x16x32_bf16 v[56:59], a[8:11], v[244:247], v[56:59]\n"
"v_cvt_pk_bf16_f32 v86, v86, v87\n"
"v_accvgpr_write_b32 a42, v84\n"
"v_mfma_f32_16x16x32_bf16 v[56:59], a[12:15], v[252:255], v[56:59]\n"
"v_accvgpr_write_b32 a43, v86\n"
"v_mul_f32 v88, v17, v88\n"
"v_mfma_f32_16x16x32_bf16 v[64:67], a[16:19], v[216:219], v[64:67]\n"
"ds_write_b128 v23, v[60:63] offset:0\n"
"v_mfma_f32_16x16x32_bf16 v[64:67], a[20:23], a[240:243], v[64:67]\n"
"s_waitcnt vmcnt(30) lgkmcnt(0)\n"
"v_mul_f32 v89, v17, v89\n"
"v_mul_f32 v90, v17, v90\n"
"v_mfma_f32_16x16x32_bf16 v[68:71], a[20:23], a[244:247], v[68:71]\n"
"s_barrier\n"
"v_mfma_f32_16x16x32_bf16 v[68:71], a[16:19], v[220:223], v[68:71]\n"
"s_addk_i32 m0, 8256\n"
"ds_read_b64_tr_b16 a[0:1], v2 offset:57792\n"
"v_mul_f32 v91, v17, v91\n"
"v_mfma_f32_16x16x32_bf16 v[76:79], a[16:19], v[228:231], v[76:79]\n"
"s_add_u32 s81, s65, 384\n"
"ds_read_b64_tr_b16 a[2:3], v2 offset:59840\n"
"v_mul_f32 v100, v18, v92\n"
"v_mfma_f32_16x16x32_bf16 v[72:75], a[16:19], v[224:227], v[72:75]\n"
"ds_read_b64_tr_b16 a[4:5], v6 offset:57792\n"
"v_mul_f32 v101, v18, v93\n"
"v_mfma_f32_16x16x32_bf16 v[72:75], a[20:23], v[232:235], v[72:75]\n"
"s_addk_i32 s79, 68\n"
"ds_read_b64_tr_b16 a[6:7], v6 offset:59840\n"
"v_mul_f32 v102, v18, v94\n"
"v_mfma_f32_16x16x32_bf16 v[64:67], a[24:27], v[236:239], v[64:67]\n"
"buffer_load_dwordx4 v25, s[48:51], s81 offen offset:0 nt lds\n"
"v_mfma_f32_16x16x32_bf16 v[64:67], a[28:31], a[248:251], v[64:67]\n"
"s_sub_u32 s0, s79, s2\n"
"ds_read_b64_tr_b16 a[8:9], v2 offset:61888\n"
"v_mul_f32 v103, v18, v95\n"
"v_mfma_f32_16x16x32_bf16 v[68:71], a[28:31], a[252:255], v[68:71]\n"
"ds_read_b64_tr_b16 a[10:11], v2 offset:63936\n"
"v_cvt_pk_bf16_f32 v88, v88, v89\n"
"v_mfma_f32_16x16x32_bf16 v[68:71], a[24:27], v[240:243], v[68:71]\n"
"s_sub_u32 s0, s0, s23\n"
"ds_read_b64_tr_b16 a[12:13], v6 offset:61888\n"
"v_cvt_pk_bf16_f32 v90, v90, v91\n"
"v_mfma_f32_16x16x32_bf16 v[76:79], a[24:27], v[248:251], v[76:79]\n"
"ds_read_b64_tr_b16 a[14:15], v6 offset:63936\n"
"v_accvgpr_write_b32 a50, v88\n"
"v_mfma_f32_16x16x32_bf16 v[72:75], a[24:27], v[244:247], v[72:75]\n"
"s_waitcnt lgkmcnt(4)\n"
"ds_read_b64_tr_b16 a[16:17], v3 offset:57792\n"
"v_accvgpr_write_b32 a51, v90\n"
"v_mfma_f32_16x16x32_bf16 v[72:75], a[28:31], v[252:255], v[72:75]\n"
"s_max_i32 s0, s0, 0\n"
"ds_read_b64_tr_b16 a[18:19], v3 offset:59840\n"
"v_mul_f32 v48, v15, v48\n"
"v_mfma_f32_16x16x32_bf16 v[32:35], a[0:3], v[108:111], 0\n"
"ds_read_b64_tr_b16 a[20:21], v7 offset:57792\n"
"v_mul_f32 v49, v15, v49\n"
"v_mfma_f32_16x16x32_bf16 v[32:35], a[4:7], a[180:183], v[32:35]\n"
"s_add_u32 s79, s2, s0\n"
"ds_read_b64_tr_b16 a[22:23], v7 offset:59840\n"
"v_mul_f32 v50, v15, v50\n"
"v_mfma_f32_16x16x32_bf16 v[36:39], a[4:7], a[184:187], 0\n"
"ds_read_b64_tr_b16 a[24:25], v3 offset:61888\n"
"v_mul_f32 v51, v15, v51\n"
"v_mfma_f32_16x16x32_bf16 v[36:39], a[0:3], v[112:115], v[36:39]\n"
"s_lshr_b64 vcc, s[0:1], 2\n"
"ds_read_b64_tr_b16 a[26:27], v3 offset:63936\n"
"v_cvt_pk_bf16_f32 v48, v48, v49\n"
"v_mfma_f32_16x16x32_bf16 v[44:47], a[0:3], v[120:123], 0\n"
"ds_read_b64_tr_b16 a[28:29], v7 offset:61888\n"
"v_cvt_pk_bf16_f32 v50, v50, v51\n"
"v_mfma_f32_16x16x32_bf16 v[40:43], a[0:3], v[116:119], 0\n"
"s_waitcnt lgkmcnt(6)\n"
"ds_read_b64_tr_b16 a[30:31], v7 offset:63936\n"
"v_accvgpr_write_b32 a36, v48\n"
"v_mfma_f32_16x16x32_bf16 v[40:43], a[4:7], v[124:127], v[40:43]\n"
"s_cbranch_vccnz PC34_%=\n"
"PB34_%=:\n"
"v_accvgpr_write_b32 a37, v50\n"
"v_mfma_f32_16x16x32_bf16 v[32:35], a[8:11], v[128:131], v[32:35]\n"
"buffer_load_dwordx4 v26, s[48:51], s81 offen offset:1024 nt lds\n"
"v_mfma_f32_16x16x32_bf16 v[32:35], a[12:15], a[188:191], v[32:35]\n"
"ds_read_b64_tr_b16 a[0:1], v4 offset:57792\n"
"v_mul_f32 v52, v16, v52\n"
"v_mfma_f32_16x16x32_bf16 v[36:39], a[12:15], a[192:195], v[36:39]\n"
"ds_read_b64_tr_b16 a[2:3], v4 offset:59840\n"
"v_mul_f32 v53, v16, v53\n"
"v_mfma_f32_16x16x32_bf16 v[36:39], a[8:11], v[132:135], v[36:39]\n"
"ds_read_b64_tr_b16 a[4:5], v8 offset:57792\n"
"v_mul_f32 v54, v16, v54\n"
"v_mfma_f32_16x16x32_bf16 v[44:47], a[8:11], v[140:143], v[44:47]\n"
"ds_read_b64_tr_b16 a[6:7], v8 offset:59840\n"
"v_mul_f32 v55, v16, v55\n"
"v_mfma_f32_16x16x32_bf16 v[40:43], a[8:11], v[136:139], v[40:43]\n"
"s_waitcnt lgkmcnt(4)\n"
"ds_read_b64_tr_b16 a[8:9], v4 offset:61888\n"
"v_cvt_pk_bf16_f32 v52, v52, v53\n"
"v_mfma_f32_16x16x32_bf16 v[40:43], a[12:15], a[196:199], v[40:43]\n"
"ds_read_b64_tr_b16 a[10:11], v4 offset:63936\n"
"v_cvt_pk_bf16_f32 v54, v54, v55\n"
"v_mfma_f32_16x16x32_bf16 v[80:83], a[16:19], v[108:111], 0\n"
"ds_read_b64_tr_b16 a[12:13], v8 offset:61888\n"
"v_accvgpr_write_b32 a44, v52\n"
"v_mfma_f32_16x16x32_bf16 v[80:83], a[20:23], a[180:183], v[80:83]\n"
"ds_read_b64_tr_b16 a[14:15], v8 offset:63936\n"
"v_accvgpr_write_b32 a45, v54\n"
"v_mfma_f32_16x16x32_bf16 v[84:87], a[20:23], a[184:187], 0\n"
"ds_write_b128 v23, v[76:79] offset:1024\n"
"v_mfma_f32_16x16x32_bf16 v[84:87], a[16:19], v[112:115], v[84:87]\n"
"v_mul_f32 v56, v17, v56\n"
"v_mul_f32 v57, v17, v57\n"
"v_mfma_f32_16x16x32_bf16 v[92:95], a[16:19], v[120:123], 0\n"
"v_mul_f32 v58, v17, v58\n"
"v_mul_f32 v59, v17, v59\n"
"v_mfma_f32_16x16x32_bf16 v[88:91], a[16:19], v[116:119], 0\n"
"ds_read_b64_tr_b16 a[16:17], v5 offset:57792\n"
"v_cvt_pk_bf16_f32 v56, v56, v57\n"
"v_mfma_f32_16x16x32_bf16 v[88:91], a[20:23], v[124:127], v[88:91]\n"
"buffer_load_dwordx4 v27, s[48:51], s81 offen offset:2048 nt lds\n"
"v_mfma_f32_16x16x32_bf16 v[80:83], a[24:27], v[128:131], v[80:83]\n"
"ds_read_b64_tr_b16 a[18:19], v5 offset:59840\n"
"v_cvt_pk_bf16_f32 v58, v58, v59\n"
"v_mfma_f32_16x16x32_bf16 v[80:83], a[28:31], a[188:191], v[80:83]\n"
"ds_read_b64_tr_b16 a[20:21], v9 offset:57792\n"
"v_accvgpr_write_b32 a52, v56\n"
"v_mfma_f32_16x16x32_bf16 v[84:87], a[28:31], a[192:195], v[84:87]\n"
"ds_read_b64_tr_b16 a[22:23], v9 offset:59840\n"
"v_accvgpr_write_b32 a53, v58\n"
"v_mfma_f32_16x16x32_bf16 v[84:87], a[24:27], v[132:135], v[84:87]\n"
"v_mul_f32 v64, v15, v64\n"
"v_mul_f32 v65, v15, v65\n"
"v_mfma_f32_16x16x32_bf16 v[92:95], a[24:27], v[140:143], v[92:95]\n"
"buffer_load_dwordx4 a[168:171], v27, s[48:51], s30 offen offset:2048 nt\n"
"v_mfma_f32_16x16x32_bf16 v[88:91], a[24:27], v[136:139], v[88:91]\n"
"s_waitcnt lgkmcnt(3)\n"
"ds_read_b64_tr_b16 a[24:25], v5 offset:61888\n"
"v_mul_f32 v66, v15, v66\n"
"v_mfma_f32_16x16x32_bf16 v[88:91], a[28:31], a[196:199], v[88:91]\n"
"ds_read_b64_tr_b16 a[26:27], v5 offset:63936\n"
"v_mul_f32 v67, v15, v67\n"
"v_mfma_f32_16x16x32_bf16 v[48:51], a[0:3], v[108:111], 0\n"
"ds_read_b64_tr_b16 a[28:29], v9 offset:61888\n"
"v_cvt_pk_bf16_f32 v64, v64, v65\n"
"v_mfma_f32_16x16x32_bf16 v[48:51], a[4:7], a[180:183], v[48:51]\n"
"ds_read_b64_tr_b16 a[30:31], v9 offset:63936\n"
"v_cvt_pk_bf16_f32 v66, v66, v67\n"
"v_mfma_f32_16x16x32_bf16 v[52:55], a[4:7], a[184:187], 0\n"
"v_accvgpr_write_b32 a38, v64\n"
"v_accvgpr_write_b32 a39, v66\n"
"v_mfma_f32_16x16x32_bf16 v[52:55], a[0:3], v[112:115], v[52:55]\n"
"v_mul_f32 v68, v16, v68\n"
"v_mul_f32 v69, v16, v69\n"
"v_mfma_f32_16x16x32_bf16 v[60:63], a[0:3], v[120:123], 0\n"
"v_mul_f32 v70, v16, v70\n"
"v_mul_f32 v71, v16, v71\n"
"v_mfma_f32_16x16x32_bf16 v[56:59], a[0:3], v[116:119], 0\n"
"v_cvt_pk_bf16_f32 v68, v68, v69\n"
"v_cvt_pk_bf16_f32 v70, v70, v71\n"
"v_mfma_f32_16x16x32_bf16 v[56:59], a[4:7], v[124:127], v[56:59]\n"
"buffer_load_dwordx4 v28, s[48:51], s81 offen offset:3072 nt lds\n"
"v_mfma_f32_16x16x32_bf16 v[48:51], a[8:11], v[128:131], v[48:51]\n"
"v_accvgpr_write_b32 a46, v68\n"
"v_accvgpr_write_b32 a47, v70\n"
"v_mfma_f32_16x16x32_bf16 v[48:51], a[12:15], a[188:191], v[48:51]\n"
"s_memrealtime s[2:3]\n"
"v_mul_f32 v72, v17, v72\n"
"v_mul_f32 v73, v17, v73\n"
"v_mfma_f32_16x16x32_bf16 v[52:55], a[12:15], a[192:195], v[52:55]\n"
"v_mul_f32 v74, v17, v74\n"
"v_mul_f32 v75, v17, v75\n"
"v_mfma_f32_16x16x32_bf16 v[52:55], a[8:11], v[132:135], v[52:55]\n"
"v_cvt_pk_bf16_f32 v72, v72, v73\n"
"v_cvt_pk_bf16_f32 v74, v74, v75\n"
"v_mfma_f32_16x16x32_bf16 v[60:63], a[8:11], v[140:143], v[60:63]\n"
"v_accvgpr_write_b32 a54, v72\n"
"v_accvgpr_write_b32 a55, v74\n"
"v_mfma_f32_16x16x32_bf16 v[56:59], a[8:11], v[136:139], v[56:59]\n"
"s_waitcnt lgkmcnt(4)\n"
"v_add_u32 v2, s27, v2\n"
"v_add_u32 v6, s27, v6\n"
"v_mfma_f32_16x16x32_bf16 v[56:59], a[12:15], a[196:199], v[56:59]\n"
"v_add_u32 v3, s27, v3\n"
"v_add_u32 v7, s27, v7\n"
"v_mfma_f32_16x16x32_bf16 v[64:67], a[16:19], v[108:111], 0\n"
"v_add_u32 v4, s27, v4\n"
"v_add_u32 v8, s27, v8\n"
"v_mfma_f32_16x16x32_bf16 v[64:67], a[20:23], a[180:183], v[64:67]\n"
"s_waitcnt vmcnt(31) lgkmcnt(0)\n"
"v_add_u32 v5, s27, v5\n"
"v_add_u32 v9, s27, v9\n"
"v_mfma_f32_16x16x32_bf16 v[68:71], a[20:23], a[184:187], 0\n"
"s_barrier\n"
"v_mfma_f32_16x16x32_bf16 v[68:71], a[16:19], v[112:115], v[68:71]\n"
"s_addk_i32 m0, 8256\n"
"ds_read_b64_tr_b16 a[0:1], v2 offset:57792\n"
"v_lshl_add_u32 v30, v10, 3, v30\n"
"v_mfma_f32_16x16x32_bf16 v[76:79], a[16:19], v[120:123], 0\n"
"s_add_u32 s81, s65, 65920\n"
"ds_read_b64_tr_b16 a[2:3], v2 offset:59840\n"
"v_lshl_add_u32 v0, v10, 3, s95\n"
"v_mfma_f32_16x16x32_bf16 v[72:75], a[16:19], v[116:119], 0\n"
"ds_read_b64_tr_b16 a[4:5], v6 offset:57792\n"
"v_add_u32 v21, s83, v30\n"
"v_mfma_f32_16x16x32_bf16 v[72:75], a[20:23], v[124:127], v[72:75]\n"
"s_addk_i32 s79, 68\n"
"ds_read_b64_tr_b16 a[6:7], v6 offset:59840\n"
"v_add_u32 v22, s84, v30\n"
"v_mfma_f32_16x16x32_bf16 v[64:67], a[24:27], v[128:131], v[64:67]\n"
"buffer_load_dwordx4 v25, s[48:51], s81 offen offset:0 nt lds\n"
"v_mfma_f32_16x16x32_bf16 v[64:67], a[28:31], a[188:191], v[64:67]\n"
"s_sub_u32 s0, s79, s2\n"
"ds_read_b64_tr_b16 a[8:9], v2 offset:61888\n"
"v_lshl_add_u32 v10, s60, 6, v1\n"
"v_mfma_f32_16x16x32_bf16 v[68:71], a[28:31], a[192:195], v[68:71]\n"
"ds_read_b64_tr_b16 a[10:11], v2 offset:63936\n"
"v_subrev_u32 v2, s27, v2\n"
"v_mfma_f32_16x16x32_bf16 v[68:71], a[24:27], v[132:135], v[68:71]\n"
"s_sub_u32 s0, s0, s23\n"
"ds_read_b64_tr_b16 a[12:13], v6 offset:61888\n"
"v_lshl_add_u32 v29, v10, 22, s85\n"
"v_mfma_f32_16x16x32_bf16 v[76:79], a[24:27], v[140:143], v[76:79]\n"
"s_add_u32 s85, s91, 5\n"
"ds_read_b64_tr_b16 a[14:15], v6 offset:63936\n"
"v_subrev_u32 v6, s27, v6\n"
"v_mfma_f32_16x16x32_bf16 v[72:75], a[24:27], v[136:139], v[72:75]\n"
"s_waitcnt lgkmcnt(4)\n"
"ds_read_b64_tr_b16 a[16:17], v3 offset:57792\n"
"v_mov_b32 v30, s88\n"
"v_mfma_f32_16x16x32_bf16 v[72:75], a[28:31], a[196:199], v[72:75]\n"
"s_sub_u32 s79, s77, s2\n"
"ds_read_b64_tr_b16 a[18:19], v3 offset:59840\n"
"v_and_b32 v10, 15, v1\n"
"v_mfma_f32_16x16x32_bf16 v[32:35], a[0:3], v[144:147], v[32:35]\n"
"s_min_u32 s82, s82, s85\n"
"ds_read_b64_tr_b16 a[20:21], v7 offset:57792\n"
"s_add_u32 s85, s91, -2\n"
"v_mfma_f32_16x16x32_bf16 v[32:35], a[4:7], a[200:203], v[32:35]\n"
"s_addk_i32 s79, 2521\n"
"ds_read_b64_tr_b16 a[22:23], v7 offset:59840\n"
"s_and_b32 s85, s85, 7\n"
"v_mfma_f32_16x16x32_bf16 v[36:39], a[4:7], a[204:207], v[36:39]\n"
"s_sub_u32 s85, s82, s85\n"
"ds_read_b64_tr_b16 a[24:25], v3 offset:61888\n"
"s_add_u32 s73, s85, 8\n"
"v_mfma_f32_16x16x32_bf16 v[36:39], a[0:3], v[148:151], v[36:39]\n"
"s_max_i32 s0, s0, s79\n"
"ds_read_b64_tr_b16 a[26:27], v3 offset:63936\n"
"v_subrev_u32 v3, s27, v3\n"
"v_mfma_f32_16x16x32_bf16 v[44:47], a[0:3], v[156:159], v[44:47]\n"
"s_add_u32 s85, s85, 1\n"
"ds_read_b64_tr_b16 a[28:29], v7 offset:61888\n"
"s_min_u32 s85, s85, s73\n"
"v_mfma_f32_16x16x32_bf16 v[40:43], a[0:3], v[152:155], v[40:43]\n"
"s_waitcnt lgkmcnt(6)\n"
"ds_read_b64_tr_b16 a[30:31], v7 offset:63936\n"
"v_subrev_u32 v7, s27, v7\n"
"v_mfma_f32_16x16x32_bf16 v[40:43], a[4:7], v[160:163], v[40:43]\n"
"s_min_i32 s0, s0, 40\n"
"ds_read_b64_tr_b16 a[0:1], v4 offset:57792\n"
"s_lshl_b32 s85, s85, 11\n"
"v_mfma_f32_16x16x32_bf16 v[32:35], a[8:11], v[164:167], v[32:35]\n"
"s_add_u32 s71, s71, s85\n"
"buffer_load_dwordx4 v26, s[48:51], s81 offen offset:1024 nt lds\n"
"v_mfma_f32_16x16x32_bf16 v[32:35], a[12:15], a[208:211], v[32:35]\n"
"s_max_i32 s0, s0, 0\n"
"ds_read_b64_tr_b16 a[2:3], v4 offset:59840\n"
"s_add_u32 s85, s91, -4\n"
"v_mfma_f32_16x16x32_bf16 v[36:39], a[12:15], a[212:215], v[36:39]\n"
"s_and_b32 s85, s85, 7\n"
"ds_read_b64_tr_b16 a[4:5], v8 offset:57792\n"
"s_sub_u32 s85, s82, s85\n"
"v_mfma_f32_16x16x32_bf16 v[36:39], a[8:11], v[168:171], v[36:39]\n"
"s_add_u32 s79, s2, s0\n"
"ds_read_b64_tr_b16 a[6:7], v8 offset:59840\n"
"s_add_u32 s73, s85, 8\n"
"v_mfma_f32_16x16x32_bf16 v[44:47], a[8:11], v[176:179], v[44:47]\n"
"s_add_u32 s85, s85, 1\n"
"ds_read_b128 v[104:107], v24 offset:0\n"
"s_min_u32 s85, s85, s73\n"
"v_mfma_f32_16x16x32_bf16 v[40:43], a[8:11], v[172:175], v[40:43]\n"
"s_waitcnt lgkmcnt(5)\n"
"ds_read_b64_tr_b16 a[8:9], v4 offset:61888\n"
"s_lshr_b64 vcc, s[0:1], 2\n"
"v_mfma_f32_16x16x32_bf16 v[40:43], a[12:15], a[216:219], v[40:43]\n"
"s_lshl_b32 s85, s85, 11\n"
"ds_read_b64_tr_b16 a[10:11], v4 offset:63936\n"
"v_subrev_u32 v4, s27, v4\n"
"v_mfma_f32_16x16x32_bf16 v[80:83], a[16:19], v[144:147], v[80:83]\n"
"s_cbranch_vccnz PC35_%=\n"
"PB35_%=:\n"
"v_mfma_f32_16x16x32_bf16 v[80:83], a[20:23], a[200:203], v[80:83]\n"
"s_add_u32 s72, s72, s85\n"
"ds_read_b64_tr_b16 a[12:13], v8 offset:61888\n"
"s_add_u32 s85, s91, -6\n"
"v_mfma_f32_16x16x32_bf16 v[84:87], a[20:23], a[204:207], v[84:87]\n"
"s_waitcnt lgkmcnt(3)\n"
"ds_read_b64_tr_b16 a[14:15], v8 offset:63936\n"
"v_fmac_f32 v96, v18, v104\n"
"v_mfma_f32_16x16x32_bf16 v[84:87], a[16:19], v[148:151], v[84:87]\n"
"s_and_b32 s85, s85, 7\n"
"v_fmac_f32 v97, v18, v105\n"
"v_fmac_f32 v98, v18, v106\n"
"v_mfma_f32_16x16x32_bf16 v[92:95], a[16:19], v[156:159], v[92:95]\n"
"s_sub_u32 s85, s82, s85\n"
"v_fmac_f32 v99, v18, v107\n"
"v_subrev_u32 v8, s27, v8\n"
"v_mfma_f32_16x16x32_bf16 v[88:91], a[16:19], v[152:155], v[88:91]\n"
"ds_read_b64_tr_b16 a[16:17], v5 offset:57792\n"
"v_cvt_pk_bf16_f32 v96, v96, v97\n"
"v_mfma_f32_16x16x32_bf16 v[88:91], a[20:23], v[160:163], v[88:91]\n"
"s_add_u32 s73, s85, 8\n"
"ds_read_b64_tr_b16 a[18:19], v5 offset:59840\n"
"v_cvt_pk_bf16_f32 v98, v98, v99\n"
"v_mfma_f32_16x16x32_bf16 v[80:83], a[24:27], v[164:167], v[80:83]\n"
"s_add_u32 s85, s85, 1\n"
"buffer_load_dwordx4 v27, s[48:51], s81 offen offset:2048 nt lds\n"
"v_mfma_f32_16x16x32_bf16 v[80:83], a[28:31], a[208:211], v[80:83]\n"
"s_min_u32 s85, s85, s73\n"
"ds_read_b64_tr_b16 a[20:21], v9 offset:57792\n"
"v_accvgpr_write_b32 a56, v96\n"
"v_mfma_f32_16x16x32_bf16 v[84:87], a[28:31], a[212:215], v[84:87]\n"
"s_lshl_b32 s85, s85, 11\n"
"ds_read_b64_tr_b16 a[22:23], v9 offset:59840\n"
"v_accvgpr_write_b32 a57, v98\n"
"v_mfma_f32_16x16x32_bf16 v[84:87], a[24:27], v[168:171], v[84:87]\n"
"s_add_u32 s94, s94, s85\n"
"ds_read_b128 v[104:107], v24 offset:1024\n"
"s_mul_i32 s85, s60, 3072\n"
"v_mfma_f32_16x16x32_bf16 v[92:95], a[24:27], v[176:179], v[92:95]\n"
"s_add_u32 s85, s85, s34\n"
"s_add_u32 s85, s85, 148608\n"
"s_add_u32 s82, s75, 6\n"
"v_mfma_f32_16x16x32_bf16 v[88:91], a[24:27], v[172:175], v[88:91]\n"
"ds_read_b64_tr_b16 a[24:25], v5 offset:61888\n"
"s_add_u32 s75, s75, -1\n"
"v_mfma_f32_16x16x32_bf16 v[88:91], a[28:31], a[216:219], v[88:91]\n"
"s_min_u32 s82, s82, s75\n"
"ds_read_b64_tr_b16 a[26:27], v5 offset:63936\n"
"v_subrev_u32 v5, s27, v5\n"
"v_mfma_f32_16x16x32_bf16 v[48:51], a[0:3], v[144:147], v[48:51]\n"
"s_add_u32 s82, s82, -1\n"
"ds_read_b64_tr_b16 a[28:29], v9 offset:61888\n"
"s_lshl_b32 s82, s82, 5\n"
"v_mfma_f32_16x16x32_bf16 v[48:51], a[4:7], a[200:203], v[48:51]\n"
"s_waitcnt lgkmcnt(3)\n"
"ds_read_b64_tr_b16 a[30:31], v9 offset:63936\n"
"v_subrev_u32 v9, s27, v9\n"
"v_mfma_f32_16x16x32_bf16 v[52:55], a[4:7], a[204:207], v[52:55]\n"
"s_add_u32 s73, s24, -2885632\n"
"v_fmac_f32 v100, v18, v104\n"
"v_fmac_f32 v101, v18, v105\n"
"v_mfma_f32_16x16x32_bf16 v[52:55], a[0:3], v[148:151], v[52:55]\n"
"s_andn2_b32 s73, s73, 1536\n"
"v_fmac_f32 v102, v18, v106\n"
"v_fmac_f32 v103, v18, v107\n"
"v_mfma_f32_16x16x32_bf16 v[60:63], a[0:3], v[156:159], v[60:63]\n"
"s_add_u32 s75, s24, -2885632\n"
"v_cvt_pk_bf16_f32 v100, v100, v101\n"
"v_cvt_pk_bf16_f32 v102, v102, v103\n"
"v_mfma_f32_16x16x32_bf16 v[56:59], a[0:3], v[152:155], v[56:59]\n"
"v_accvgpr_write_b32 a58, v100\n"
"v_accvgpr_write_b32 a59, v102\n"
"v_mfma_f32_16x16x32_bf16 v[56:59], a[4:7], v[160:163], v[56:59]\n"
"s_andn2_b32 s75, s75, 1536\n"
"s_add_u32 s71, s71, 1898496\n"
"s_add_u32 s72, s72, 1898496\n"
"v_mfma_f32_16x16x32_bf16 v[48:51], a[8:11], v[164:167], v[48:51]\n"
"s_add_u32 s94, s94, 1898496\n"
"buffer_load_dwordx4 v28, s[48:51], s81 offen offset:3072 nt lds\n"
"v_mfma_f32_16x16x32_bf16 v[48:51], a[12:15], a[208:211], v[48:51]\n"
"s_memrealtime s[2:3]\n"
"v_mfma_f32_16x16x32_bf16 v[52:55], a[12:15], a[212:215], v[52:55]\n"
"v_mfma_f32_16x16x32_bf16 v[52:55], a[8:11], v[168:171], v[52:55]\n"
"v_mfma_f32_16x16x32_bf16 v[60:63], a[8:11], v[176:179], v[60:63]\n"
"v_mfma_f32_16x16x32_bf16 v[56:59], a[8:11], v[172:175], v[56:59]\n"
"buffer_load_dwordx4 a[172:175], v28, s[48:51], s30 offen offset:3072 nt\n"
"v_mfma_f32_16x16x32_bf16 v[56:59], a[12:15], a[216:219], v[56:59]\n"
"s_add_u32 s30, s63, 33024\n"
"v_lshl_add_u32 v20, v1, 4, s30\n"
"v_mfma_f32_16x16x32_bf16 v[64:67], a[16:19], v[144:147], v[64:67]\n"
"v_mfma_f32_16x16x32_bf16 v[64:67], a[20:23], a[200:203], v[64:67]\n"
"s_waitcnt vmcnt(32) lgkmcnt(0)\n"
"v_mfma_f32_16x16x32_bf16 v[68:71], a[20:23], a[204:207], v[68:71]\n"
"s_barrier\n"
"v_mfma_f32_16x16x32_bf16 v[68:71], a[16:19], v[148:151], v[68:71]\n"
"s_addk_i32 s79, 63\n"
"ds_read_b64_tr_b16 a[0:1], v2 offset:33024\n"
"v_mfma_f32_16x16x32_bf16 v[76:79], a[16:19], v[156:159], v[76:79]\n"
"s_addk_i32 m0, 8256\n"
"ds_read_b64_tr_b16 a[2:3], v2 offset:35072\n"
"s_add_u32 s81, s65, 131456\n"
"v_mfma_f32_16x16x32_bf16 v[72:75], a[16:19], v[152:155], v[72:75]\n"
"ds_read_b64_tr_b16 a[4:5], v6 offset:33024\n"
"s_sub_u32 s0, s79, s2\n"
"v_mfma_f32_16x16x32_bf16 v[72:75], a[20:23], v[160:163], v[72:75]\n"
"ds_read_b64_tr_b16 a[6:7], v6 offset:35072\n"
"v_mfma_f32_16x16x32_bf16 v[64:67], a[24:27], v[164:167], v[64:67]\n"
"s_sub_u32 s0, s0, s23\n"
"buffer_load_dwordx4 v25, s[48:51], s81 offen offset:0 nt lds\n"
"v_mfma_f32_16x16x32_bf16 v[64:67], a[28:31], a[208:211], v[64:67]\n"
"ds_read_b64_tr_b16 a[8:9], v2 offset:37120\n"
"v_lshrrev_b32 v25, 5, v1\n"
"v_mfma_f32_16x16x32_bf16 v[68:71], a[28:31], a[212:215], v[68:71]\n"
"s_max_i32 s0, s0, 0\n"
"ds_read_b64_tr_b16 a[10:11], v2 offset:39168\n"
"v_mfma_f32_16x16x32_bf16 v[68:71], a[24:27], v[168:171], v[68:71]\n"
"ds_read_b64_tr_b16 a[12:13], v6 offset:37120\n"
"v_mfma_f32_16x16x32_bf16 v[76:79], a[24:27], v[176:179], v[76:79]\n"
"s_add_u32 s79, s2, s0\n"
"ds_read_b64_tr_b16 a[14:15], v6 offset:39168\n"
"v_mfma_f32_16x16x32_bf16 v[72:75], a[24:27], v[172:175], v[72:75]\n"
"s_waitcnt lgkmcnt(4)\n"
"ds_read_b64_tr_b16 a[16:17], v3 offset:33024\n"
"v_mfma_f32_16x16x32_bf16 v[72:75], a[28:31], a[216:219], v[72:75]\n"
"s_lshr_b64 vcc, s[0:1], 2\n"
"ds_read_b64_tr_b16 a[18:19], v3 offset:35072\n"
"v_mfma_f32_16x16x32_bf16 v[32:35], a[0:3], v[180:183], v[32:35]\n"
"ds_read_b64_tr_b16 a[20:21], v7 offset:33024\n"
"v_mfma_f32_16x16x32_bf16 v[32:35], a[4:7], a[220:223], v[32:35]\n"
"s_cbranch_vccnz PC36_%=\n"
"PB36_%=:\n"
"v_mfma_f32_16x16x32_bf16 v[36:39], a[4:7], a[224:227], v[36:39]\n"
"ds_read_b64_tr_b16 a[22:23], v7 offset:35072\n"
"v_mfma_f32_16x16x32_bf16 v[36:39], a[0:3], v[184:187], v[36:39]\n"
"ds_read_b64_tr_b16 a[24:25], v3 offset:37120\n"
"v_mfma_f32_16x16x32_bf16 v[44:47], a[0:3], v[192:195], v[44:47]\n"
"ds_read_b64_tr_b16 a[26:27], v3 offset:39168\n"
"v_mfma_f32_16x16x32_bf16 v[40:43], a[0:3], v[188:191], v[40:43]\n"
"s_waitcnt lgkmcnt(5)\n"
"ds_read_b64_tr_b16 a[28:29], v7 offset:37120\n"
"v_mfma_f32_16x16x32_bf16 v[40:43], a[4:7], v[196:199], v[40:43]\n"
"ds_read_b64_tr_b16 a[30:31], v7 offset:39168\n"
"v_mfma_f32_16x16x32_bf16 v[32:35], a[8:11], v[200:203], v[32:35]\n"
"buffer_load_dwordx4 v26, s[48:51], s81 offen offset:1024 nt lds\n"
"v_mfma_f32_16x16x32_bf16 v[32:35], a[12:15], a[228:231], v[32:35]\n"
"ds_read_b64_tr_b16 a[0:1], v4 offset:33024\n"
"v_mfma_f32_16x16x32_bf16 v[36:39], a[12:15], a[232:235], v[36:39]\n"
"ds_read_b64_tr_b16 a[2:3], v4 offset:35072\n"
"v_mfma_f32_16x16x32_bf16 v[36:39], a[8:11], v[204:207], v[36:39]\n"
"ds_read_b64_tr_b16 a[4:5], v8 offset:33024\n"
"v_mfma_f32_16x16x32_bf16 v[44:47], a[8:11], v[212:215], v[44:47]\n"
"ds_read_b64_tr_b16 a[6:7], v8 offset:35072\n"
"v_mfma_f32_16x16x32_bf16 v[40:43], a[8:11], v[208:211], v[40:43]\n"
"s_waitcnt lgkmcnt(5)\n"
"ds_read_b64_tr_b16 a[8:9], v4 offset:37120\n"
"v_mfma_f32_16x16x32_bf16 v[40:43], a[12:15], a[236:239], v[40:43]\n"
"ds_read_b64_tr_b16 a[10:11], v4 offset:39168\n"
"v_mfma_f32_16x16x32_bf16 v[80:83], a[16:19], v[180:183], v[80:83]\n"
"ds_read_b64_tr_b16 a[12:13], v8 offset:37120\n"
"v_mfma_f32_16x16x32_bf16 v[80:83], a[20:23], a[220:223], v[80:83]\n"
"ds_read_b64_tr_b16 a[14:15], v8 offset:39168\n"
"v_mfma_f32_16x16x32_bf16 v[84:87], a[20:23], a[224:227], v[84:87]\n"
"PG10_%=:\n"
"s_cbranch_execz PG14_%=\n"
"v_mfma_f32_16x16x32_bf16 v[84:87], a[16:19], v[184:187], v[84:87]\n"
"v_mfma_f32_16x16x32_bf16 v[92:95], a[16:19], v[192:195], v[92:95]\n"
"v_mfma_f32_16x16x32_bf16 v[88:91], a[16:19], v[188:191], v[88:91]\n"
"s_waitcnt lgkmcnt(3)\n"
"ds_read_b64_tr_b16 a[16:17], v5 offset:33024\n"
"v_mfma_f32_16x16x32_bf16 v[88:91], a[20:23], v[196:199], v[88:91]\n"
"ds_read_b64_tr_b16 a[18:19], v5 offset:35072\n"
"v_mfma_f32_16x16x32_bf16 v[80:83], a[24:27], v[200:203], v[80:83]\n"
"buffer_load_dwordx4 v27, s[48:51], s81 offen offset:2048 nt lds\n"
"v_mfma_f32_16x16x32_bf16 v[80:83], a[28:31], a[228:231], v[80:83]\n"
"ds_read_b64_tr_b16 a[20:21], v9 offset:33024\n"
"v_mul_u32_u24 v27, 144, v10\n"
"v_mfma_f32_16x16x32_bf16 v[84:87], a[28:31], a[232:235], v[84:87]\n"
"ds_read_b64_tr_b16 a[22:23], v9 offset:35072\n"
"v_mfma_f32_16x16x32_bf16 v[84:87], a[24:27], v[204:207], v[84:87]\n"
"v_mfma_f32_16x16x32_bf16 v[92:95], a[24:27], v[212:215], v[92:95]\n"
"v_mfma_f32_16x16x32_bf16 v[88:91], a[24:27], v[208:211], v[88:91]\n"
"ds_read_b64_tr_b16 a[24:25], v5 offset:37120\n"
"v_mfma_f32_16x16x32_bf16 v[88:91], a[28:31], a[236:239], v[88:91]\n"
"ds_read_b64_tr_b16 a[26:27], v5 offset:39168\n"
"v_mfma_f32_16x16x32_bf16 v[48:51], a[0:3], v[180:183], v[48:51]\n"
"ds_read_b64_tr_b16 a[28:29], v9 offset:37120\n"
"v_mfma_f32_16x16x32_bf16 v[48:51], a[4:7], a[220:223], v[48:51]\n"
"ds_read_b64_tr_b16 a[30:31], v9 offset:39168\n"
"v_mfma_f32_16x16x32_bf16 v[52:55], a[4:7], a[224:227], v[52:55]\n"
"v_mfma_f32_16x16x32_bf16 v[52:55], a[0:3], v[184:187], v[52:55]\n"
"v_mfma_f32_16x16x32_bf16 v[60:63], a[0:3], v[192:195], v[60:63]\n"
"v_mfma_f32_16x16x32_bf16 v[56:59], a[0:3], v[188:191], v[56:59]\n"
"s_waitcnt lgkmcnt(3)\n"
"v_mfma_f32_16x16x32_bf16 v[56:59], a[4:7], v[196:199], v[56:59]\n"
"v_mfma_f32_16x16x32_bf16 v[48:51], a[8:11], v[200:203], v[48:51]\n"
"v_mfma_f32_16x16x32_bf16 v[48:51], a[12:15], a[228:231], v[48:51]\n"
"s_memrealtime s[2:3]\n"
"buffer_load_dwordx4 v28, s[48:51], s81 offen offset:3072 nt lds\n"
"v_mfma_f32_16x16x32_bf16 v[52:55], a[12:15], a[232:235], v[52:55]\n"
"v_mfma_f32_16x16x32_bf16 v[52:55], a[8:11], v[204:207], v[52:55]\n"
"v_mfma_f32_16x16x32_bf16 v[60:63], a[8:11], v[212:215], v[60:63]\n"
"v_mfma_f32_16x16x32_bf16 v[56:59], a[8:11], v[208:211], v[56:59]\n"
"v_mfma_f32_16x16x32_bf16 v[56:59], a[12:15], a[236:239], v[56:59]\n"
"v_mfma_f32_16x16x32_bf16 v[64:67], a[16:19], v[180:183], v[64:67]\n"
"v_mfma_f32_16x16x32_bf16 v[64:67], a[20:23], a[220:223], v[64:67]\n"
"s_waitcnt vmcnt(32) lgkmcnt(0)\n"
"v_mfma_f32_16x16x32_bf16 v[68:71], a[20:23], a[224:227], v[68:71]\n"
"s_barrier\n"
"v_mfma_f32_16x16x32_bf16 v[68:71], a[16:19], v[184:187], v[68:71]\n"
"s_addk_i32 s79, 63\n"
"ds_read_b64_tr_b16 a[0:1], v2 offset:0\n"
"v_mfma_f32_16x16x32_bf16 v[76:79], a[16:19], v[192:195], v[76:79]\n"
"ds_read_b64_tr_b16 a[2:3], v2 offset:2048\n"
"v_mfma_f32_16x16x32_bf16 v[72:75], a[16:19], v[188:191], v[72:75]\n"
"ds_read_b64_tr_b16 a[4:5], v6 offset:0\n"
"s_sub_u32 s0, s79, s2\n"
"v_mfma_f32_16x16x32_bf16 v[72:75], a[20:23], v[196:199], v[72:75]\n"
"ds_read_b64_tr_b16 a[6:7], v6 offset:2048\n"
"v_mfma_f32_16x16x32_bf16 v[64:67], a[24:27], v[200:203], v[64:67]\n"
"s_sub_u32 s0, s0, s23\n"
"ds_read_b64_tr_b16 a[8:9], v2 offset:4096\n"
"v_mfma_f32_16x16x32_bf16 v[64:67], a[28:31], a[228:231], v[64:67]\n"
"ds_read_b64_tr_b16 a[10:11], v2 offset:6144\n"
"v_mfma_f32_16x16x32_bf16 v[68:71], a[28:31], a[232:235], v[68:71]\n"
"s_max_i32 s0, s0, 0\n"
"ds_read_b64_tr_b16 a[12:13], v6 offset:4096\n"
"v_mfma_f32_16x16x32_bf16 v[68:71], a[24:27], v[204:207], v[68:71]\n"
"ds_read_b64_tr_b16 a[14:15], v6 offset:6144\n"
"v_mfma_f32_16x16x32_bf16 v[76:79], a[24:27], v[212:215], v[76:79]\n"
"s_add_u32 s79, s2, s0\n"
"ds_read_b64_tr_b16 a[16:17], v3 offset:0\n"
"v_mfma_f32_16x16x32_bf16 v[72:75], a[24:27], v[208:211], v[72:75]\n"
"s_waitcnt lgkmcnt(5)\n"
"ds_read_b64_tr_b16 a[18:19], v3 offset:2048\n"
"v_mfma_f32_16x16x32_bf16 v[72:75], a[28:31], a[236:239], v[72:75]\n"
"s_lshr_b64 vcc, s[0:1], 2\n"
"ds_read_b64_tr_b16 a[20:21], v7 offset:0\n"
"v_mfma_f32_16x16x32_bf16 v[32:35], a[0:3], v[216:219], v[32:35]\n"
"ds_read_b64_tr_b16 a[22:23], v7 offset:2048\n"
"v_mfma_f32_16x16x32_bf16 v[32:35], a[4:7], a[240:243], v[32:35]\n"
"s_cbranch_vccnz PC37_%=\n"
"PB37_%=:\n"
"v_mfma_f32_16x16x32_bf16 v[36:39], a[4:7], a[244:247], v[36:39]\n"
"ds_read_b64_tr_b16 a[24:25], v3 offset:4096\n"
"v_mfma_f32_16x16x32_bf16 v[36:39], a[0:3], v[220:223], v[36:39]\n"
"ds_read_b64_tr_b16 a[26:27], v3 offset:6144\n"
"v_mfma_f32_16x16x32_bf16 v[44:47], a[0:3], v[228:231], v[44:47]\n"
"ds_read_b64_tr_b16 a[28:29], v7 offset:4096\n"
"v_mfma_f32_16x16x32_bf16 v[40:43], a[0:3], v[224:227], v[40:43]\n"
"s_waitcnt lgkmcnt(5)\n"
"ds_read_b64_tr_b16 a[30:31], v7 offset:6144\n"
"v_mfma_f32_16x16x32_bf16 v[40:43], a[4:7], v[232:235], v[40:43]\n"
"ds_read_b64_tr_b16 a[0:1], v4 offset:0\n"
"v_mfma_f32_16x16x32_bf16 v[32:35], a[8:11], v[236:239], v[32:35]\n"
"ds_read_b64_tr_b16 a[2:3], v4 offset:2048\n"
"v_mfma_f32_16x16x32_bf16 v[32:35], a[12:15], a[248:251], v[32:35]\n"
"ds_read_b64_tr_b16 a[4:5], v8 offset:0\n"
"v_mfma_f32_16x16x32_bf16 v[36:39], a[12:15], a[252:255], v[36:39]\n"
"ds_read_b64_tr_b16 a[6:7], v8 offset:2048\n"
"v_mfma_f32_16x16x32_bf16 v[36:39], a[8:11], v[240:243], v[36:39]\n"
"v_mfma_f32_16x16x32_bf16 v[44:47], a[8:11], v[248:251], v[44:47]\n"
"v_mfma_f32_16x16x32_bf16 v[40:43], a[8:11], v[244:247], v[40:43]\n"
"s_waitcnt lgkmcnt(4)\n"
"ds_read_b64_tr_b16 a[8:9], v4 offset:4096\n"
"v_mfma_f32_16x16x32_bf16 v[40:43], a[12:15], v[252:255], v[40:43]\n"
"ds_read_b64_tr_b16 a[10:11], v4 offset:6144\n"
"v_mul_f32 v32, v15, v32\n"
"v_mfma_f32_16x16x32_bf16 v[80:83], a[16:19], v[216:219], v[80:83]\n"
"ds_read_b64_tr_b16 a[12:13], v8 offset:4096\n"
"v_mul_f32 v33, v15, v33\n"
"v_mfma_f32_16x16x32_bf16 v[80:83], a[20:23], a[240:243], v[80:83]\n"
"ds_read_b64_tr_b16 a[14:15], v8 offset:6144\n"
"v_mul_f32 v34, v15, v34\n"
"v_mfma_f32_16x16x32_bf16 v[84:87], a[20:23], a[244:247], v[84:87]\n"
"v_mul_f32 v35, v15, v35\n"
"v_cvt_pk_bf16_f32 v32, v32, v33\n"
"v_mfma_f32_16x16x32_bf16 v[84:87], a[16:19], v[220:223], v[84:87]\n"
"v_cvt_pk_bf16_f32 v34, v34, v35\n"
"v_accvgpr_write_b32 a60, v32\n"
"v_mfma_f32_16x16x32_bf16 v[92:95], a[16:19], v[228:231], v[92:95]\n"
"v_accvgpr_write_b32 a61, v34\n"
"v_mul_f32 v36, v16, v36\n"
"v_mfma_f32_16x16x32_bf16 v[88:91], a[16:19], v[224:227], v[88:91]\n"
"ds_read_b64_tr_b16 a[16:17], v5 offset:0\n"
"v_mul_f32 v37, v16, v37\n"
"v_mfma_f32_16x16x32_bf16 v[88:91], a[20:23], v[232:235], v[88:91]\n"
"ds_read_b64_tr_b16 a[18:19], v5 offset:2048\n"
"v_mul_f32 v38, v16, v38\n"
"v_mfma_f32_16x16x32_bf16 v[80:83], a[24:27], v[236:239], v[80:83]\n"
"ds_read_b64_tr_b16 a[20:21], v9 offset:0\n"
"v_mul_f32 v39, v16, v39\n"
"v_mfma_f32_16x16x32_bf16 v[80:83], a[28:31], a[248:251], v[80:83]\n"
"ds_read_b64_tr_b16 a[22:23], v9 offset:2048\n"
"v_cvt_pk_bf16_f32 v36, v36, v37\n"
"v_mfma_f32_16x16x32_bf16 v[84:87], a[28:31], a[252:255], v[84:87]\n"
"v_cvt_pk_bf16_f32 v38, v38, v39\n"
"v_accvgpr_write_b32 a68, v36\n"
"v_mfma_f32_16x16x32_bf16 v[84:87], a[24:27], v[240:243], v[84:87]\n"
"v_accvgpr_write_b32 a69, v38\n"
"v_mul_f32 v40, v17, v40\n"
"v_mfma_f32_16x16x32_bf16 v[92:95], a[24:27], v[248:251], v[92:95]\n"
"v_mul_f32 v41, v17, v41\n"
"v_mul_f32 v42, v17, v42\n"
"v_mfma_f32_16x16x32_bf16 v[88:91], a[24:27], v[244:247], v[88:91]\n"
"s_waitcnt lgkmcnt(3)\n"
"ds_read_b64_tr_b16 a[24:25], v5 offset:4096\n"
"v_mul_f32 v43, v17, v43\n"
"v_mfma_f32_16x16x32_bf16 v[88:91], a[28:31], v[252:255], v[88:91]\n"
"ds_read_b64_tr_b16 a[26:27], v5 offset:6144\n"
"v_mul_f32 v96, v18, v44\n"
"v_mfma_f32_16x16x32_bf16 v[48:51], a[0:3], v[216:219], v[48:51]\n"
"ds_read_b64_tr_b16 a[28:29], v9 offset:4096\n"
"v_mul_f32 v97, v18, v45\n"
"v_mfma_f32_16x16x32_bf16 v[48:51], a[4:7], a[240:243], v[48:51]\n"
"ds_read_b64_tr_b16 a[30:31], v9 offset:6144\n"
"v_mul_f32 v98, v18, v46\n"
"v_mfma_f32_16x16x32_bf16 v[52:55], a[4:7], a[244:247], v[52:55]\n"
"v_mul_f32 v99, v18, v47\n"
"v_cvt_pk_bf16_f32 v40, v40, v41\n"
"v_mfma_f32_16x16x32_bf16 v[52:55], a[0:3], v[220:223], v[52:55]\n"
"v_cvt_pk_bf16_f32 v42, v42, v43\n"
"v_accvgpr_write_b32 a76, v40\n"
"v_mfma_f32_16x16x32_bf16 v[60:63], a[0:3], v[228:231], v[60:63]\n"
"v_accvgpr_write_b32 a77, v42\n"
"v_mul_f32 v80, v15, v80\n"
"v_mfma_f32_16x16x32_bf16 v[56:59], a[0:3], v[224:227], v[56:59]\n"
"v_mul_f32 v81, v15, v81\n"
"v_mul_f32 v82, v15, v82\n"
"v_mfma_f32_16x16x32_bf16 v[56:59], a[4:7], v[232:235], v[56:59]\n"
"v_mul_f32 v83, v15, v83\n"
"v_cvt_pk_bf16_f32 v80, v80, v81\n"
"v_mfma_f32_16x16x32_bf16 v[48:51], a[8:11], v[236:239], v[48:51]\n"
"v_cvt_pk_bf16_f32 v82, v82, v83\n"
"v_accvgpr_write_b32 a62, v80\n"
"v_mfma_f32_16x16x32_bf16 v[48:51], a[12:15], a[248:251], v[48:51]\n"
"s_memrealtime s[2:3]\n"
"v_accvgpr_write_b32 a63, v82\n"
"v_mul_f32 v84, v16, v84\n"
"v_mfma_f32_16x16x32_bf16 v[52:55], a[12:15], a[252:255], v[52:55]\n"
"v_mul_f32 v85, v16, v85\n"
"v_mul_f32 v86, v16, v86\n"
"v_mfma_f32_16x16x32_bf16 v[52:55], a[8:11], v[240:243], v[52:55]\n"
"v_mul_f32 v87, v16, v87\n"
"v_cvt_pk_bf16_f32 v84, v84, v85\n"
"v_mfma_f32_16x16x32_bf16 v[60:63], a[8:11], v[248:251], v[60:63]\n"
"v_cvt_pk_bf16_f32 v86, v86, v87\n"
"v_accvgpr_write_b32 a70, v84\n"
"v_mfma_f32_16x16x32_bf16 v[56:59], a[8:11], v[244:247], v[56:59]\n"
"s_waitcnt lgkmcnt(4)\n"
"v_accvgpr_write_b32 a71, v86\n"
"v_mul_f32 v88, v17, v88\n"
"v_mfma_f32_16x16x32_bf16 v[56:59], a[12:15], v[252:255], v[56:59]\n"
"v_mul_f32 v89, v17, v89\n"
"v_mul_f32 v90, v17, v90\n"
"v_mfma_f32_16x16x32_bf16 v[64:67], a[16:19], v[216:219], v[64:67]\n"
"ds_write_b128 v23, v[60:63] offset:0\n"
"v_mfma_f32_16x16x32_bf16 v[64:67], a[20:23], a[240:243], v[64:67]\n"
"s_waitcnt vmcnt(28) lgkmcnt(0)\n"
"v_mul_f32 v91, v17, v91\n"
"v_mul_f32 v100, v18, v92\n"
"v_mfma_f32_16x16x32_bf16 v[68:71], a[20:23], a[244:247], v[68:71]\n"
"s_barrier\n"
"v_mfma_f32_16x16x32_bf16 v[68:71], a[16:19], v[220:223], v[68:71]\n"
"s_add_u32 m0, s63, 0\n"
"ds_read_b64_tr_b16 a[0:1], v2 offset:8256\n"
"v_mul_f32 v101, v18, v93\n"
"v_mfma_f32_16x16x32_bf16 v[76:79], a[16:19], v[228:231], v[76:79]\n"
"s_add_u32 s29, s66, 0\n"
"ds_read_b64_tr_b16 a[2:3], v2 offset:10304\n"
"v_mul_f32 v102, v18, v94\n"
"v_mfma_f32_16x16x32_bf16 v[72:75], a[16:19], v[224:227], v[72:75]\n"
"ds_read_b64_tr_b16 a[4:5], v6 offset:8256\n"
"v_mul_f32 v103, v18, v95\n"
"v_mfma_f32_16x16x32_bf16 v[72:75], a[20:23], v[232:235], v[72:75]\n"
"s_addk_i32 s79, 68\n"
"ds_read_b64_tr_b16 a[6:7], v6 offset:10304\n"
"v_cvt_pk_bf16_f32 v88, v88, v89\n"
"v_mfma_f32_16x16x32_bf16 v[64:67], a[24:27], v[236:239], v[64:67]\n"
"buffer_load_dwordx4 v11, s[36:39], s29 offen offset:0 lds\n"
"v_mfma_f32_16x16x32_bf16 v[64:67], a[28:31], a[248:251], v[64:67]\n"
"s_sub_u32 s0, s79, s2\n"
"ds_read_b64_tr_b16 a[8:9], v2 offset:12352\n"
"v_cvt_pk_bf16_f32 v90, v90, v91\n"
"v_mfma_f32_16x16x32_bf16 v[68:71], a[28:31], a[252:255], v[68:71]\n"
"ds_read_b64_tr_b16 a[10:11], v2 offset:14400\n"
"v_accvgpr_write_b32 a78, v88\n"
"v_mfma_f32_16x16x32_bf16 v[68:71], a[24:27], v[240:243], v[68:71]\n"
"s_sub_u32 s0, s0, s23\n"
"ds_read_b64_tr_b16 a[12:13], v6 offset:12352\n"
"v_accvgpr_write_b32 a79, v90\n"
"v_mfma_f32_16x16x32_bf16 v[76:79], a[24:27], v[248:251], v[76:79]\n"
"ds_read_b64_tr_b16 a[14:15], v6 offset:14400\n"
"v_mul_f32 v48, v15, v48\n"
"v_mfma_f32_16x16x32_bf16 v[72:75], a[24:27], v[244:247], v[72:75]\n"
"s_waitcnt lgkmcnt(4)\n"
"ds_read_b64_tr_b16 a[16:17], v3 offset:8256\n"
"v_mul_f32 v49, v15, v49\n"
"v_mfma_f32_16x16x32_bf16 v[72:75], a[28:31], v[252:255], v[72:75]\n"
"s_sub_u32 s79, s77, s2\n"
"ds_read_b64_tr_b16 a[18:19], v3 offset:10304\n"
"v_mul_f32 v50, v15, v50\n"
"v_mfma_f32_16x16x32_bf16 v[32:35], a[0:3], v[108:111], 0\n"
"s_add_u32 s77, s24, -2885632\n"
"ds_read_b64_tr_b16 a[20:21], v7 offset:8256\n"
"v_mul_f32 v51, v15, v51\n"
"v_mfma_f32_16x16x32_bf16 v[32:35], a[4:7], a[180:183], v[32:35]\n"
"s_addk_i32 s79, 2726\n"
"ds_read_b64_tr_b16 a[22:23], v7 offset:10304\n"
"v_cvt_pk_bf16_f32 v48, v48, v49\n"
"v_mfma_f32_16x16x32_bf16 v[36:39], a[4:7], a[184:187], 0\n"
"s_andn2_b32 s77, s77, 1536\n"
"ds_read_b64_tr_b16 a[24:25], v3 offset:12352\n"
"v_cvt_pk_bf16_f32 v50, v50, v51\n"
"v_mfma_f32_16x16x32_bf16 v[36:39], a[0:3], v[112:115], v[36:39]\n"
"s_max_i32 s0, s0, s79\n"
"ds_read_b64_tr_b16 a[26:27], v3 offset:14400\n"
"v_accvgpr_write_b32 a64, v48\n"
"v_mfma_f32_16x16x32_bf16 v[44:47], a[0:3], v[120:123], 0\n"
"ds_read_b64_tr_b16 a[28:29], v7 offset:12352\n"
"v_accvgpr_write_b32 a65, v50\n"
"v_mfma_f32_16x16x32_bf16 v[40:43], a[0:3], v[116:119], 0\n"
"s_waitcnt lgkmcnt(6)\n"
"buffer_load_dwordx4 v12, s[36:39], s29 offen offset:1024 lds\n"
"v_mfma_f32_16x16x32_bf16 v[40:43], a[4:7], v[124:127], v[40:43]\n"
"s_min_i32 s0, s0, 40\n"
"ds_read_b64_tr_b16 a[30:31], v7 offset:14400\n"
"v_mul_f32 v52, v16, v52\n"
"v_mfma_f32_16x16x32_bf16 v[32:35], a[8:11], v[128:131], v[32:35]\n"
"ds_read_b64_tr_b16 a[0:1], v4 offset:8256\n"
"v_mul_f32 v53, v16, v53\n"
"v_mfma_f32_16x16x32_bf16 v[32:35], a[12:15], a[188:191], v[32:35]\n"
"s_max_i32 s0, s0, 0\n"
"ds_read_b64_tr_b16 a[2:3], v4 offset:10304\n"
"v_mul_f32 v54, v16, v54\n"
"v_mfma_f32_16x16x32_bf16 v[36:39], a[12:15], a[192:195], v[36:39]\n"
"ds_read_b64_tr_b16 a[4:5], v8 offset:8256\n"
"v_mul_f32 v55, v16, v55\n"
"v_mfma_f32_16x16x32_bf16 v[36:39], a[8:11], v[132:135], v[36:39]\n"
"s_add_u32 s79, s2, s0\n"
"ds_read_b64_tr_b16 a[6:7], v8 offset:10304\n"
"v_cvt_pk_bf16_f32 v52, v52, v53\n"
"v_mfma_f32_16x16x32_bf16 v[44:47], a[8:11], v[140:143], v[44:47]\n"
"ds_write_b128 v23, v[76:79] offset:1024\n"
"v_mfma_f32_16x16x32_bf16 v[40:43], a[8:11], v[136:139], v[40:43]\n"
"s_waitcnt lgkmcnt(6)\n"
"ds_read_b64_tr_b16 a[8:9], v4 offset:12352\n"
"v_cvt_pk_bf16_f32 v54, v54, v55\n"
"v_mfma_f32_16x16x32_bf16 v[40:43], a[12:15], a[196:199], v[40:43]\n"
"s_lshr_b64 vcc, s[0:1], 2\n"
"ds_read_b64_tr_b16 a[10:11], v4 offset:14400\n"
"v_accvgpr_write_b32 a72, v52\n"
"v_mfma_f32_16x16x32_bf16 v[80:83], a[16:19], v[108:111], 0\n"
"ds_read_b64_tr_b16 a[12:13], v8 offset:12352\n"
"v_accvgpr_write_b32 a73, v54\n"
"v_mfma_f32_16x16x32_bf16 v[80:83], a[20:23], a[180:183], v[80:83]\n"
"s_cbranch_vccnz PC38_%=\n"
"PB38_%=:\n"
"v_mul_f32 v56, v17, v56\n"
"v_mfma_f32_16x16x32_bf16 v[84:87], a[20:23], a[184:187], 0\n"
"ds_read_b64_tr_b16 a[14:15], v8 offset:14400\n"
"v_mul_f32 v57, v17, v57\n"
"v_mfma_f32_16x16x32_bf16 v[84:87], a[16:19], v[112:115], v[84:87]\n"
"v_mul_f32 v58, v17, v58\n"
"v_mul_f32 v59, v17, v59\n"
"v_mfma_f32_16x16x32_bf16 v[92:95], a[16:19], v[120:123], 0\n"
"buffer_load_dwordx4 v13, s[36:39], s29 offen offset:2048 lds\n"
"v_mfma_f32_16x16x32_bf16 v[88:91], a[16:19], v[116:119], 0\n"
"s_waitcnt lgkmcnt(3)\n"
"ds_read_b64_tr_b16 a[16:17], v5 offset:8256\n"
"v_cvt_pk_bf16_f32 v56, v56, v57\n"
"v_mfma_f32_16x16x32_bf16 v[88:91], a[20:23], v[124:127], v[88:91]\n"
"ds_read_b64_tr_b16 a[18:19], v5 offset:10304\n"
"v_cvt_pk_bf16_f32 v58, v58, v59\n"
"v_mfma_f32_16x16x32_bf16 v[80:83], a[24:27], v[128:131], v[80:83]\n"
"ds_read_b64_tr_b16 a[20:21], v9 offset:8256\n"
"v_accvgpr_write_b32 a80, v56\n"
"v_mfma_f32_16x16x32_bf16 v[80:83], a[28:31], a[188:191], v[80:83]\n"
"ds_read_b64_tr_b16 a[22:23], v9 offset:10304\n"
"v_accvgpr_write_b32 a81, v58\n"
"v_mfma_f32_16x16x32_bf16 v[84:87], a[28:31], a[192:195], v[84:87]\n"
"v_mul_f32 v64, v15, v64\n"
"v_mul_f32 v65, v15, v65\n"
"v_mfma_f32_16x16x32_bf16 v[84:87], a[24:27], v[132:135], v[84:87]\n"
"v_mul_f32 v66, v15, v66\n"
"v_mul_f32 v67, v15, v67\n"
"v_mfma_f32_16x16x32_bf16 v[92:95], a[24:27], v[140:143], v[92:95]\n"
"s_add_u32 s93, s56, 0\n"
"v_cvt_pk_bf16_f32 v64, v64, v65\n"
"v_cvt_pk_bf16_f32 v66, v66, v67\n"
"v_mfma_f32_16x16x32_bf16 v[88:91], a[24:27], v[136:139], v[88:91]\n"
"ds_read_b64_tr_b16 a[24:25], v5 offset:12352\n"
"v_accvgpr_write_b32 a66, v64\n"
"v_mfma_f32_16x16x32_bf16 v[88:91], a[28:31], a[196:199], v[88:91]\n"
"ds_read_b64_tr_b16 a[26:27], v5 offset:14400\n"
"v_accvgpr_write_b32 a67, v66\n"
"v_mfma_f32_16x16x32_bf16 v[48:51], a[0:3], v[108:111], 0\n"
"buffer_store_dwordx4 a[32:35], v19, s[52:55], s93 offen offset:0 sc1\n"
"v_mfma_f32_16x16x32_bf16 v[48:51], a[4:7], a[180:183], v[48:51]\n"
"ds_read_b64_tr_b16 a[28:29], v9 offset:12352\n"
"v_mul_f32 v68, v16, v68\n"
"v_mfma_f32_16x16x32_bf16 v[52:55], a[4:7], a[184:187], 0\n"
"ds_read_b64_tr_b16 a[30:31], v9 offset:14400\n"
"v_mul_f32 v69, v16, v69\n"
"v_mfma_f32_16x16x32_bf16 v[52:55], a[0:3], v[112:115], v[52:55]\n"
"v_mul_f32 v70, v16, v70\n"
"v_mul_f32 v71, v16, v71\n"
"v_mfma_f32_16x16x32_bf16 v[60:63], a[0:3], v[120:123], 0\n"
"buffer_load_dwordx4 v14, s[36:39], s29 offen offset:3072 lds\n"
"v_mfma_f32_16x16x32_bf16 v[56:59], a[0:3], v[116:119], 0\n"
"s_waitcnt lgkmcnt(3)\n"
"v_cvt_pk_bf16_f32 v68, v68, v69\n"
"v_cvt_pk_bf16_f32 v70, v70, v71\n"
"v_mfma_f32_16x16x32_bf16 v[56:59], a[4:7], v[124:127], v[56:59]\n"
"v_accvgpr_write_b32 a74, v68\n"
"v_accvgpr_write_b32 a75, v70\n"
"v_mfma_f32_16x16x32_bf16 v[48:51], a[8:11], v[128:131], v[48:51]\n"
"v_mul_f32 v72, v17, v72\n"
"v_mul_f32 v73, v17, v73\n"
"v_mfma_f32_16x16x32_bf16 v[48:51], a[12:15], a[188:191], v[48:51]\n"
"v_mul_f32 v74, v17, v74\n"
"v_mul_f32 v75, v17, v75\n"
"v_mfma_f32_16x16x32_bf16 v[52:55], a[12:15], a[192:195], v[52:55]\n"
"v_cvt_pk_bf16_f32 v72, v72, v73\n"
"v_cvt_pk_bf16_f32 v74, v74, v75\n"
"v_mfma_f32_16x16x32_bf16 v[52:55], a[8:11], v[132:135], v[52:55]\n"
"v_accvgpr_write_b32 a82, v72\n"
"v_accvgpr_write_b32 a83, v74\n"
"v_mfma_f32_16x16x32_bf16 v[60:63], a[8:11], v[140:143], v[60:63]\n"
"v_mfma_f32_16x16x32_bf16 v[56:59], a[8:11], v[136:139], v[56:59]\n"
"v_mfma_f32_16x16x32_bf16 v[56:59], a[12:15], a[196:199], v[56:59]\n"
"s_addk_i32 s93, 4096\n"
"v_mfma_f32_16x16x32_bf16 v[64:67], a[16:19], v[108:111], 0\n"
"v_mfma_f32_16x16x32_bf16 v[64:67], a[20:23], a[180:183], v[64:67]\n"
"s_waitcnt vmcnt(29) lgkmcnt(0)\n"
"v_mfma_f32_16x16x32_bf16 v[68:71], a[20:23], a[184:187], 0\n"
"s_barrier\n"
"v_mfma_f32_16x16x32_bf16 v[68:71], a[16:19], v[112:115], v[68:71]\n"
"s_addk_i32 m0, 8256\n"
"buffer_store_dwordx4 a[36:39], v19, s[52:55], s93 offen offset:0 sc1\n"
"v_mfma_f32_16x16x32_bf16 v[76:79], a[16:19], v[120:123], 0\n"
"s_add_u32 s29, s66, 57344\n"
"ds_read_b64_tr_b16 a[0:1], v2 offset:16512\n"
"v_mfma_f32_16x16x32_bf16 v[72:75], a[16:19], v[116:119], 0\n"
"ds_read_b64_tr_b16 a[2:3], v2 offset:18560\n"
"v_mfma_f32_16x16x32_bf16 v[72:75], a[20:23], v[124:127], v[72:75]\n"
"ds_read_b64_tr_b16 a[4:5], v6 offset:16512\n"
"v_mfma_f32_16x16x32_bf16 v[64:67], a[24:27], v[128:131], v[64:67]\n"
"ds_read_b64_tr_b16 a[6:7], v6 offset:18560\n"
"v_mfma_f32_16x16x32_bf16 v[64:67], a[28:31], a[188:191], v[64:67]\n"
"buffer_load_dwordx4 v11, s[36:39], s29 offen offset:0 lds\n"
"v_mfma_f32_16x16x32_bf16 v[68:71], a[28:31], a[192:195], v[68:71]\n"
"ds_read_b64_tr_b16 a[8:9], v2 offset:20608\n"
"v_mfma_f32_16x16x32_bf16 v[68:71], a[24:27], v[132:135], v[68:71]\n"
"ds_read_b64_tr_b16 a[10:11], v2 offset:22656\n"
"v_mfma_f32_16x16x32_bf16 v[76:79], a[24:27], v[140:143], v[76:79]\n"
"ds_read_b64_tr_b16 a[12:13], v6 offset:20608\n"
"v_mfma_f32_16x16x32_bf16 v[72:75], a[24:27], v[136:139], v[72:75]\n"
"s_waitcnt lgkmcnt(3)\n"
"ds_read_b64_tr_b16 a[14:15], v6 offset:22656\n"
"v_mfma_f32_16x16x32_bf16 v[72:75], a[28:31], a[196:199], v[72:75]\n"
"ds_read_b64_tr_b16 a[16:17], v3 offset:16512\n"
"v_mfma_f32_16x16x32_bf16 v[32:35], a[0:3], v[144:147], v[32:35]\n"
"ds_read_b64_tr_b16 a[18:19], v3 offset:18560\n"
"v_mfma_f32_16x16x32_bf16 v[32:35], a[4:7], a[200:203], v[32:35]\n"
"s_addk_i32 s93, 4096\n"
"ds_read_b64_tr_b16 a[20:21], v7 offset:16512\n"
"v_mfma_f32_16x16x32_bf16 v[36:39], a[4:7], a[204:207], v[36:39]\n"
"ds_read_b64_tr_b16 a[22:23], v7 offset:18560\n"
"v_mfma_f32_16x16x32_bf16 v[36:39], a[0:3], v[148:151], v[36:39]\n"
"ds_read_b64_tr_b16 a[24:25], v3 offset:20608\n"
"v_mfma_f32_16x16x32_bf16 v[44:47], a[0:3], v[156:159], v[44:47]\n"
"buffer_store_dwordx4 a[40:43], v19, s[52:55], s93 offen offset:0 sc1\n"
"v_mfma_f32_16x16x32_bf16 v[40:43], a[0:3], v[152:155], v[40:43]\n"
"s_waitcnt lgkmcnt(5)\n"
"ds_read_b64_tr_b16 a[26:27], v3 offset:22656\n"
"v_mfma_f32_16x16x32_bf16 v[40:43], a[4:7], v[160:163], v[40:43]\n"
"s_waitcnt vmcnt(12)\n"
"ds_read_b64_tr_b16 a[28:29], v7 offset:20608\n"
"v_mfma_f32_16x16x32_bf16 v[32:35], a[8:11], v[164:167], v[32:35]\n"
"ds_read_b64_tr_b16 a[30:31], v7 offset:22656\n"
"v_mfma_f32_16x16x32_bf16 v[32:35], a[12:15], a[208:211], v[32:35]\n"
"buffer_load_dwordx4 v12, s[36:39], s29 offen offset:1024 lds\n"
"v_mfma_f32_16x16x32_bf16 v[36:39], a[12:15], a[212:215], v[36:39]\n"
"ds_write_b128 v20, a[160:163] offset:0\n"
"v_mfma_f32_16x16x32_bf16 v[36:39], a[8:11], v[168:171], v[36:39]\n"
"ds_read_b64_tr_b16 a[0:1], v4 offset:16512\n"
"v_mfma_f32_16x16x32_bf16 v[44:47], a[8:11], v[176:179], v[44:47]\n"
"ds_read_b64_tr_b16 a[2:3], v4 offset:18560\n"
"v_mfma_f32_16x16x32_bf16 v[40:43], a[8:11], v[172:175], v[40:43]\n"
"s_waitcnt lgkmcnt(5)\n"
"ds_read_b64_tr_b16 a[4:5], v8 offset:16512\n"
"v_mfma_f32_16x16x32_bf16 v[40:43], a[12:15], a[216:219], v[40:43]\n"
"ds_read_b64_tr_b16 a[6:7], v8 offset:18560\n"
"v_mfma_f32_16x16x32_bf16 v[80:83], a[16:19], v[144:147], v[80:83]\n"
"ds_read_b64_tr_b16 a[8:9], v4 offset:20608\n"
"v_mfma_f32_16x16x32_bf16 v[80:83], a[20:23], a[200:203], v[80:83]\n"
"ds_read_b64_tr_b16 a[10:11], v4 offset:22656\n"
"v_mfma_f32_16x16x32_bf16 v[84:87], a[20:23], a[204:207], v[84:87]\n"
"ds_read_b64_tr_b16 a[12:13], v8 offset:20608\n"
"v_mfma_f32_16x16x32_bf16 v[84:87], a[16:19], v[148:151], v[84:87]\n"
"s_addk_i32 s93, 4096\n"
"ds_write_b128 v20, a[164:167] offset:1024\n"
"v_mfma_f32_16x16x32_bf16 v[92:95], a[16:19], v[156:159], v[92:95]\n"
"ds_read_b64_tr_b16 a[14:15], v8 offset:22656\n"
"v_mfma_f32_16x16x32_bf16 v[88:91], a[16:19], v[152:155], v[88:91]\n"
"s_waitcnt lgkmcnt(6)\n"
"ds_read_b128 v[104:107], v24 offset:0\n"
"v_mfma_f32_16x16x32_bf16 v[88:91], a[20:23], v[160:163], v[88:91]\n"
"buffer_store_dwordx4 a[44:47], v19, s[52:55], s93 offen offset:0 sc1\n"
"v_mfma_f32_16x16x32_bf16 v[80:83], a[24:27], v[164:167], v[80:83]\n"
"ds_read_b64_tr_b16 a[16:17], v5 offset:16512\n"
"v_mfma_f32_16x16x32_bf16 v[80:83], a[28:31], a[208:211], v[80:83]\n"
"ds_read_b64_tr_b16 a[18:19], v5 offset:18560\n"
"v_mfma_f32_16x16x32_bf16 v[84:87], a[28:31], a[212:215], v[84:87]\n"
"ds_read_b64_tr_b16 a[20:21], v9 offset:16512\n"
"v_mfma_f32_16x16x32_bf16 v[84:87], a[24:27], v[168:171], v[84:87]\n"
"s_waitcnt lgkmcnt(3)\n"
"buffer_load_dwordx4 v13, s[36:39], s29 offen offset:2048 lds\n"
"v_mfma_f32_16x16x32_bf16 v[92:95], a[24:27], v[176:179], v[92:95]\n"
"ds_read_b64_tr_b16 a[22:23], v9 offset:18560\n"
"v_fmac_f32 v96, v18, v104\n"
"v_mfma_f32_16x16x32_bf16 v[88:91], a[24:27], v[172:175], v[88:91]\n"
"ds_write_b128 v20, a[168:171] offset:2048\n"
"v_mfma_f32_16x16x32_bf16 v[88:91], a[28:31], a[216:219], v[88:91]\n"
"ds_read_b64_tr_b16 a[24:25], v5 offset:20608\n"
"v_fmac_f32 v97, v18, v105\n"
"v_mfma_f32_16x16x32_bf16 v[48:51], a[0:3], v[144:147], v[48:51]\n"
"ds_read_b64_tr_b16 a[26:27], v5 offset:22656\n"
"v_fmac_f32 v98, v18, v106\n"
"v_mfma_f32_16x16x32_bf16 v[48:51], a[4:7], a[200:203], v[48:51]\n"
"ds_read_b64_tr_b16 a[28:29], v9 offset:20608\n"
"v_fmac_f32 v99, v18, v107\n"
"v_mfma_f32_16x16x32_bf16 v[52:55], a[4:7], a[204:207], v[52:55]\n"
"ds_read_b128 v[104:107], v24 offset:1024\n"
"v_cvt_pk_bf16_f32 v96, v96, v97\n"
"v_mfma_f32_16x16x32_bf16 v[52:55], a[0:3], v[148:151], v[52:55]\n"
"buffer_load_dwordx4 v14, s[36:39], s29 offen offset:3072 lds\n"
"v_mfma_f32_16x16x32_bf16 v[60:63], a[0:3], v[156:159], v[60:63]\n"
"ds_read_b64_tr_b16 a[30:31], v9 offset:22656\n"
"v_cvt_pk_bf16_f32 v98, v98, v99\n"
"v_mfma_f32_16x16x32_bf16 v[56:59], a[0:3], v[152:155], v[56:59]\n"
"v_accvgpr_write_b32 a84, v96\n"
"v_accvgpr_write_b32 a85, v98\n"
"v_mfma_f32_16x16x32_bf16 v[56:59], a[4:7], v[160:163], v[56:59]\n"
"s_addk_i32 s93, 4096\n"
"v_mfma_f32_16x16x32_bf16 v[48:51], a[8:11], v[164:167], v[48:51]\n"
"s_waitcnt lgkmcnt(1)\n"
"ds_write_b128 v20, a[172:175] offset:3072\n"
"v_mfma_f32_16x16x32_bf16 v[48:51], a[12:15], a[208:211], v[48:51]\n"
"buffer_store_dwordx4 a[48:51], v19, s[52:55], s93 offen offset:0 sc1\n"
"v_mfma_f32_16x16x32_bf16 v[52:55], a[12:15], a[212:215], v[52:55]\n"
"v_fmac_f32 v100, v18, v104\n"
"v_fmac_f32 v101, v18, v105\n"
"v_mfma_f32_16x16x32_bf16 v[52:55], a[8:11], v[168:171], v[52:55]\n"
"v_fmac_f32 v102, v18, v106\n"
"v_fmac_f32 v103, v18, v107\n"
"v_mfma_f32_16x16x32_bf16 v[60:63], a[8:11], v[176:179], v[60:63]\n"
"v_cvt_pk_bf16_f32 v100, v100, v101\n"
"v_cvt_pk_bf16_f32 v102, v102, v103\n"
"v_mfma_f32_16x16x32_bf16 v[56:59], a[8:11], v[172:175], v[56:59]\n"
"v_accvgpr_write_b32 a86, v100\n"
"v_accvgpr_write_b32 a87, v102\n"
"v_mfma_f32_16x16x32_bf16 v[56:59], a[12:15], a[216:219], v[56:59]\n"
"v_mfma_f32_16x16x32_bf16 v[64:67], a[16:19], v[144:147], v[64:67]\n"
"v_mfma_f32_16x16x32_bf16 v[64:67], a[20:23], a[200:203], v[64:67]\n"
"s_waitcnt lgkmcnt(0)\n"
"v_mfma_f32_16x16x32_bf16 v[68:71], a[20:23], a[204:207], v[68:71]\n"
"s_barrier\n"
"v_mfma_f32_16x16x32_bf16 v[68:71], a[16:19], v[148:151], v[68:71]\n"
"ds_read_b64_tr_b16 a[0:1], v2 offset:24768\n"
"v_mfma_f32_16x16x32_bf16 v[76:79], a[16:19], v[156:159], v[76:79]\n"
"ds_read_b64_tr_b16 a[2:3], v2 offset:26816\n"
"v_mfma_f32_16x16x32_bf16 v[72:75], a[16:19], v[152:155], v[72:75]\n"
"ds_read_b64_tr_b16 a[4:5], v6 offset:24768\n"
"v_mfma_f32_16x16x32_bf16 v[72:75], a[20:23], v[160:163], v[72:75]\n"
"ds_read_b64_tr_b16 a[6:7], v6 offset:26816\n"
"v_mfma_f32_16x16x32_bf16 v[64:67], a[24:27], v[164:167], v[64:67]\n"
"s_addk_i32 s93, 4096\n"
"ds_read_b64_tr_b16 a[8:9], v2 offset:28864\n"
"v_mfma_f32_16x16x32_bf16 v[64:67], a[28:31], a[208:211], v[64:67]\n"
"ds_read_b64_tr_b16 a[10:11], v2 offset:30912\n"
"v_mfma_f32_16x16x32_bf16 v[68:71], a[28:31], a[212:215], v[68:71]\n"
"ds_read_b64_tr_b16 a[12:13], v6 offset:28864\n"
"v_mfma_f32_16x16x32_bf16 v[68:71], a[24:27], v[168:171], v[68:71]\n"
"buffer_store_dwordx4 a[52:55], v19, s[52:55], s93 offen offset:0 sc1\n"
"v_mfma_f32_16x16x32_bf16 v[76:79], a[24:27], v[176:179], v[76:79]\n"
"s_addk_i32 m0, 8256\n"
"ds_read_b64_tr_b16 a[14:15], v6 offset:30912\n"
"s_add_u32 s29, s66, 114688\n"
"v_mfma_f32_16x16x32_bf16 v[72:75], a[24:27], v[172:175], v[72:75]\n"
"s_waitcnt lgkmcnt(4)\n"
"ds_read_b64_tr_b16 a[16:17], v3 offset:24768\n"
"v_mfma_f32_16x16x32_bf16 v[72:75], a[28:31], a[216:219], v[72:75]\n"
"ds_read_b64_tr_b16 a[18:19], v3 offset:26816\n"
"v_mfma_f32_16x16x32_bf16 v[32:35], a[0:3], v[180:183], v[32:35]\n"
"buffer_load_dwordx4 v11, s[36:39], s29 offen offset:0 lds\n"
"v_mfma_f32_16x16x32_bf16 v[32:35], a[4:7], a[220:223], v[32:35]\n"
"ds_read_b64_tr_b16 a[20:21], v7 offset:24768\n"
"v_mfma_f32_16x16x32_bf16 v[36:39], a[4:7], a[224:227], v[36:39]\n"
"ds_read_b64_tr_b16 a[22:23], v7 offset:26816\n"
"v_mfma_f32_16x16x32_bf16 v[36:39], a[0:3], v[184:187], v[36:39]\n"
"ds_read_b64_tr_b16 a[24:25], v3 offset:28864\n"
"v_mfma_f32_16x16x32_bf16 v[44:47], a[0:3], v[192:195], v[44:47]\n"
"ds_read_b64_tr_b16 a[26:27], v3 offset:30912\n"
"v_mfma_f32_16x16x32_bf16 v[40:43], a[0:3], v[188:191], v[40:43]\n"
"s_waitcnt lgkmcnt(5)\n"
"buffer_load_dwordx4 v12, s[36:39], s29 offen offset:1024 lds\n"
"v_mfma_f32_16x16x32_bf16 v[40:43], a[4:7], v[196:199], v[40:43]\n"
"ds_read_b64_tr_b16 a[28:29], v7 offset:28864\n"
"v_mfma_f32_16x16x32_bf16 v[32:35], a[8:11], v[200:203], v[32:35]\n"
"ds_read_b64_tr_b16 a[30:31], v7 offset:30912\n"
"v_mfma_f32_16x16x32_bf16 v[32:35], a[12:15], a[228:231], v[32:35]\n"
"ds_read_b64_tr_b16 a[0:1], v4 offset:24768\n"
"v_mfma_f32_16x16x32_bf16 v[36:39], a[12:15], a[232:235], v[36:39]\n"
"s_addk_i32 s93, 4096\n"
"ds_read_b64_tr_b16 a[2:3], v4 offset:26816\n"
"v_mfma_f32_16x16x32_bf16 v[36:39], a[8:11], v[204:207], v[36:39]\n"
"ds_read_b64_tr_b16 a[4:5], v8 offset:24768\n"
"v_mfma_f32_16x16x32_bf16 v[44:47], a[8:11], v[212:215], v[44:47]\n"
"ds_read_b64_tr_b16 a[6:7], v8 offset:26816\n"
"v_mfma_f32_16x16x32_bf16 v[40:43], a[8:11], v[208:211], v[40:43]\n"
"s_waitcnt lgkmcnt(6)\n"
"buffer_store_dwordx4 a[56:59], v19, s[52:55], s93 offen offset:0 sc1\n"
"v_mfma_f32_16x16x32_bf16 v[40:43], a[12:15], a[236:239], v[40:43]\n"
"ds_read_b64_tr_b16 a[8:9], v4 offset:28864\n"
"v_mfma_f32_16x16x32_bf16 v[80:83], a[16:19], v[180:183], v[80:83]\n"
"ds_read_b64_tr_b16 a[10:11], v4 offset:30912\n"
"v_mfma_f32_16x16x32_bf16 v[80:83], a[20:23], a[220:223], v[80:83]\n"
"ds_read_b64_tr_b16 a[12:13], v8 offset:28864\n"
"v_mfma_f32_16x16x32_bf16 v[84:87], a[20:23], a[224:227], v[84:87]\n"
"ds_read_b64_tr_b16 a[14:15], v8 offset:30912\n"
"v_mfma_f32_16x16x32_bf16 v[84:87], a[16:19], v[184:187], v[84:87]\n"
"v_mfma_f32_16x16x32_bf16 v[92:95], a[16:19], v[192:195], v[92:95]\n"
"buffer_load_dwordx4 v13, s[36:39], s29 offen offset:2048 lds\n"
"v_mfma_f32_16x16x32_bf16 v[88:91], a[16:19], v[188:191], v[88:91]\n"
"s_waitcnt lgkmcnt(4)\n"
"ds_read_b64_tr_b16 a[16:17], v5 offset:24768\n"
"v_mfma_f32_16x16x32_bf16 v[88:91], a[20:23], v[196:199], v[88:91]\n"
"ds_read_b64_tr_b16 a[18:19], v5 offset:26816\n"
"v_mfma_f32_16x16x32_bf16 v[80:83], a[24:27], v[200:203], v[80:83]\n"
"ds_read_b64_tr_b16 a[20:21], v9 offset:24768\n"
"PG11_%=:\n"
"s_cbranch_execz PG15_%=\n"
"v_mfma_f32_16x16x32_bf16 v[80:83], a[28:31], a[228:231], v[80:83]\n"
"ds_read_b64_tr_b16 a[22:23], v9 offset:26816\n"
"v_mfma_f32_16x16x32_bf16 v[84:87], a[28:31], a[232:235], v[84:87]\n"
"v_mfma_f32_16x16x32_bf16 v[84:87], a[24:27], v[204:207], v[84:87]\n"
"v_mfma_f32_16x16x32_bf16 v[92:95], a[24:27], v[212:215], v[92:95]\n"
"s_addk_i32 s93, 4096\n"
"v_mfma_f32_16x16x32_bf16 v[88:91], a[24:27], v[208:211], v[88:91]\n"
"ds_read_b64_tr_b16 a[24:25], v5 offset:28864\n"
"v_mfma_f32_16x16x32_bf16 v[88:91], a[28:31], a[236:239], v[88:91]\n"
"ds_read_b64_tr_b16 a[26:27], v5 offset:30912\n"
"v_mfma_f32_16x16x32_bf16 v[48:51], a[0:3], v[180:183], v[48:51]\n"
"buffer_store_dwordx4 a[60:63], v19, s[52:55], s93 offen offset:0 sc1\n"
"v_mfma_f32_16x16x32_bf16 v[48:51], a[4:7], a[220:223], v[48:51]\n"
"ds_read_b64_tr_b16 a[28:29], v9 offset:28864\n"
"v_mfma_f32_16x16x32_bf16 v[52:55], a[4:7], a[224:227], v[52:55]\n"
"ds_read_b64_tr_b16 a[30:31], v9 offset:30912\n"
"v_mfma_f32_16x16x32_bf16 v[52:55], a[0:3], v[184:187], v[52:55]\n"
"v_mfma_f32_16x16x32_bf16 v[60:63], a[0:3], v[192:195], v[60:63]\n"
"buffer_load_dwordx4 v14, s[36:39], s29 offen offset:3072 lds\n"
"v_mfma_f32_16x16x32_bf16 v[56:59], a[0:3], v[188:191], v[56:59]\n"
"s_waitcnt lgkmcnt(3)\n"
"v_mfma_f32_16x16x32_bf16 v[56:59], a[4:7], v[196:199], v[56:59]\n"
"v_mfma_f32_16x16x32_bf16 v[48:51], a[8:11], v[200:203], v[48:51]\n"
"v_mfma_f32_16x16x32_bf16 v[48:51], a[12:15], a[228:231], v[48:51]\n"
"v_mfma_f32_16x16x32_bf16 v[52:55], a[12:15], a[232:235], v[52:55]\n"
"v_mfma_f32_16x16x32_bf16 v[52:55], a[8:11], v[204:207], v[52:55]\n"
"v_mfma_f32_16x16x32_bf16 v[60:63], a[8:11], v[212:215], v[60:63]\n"
"v_mfma_f32_16x16x32_bf16 v[56:59], a[8:11], v[208:211], v[56:59]\n"
"v_mfma_f32_16x16x32_bf16 v[56:59], a[12:15], a[236:239], v[56:59]\n"
"v_mfma_f32_16x16x32_bf16 v[64:67], a[16:19], v[180:183], v[64:67]\n"
"v_mfma_f32_16x16x32_bf16 v[64:67], a[20:23], a[220:223], v[64:67]\n"
"s_waitcnt lgkmcnt(0)\n"
"v_mfma_f32_16x16x32_bf16 v[68:71], a[20:23], a[224:227], v[68:71]\n"
"s_barrier\n"
"v_mfma_f32_16x16x32_bf16 v[68:71], a[16:19], v[184:187], v[68:71]\n"
"ds_read_b64_tr_b16 a[0:1], v2 offset:41280\n"
"v_mfma_f32_16x16x32_bf16 v[76:79], a[16:19], v[192:195], v[76:79]\n"
"s_addk_i32 s93, 4096\n"
"ds_read_b64_tr_b16 a[2:3], v2 offset:43328\n"
"v_mfma_f32_16x16x32_bf16 v[72:75], a[16:19], v[188:191], v[72:75]\n"
"ds_read_b64_tr_b16 a[4:5], v6 offset:41280\n"
"v_mfma_f32_16x16x32_bf16 v[72:75], a[20:23], v[196:199], v[72:75]\n"
"ds_read_b64_tr_b16 a[6:7], v6 offset:43328\n"
"v_mfma_f32_16x16x32_bf16 v[64:67], a[24:27], v[200:203], v[64:67]\n"
"buffer_store_dwordx4 a[64:67], v19, s[52:55], s93 offen offset:0 sc1\n"
"v_mfma_f32_16x16x32_bf16 v[64:67], a[28:31], a[228:231], v[64:67]\n"
"s_addk_i32 m0, 8256\n"
"ds_read_b64_tr_b16 a[8:9], v2 offset:45376\n"
"s_add_u32 s29, s66, 172032\n"
"v_mfma_f32_16x16x32_bf16 v[68:71], a[28:31], a[232:235], v[68:71]\n"
"ds_read_b64_tr_b16 a[10:11], v2 offset:47424\n"
"v_mfma_f32_16x16x32_bf16 v[68:71], a[24:27], v[204:207], v[68:71]\n"
"ds_read_b64_tr_b16 a[12:13], v6 offset:45376\n"
"v_mfma_f32_16x16x32_bf16 v[76:79], a[24:27], v[212:215], v[76:79]\n"
"buffer_load_dwordx4 v11, s[36:39], s29 offen offset:0 lds\n"
"v_mfma_f32_16x16x32_bf16 v[72:75], a[24:27], v[208:211], v[72:75]\n"
"s_waitcnt lgkmcnt(3)\n"
"ds_read_b64_tr_b16 a[14:15], v6 offset:47424\n"
"v_mfma_f32_16x16x32_bf16 v[72:75], a[28:31], a[236:239], v[72:75]\n"
"ds_read_b64_tr_b16 a[16:17], v3 offset:41280\n"
"v_mfma_f32_16x16x32_bf16 v[32:35], a[0:3], v[216:219], v[32:35]\n"
"ds_read_b64_tr_b16 a[18:19], v3 offset:43328\n"
"v_mfma_f32_16x16x32_bf16 v[32:35], a[4:7], a[240:243], v[32:35]\n"
"ds_read_b64_tr_b16 a[20:21], v7 offset:41280\n"
"v_mfma_f32_16x16x32_bf16 v[36:39], a[4:7], a[244:247], v[36:39]\n"
"ds_read_b64_tr_b16 a[22:23], v7 offset:43328\n"
"v_mfma_f32_16x16x32_bf16 v[36:39], a[0:3], v[220:223], v[36:39]\n"
"ds_read_b64_tr_b16 a[24:25], v3 offset:45376\n"
"v_mfma_f32_16x16x32_bf16 v[44:47], a[0:3], v[228:231], v[44:47]\n"
"ds_read_b64_tr_b16 a[26:27], v3 offset:47424\n"
"v_mfma_f32_16x16x32_bf16 v[40:43], a[0:3], v[224:227], v[40:43]\n"
"s_waitcnt lgkmcnt(6)\n"
"buffer_load_dwordx4 v12, s[36:39], s29 offen offset:1024 lds\n"
"v_mfma_f32_16x16x32_bf16 v[40:43], a[4:7], v[232:235], v[40:43]\n"
"ds_read_b64_tr_b16 a[28:29], v7 offset:45376\n"
"v_mfma_f32_16x16x32_bf16 v[32:35], a[8:11], v[236:239], v[32:35]\n"
"ds_read_b64_tr_b16 a[30:31], v7 offset:47424\n"
"v_mfma_f32_16x16x32_bf16 v[32:35], a[12:15], a[248:251], v[32:35]\n"
"ds_read_b64_tr_b16 a[0:1], v4 offset:41280\n"
"v_mfma_f32_16x16x32_bf16 v[36:39], a[12:15], a[252:255], v[36:39]\n"
"ds_read_b64_tr_b16 a[2:3], v4 offset:43328\n"
"v_mfma_f32_16x16x32_bf16 v[36:39], a[8:11], v[240:243], v[36:39]\n"
"ds_read_b64_tr_b16 a[4:5], v8 offset:41280\n"
"v_mfma_f32_16x16x32_bf16 v[44:47], a[8:11], v[248:251], v[44:47]\n"
"s_addk_i32 s93, 4096\n"
"ds_read_b64_tr_b16 a[6:7], v8 offset:43328\n"
"v_mul_f32 v32, v15, v32\n"
"v_mfma_f32_16x16x32_bf16 v[40:43], a[8:11], v[244:247], v[40:43]\n"
"s_waitcnt lgkmcnt(6)\n"
"ds_read_b64_tr_b16 a[8:9], v4 offset:45376\n"
"v_mul_f32 v33, v15, v33\n"
"v_mfma_f32_16x16x32_bf16 v[40:43], a[12:15], v[252:255], v[40:43]\n"
"ds_read_b64_tr_b16 a[10:11], v4 offset:47424\n"
"v_mul_f32 v34, v15, v34\n"
"v_mfma_f32_16x16x32_bf16 v[80:83], a[16:19], v[216:219], v[80:83]\n"
"buffer_store_dwordx4 a[68:71], v19, s[52:55], s93 offen offset:0 sc1\n"
"v_mfma_f32_16x16x32_bf16 v[80:83], a[20:23], a[240:243], v[80:83]\n"
"ds_read_b64_tr_b16 a[12:13], v8 offset:45376\n"
"v_mul_f32 v35, v15, v35\n"
"v_mfma_f32_16x16x32_bf16 v[84:87], a[20:23], a[244:247], v[84:87]\n"
"ds_read_b64_tr_b16 a[14:15], v8 offset:47424\n"
"v_cvt_pk_bf16_f32 v32, v32, v33\n"
"v_mfma_f32_16x16x32_bf16 v[84:87], a[16:19], v[220:223], v[84:87]\n"
"v_cvt_pk_bf16_f32 v34, v34, v35\n"
"v_accvgpr_write_b32 a88, v32\n"
"v_mfma_f32_16x16x32_bf16 v[92:95], a[16:19], v[228:231], v[92:95]\n"
"buffer_load_dwordx4 v13, s[36:39], s29 offen offset:2048 lds\n"
"v_mfma_f32_16x16x32_bf16 v[88:91], a[16:19], v[224:227], v[88:91]\n"
"s_waitcnt lgkmcnt(3)\n"
"ds_read_b64_tr_b16 a[16:17], v5 offset:41280\n"
"v_accvgpr_write_b32 a89, v34\n"
"v_mfma_f32_16x16x32_bf16 v[88:91], a[20:23], v[232:235], v[88:91]\n"
"ds_read_b64_tr_b16 a[18:19], v5 offset:43328\n"
"v_mul_f32 v36, v16, v36\n"
"v_mfma_f32_16x16x32_bf16 v[80:83], a[24:27], v[236:239], v[80:83]\n"
"ds_read_b64_tr_b16 a[20:21], v9 offset:41280\n"
"v_mul_f32 v37, v16, v37\n"
"v_mfma_f32_16x16x32_bf16 v[80:83], a[28:31], a[248:251], v[80:83]\n"
"ds_read_b64_tr_b16 a[22:23], v9 offset:43328\n"
"v_mul_f32 v38, v16, v38\n"
"v_mfma_f32_16x16x32_bf16 v[84:87], a[28:31], a[252:255], v[84:87]\n"
"v_mul_f32 v39, v16, v39\n"
"v_cvt_pk_bf16_f32 v36, v36, v37\n"
"v_mfma_f32_16x16x32_bf16 v[84:87], a[24:27], v[240:243], v[84:87]\n"
"v_cvt_pk_bf16_f32 v38, v38, v39\n"
"v_accvgpr_write_b32 a96, v36\n"
"v_mfma_f32_16x16x32_bf16 v[92:95], a[24:27], v[248:251], v[92:95]\n"
"v_accvgpr_write_b32 a97, v38\n"
"v_mul_f32 v40, v17, v40\n"
"v_mfma_f32_16x16x32_bf16 v[88:91], a[24:27], v[244:247], v[88:91]\n"
"ds_read_b64_tr_b16 a[24:25], v5 offset:45376\n"
"v_mul_f32 v41, v17, v41\n"
"v_mfma_f32_16x16x32_bf16 v[88:91], a[28:31], v[252:255], v[88:91]\n"
"ds_read_b64_tr_b16 a[26:27], v5 offset:47424\n"
"v_mul_f32 v42, v17, v42\n"
"v_mfma_f32_16x16x32_bf16 v[48:51], a[0:3], v[216:219], v[48:51]\n"
"ds_read_b64_tr_b16 a[28:29], v9 offset:45376\n"
"v_mul_f32 v43, v17, v43\n"
"v_mfma_f32_16x16x32_bf16 v[48:51], a[4:7], a[240:243], v[48:51]\n"
"ds_read_b64_tr_b16 a[30:31], v9 offset:47424\n"
"v_mul_f32 v96, v18, v44\n"
"v_mfma_f32_16x16x32_bf16 v[52:55], a[4:7], a[244:247], v[52:55]\n"
"v_mul_f32 v97, v18, v45\n"
"v_mul_f32 v98, v18, v46\n"
"v_mfma_f32_16x16x32_bf16 v[52:55], a[0:3], v[220:223], v[52:55]\n"
"buffer_load_dwordx4 v14, s[36:39], s29 offen offset:3072 lds\n"
"v_mfma_f32_16x16x32_bf16 v[60:63], a[0:3], v[228:231], v[60:63]\n"
"s_addk_i32 s93, 4096\n"
"v_mul_f32 v99, v18, v47\n"
"v_cvt_pk_bf16_f32 v40, v40, v41\n"
"v_mfma_f32_16x16x32_bf16 v[56:59], a[0:3], v[224:227], v[56:59]\n"
"s_waitcnt lgkmcnt(3)\n"
"v_cvt_pk_bf16_f32 v42, v42, v43\n"
"v_accvgpr_write_b32 a104, v40\n"
"v_mfma_f32_16x16x32_bf16 v[56:59], a[4:7], v[232:235], v[56:59]\n"
"v_accvgpr_write_b32 a105, v42\n"
"v_mul_f32 v80, v15, v80\n"
"v_mfma_f32_16x16x32_bf16 v[48:51], a[8:11], v[236:239], v[48:51]\n"
"buffer_store_dwordx4 a[72:75], v19, s[52:55], s93 offen offset:0 sc1\n"
"v_mfma_f32_16x16x32_bf16 v[48:51], a[12:15], a[248:251], v[48:51]\n"
"v_mul_f32 v81, v15, v81\n"
"v_mul_f32 v82, v15, v82\n"
"v_mfma_f32_16x16x32_bf16 v[52:55], a[12:15], a[252:255], v[52:55]\n"
"v_mul_f32 v83, v15, v83\n"
"v_cvt_pk_bf16_f32 v80, v80, v81\n"
"v_mfma_f32_16x16x32_bf16 v[52:55], a[8:11], v[240:243], v[52:55]\n"
"v_cvt_pk_bf16_f32 v82, v82, v83\n"
"v_accvgpr_write_b32 a90, v80\n"
"v_mfma_f32_16x16x32_bf16 v[60:63], a[8:11], v[248:251], v[60:63]\n"
"v_accvgpr_write_b32 a91, v82\n"
"v_mul_f32 v84, v16, v84\n"
"v_mfma_f32_16x16x32_bf16 v[56:59], a[8:11], v[244:247], v[56:59]\n"
"v_mul_f32 v85, v16, v85\n"
"v_mul_f32 v86, v16, v86\n"
"v_mfma_f32_16x16x32_bf16 v[56:59], a[12:15], v[252:255], v[56:59]\n"
"v_mul_f32 v87, v16, v87\n"
"v_cvt_pk_bf16_f32 v84, v84, v85\n"
"v_mfma_f32_16x16x32_bf16 v[64:67], a[16:19], v[216:219], v[64:67]\n"
"ds_write_b128 v23, v[60:63] offset:0\n"
"v_mfma_f32_16x16x32_bf16 v[64:67], a[20:23], a[240:243], v[64:67]\n"
"s_waitcnt lgkmcnt(1)\n"
"v_cvt_pk_bf16_f32 v86, v86, v87\n"
"v_accvgpr_write_b32 a98, v84\n"
"v_mfma_f32_16x16x32_bf16 v[68:71], a[20:23], a[244:247], v[68:71]\n"
"s_barrier\n"
"v_mfma_f32_16x16x32_bf16 v[68:71], a[16:19], v[220:223], v[68:71]\n"
"s_addk_i32 m0, 16512\n"
"ds_read_b64_tr_b16 a[0:1], v2 offset:49536\n"
"v_accvgpr_write_b32 a99, v86\n"
"v_mfma_f32_16x16x32_bf16 v[76:79], a[16:19], v[228:231], v[76:79]\n"
"s_add_u32 s29, s67, 0\n"
"ds_read_b64_tr_b16 a[2:3], v2 offset:51584\n"
"v_mul_f32 v88, v17, v88\n"
"v_mfma_f32_16x16x32_bf16 v[72:75], a[16:19], v[224:227], v[72:75]\n"
"ds_read_b64_tr_b16 a[4:5], v6 offset:49536\n"
"v_mul_f32 v89, v17, v89\n"
"v_mfma_f32_16x16x32_bf16 v[72:75], a[20:23], v[232:235], v[72:75]\n"
"ds_read_b64_tr_b16 a[6:7], v6 offset:51584\n"
"v_mul_f32 v90, v17, v90\n"
"v_mfma_f32_16x16x32_bf16 v[64:67], a[24:27], v[236:239], v[64:67]\n"
"buffer_load_dwordx4 v11, s[36:39], s29 offen offset:0 lds\n"
"v_mfma_f32_16x16x32_bf16 v[64:67], a[28:31], a[248:251], v[64:67]\n"
"ds_read_b64_tr_b16 a[8:9], v2 offset:53632\n"
"v_mul_f32 v91, v17, v91\n"
"v_mfma_f32_16x16x32_bf16 v[68:71], a[28:31], a[252:255], v[68:71]\n"
"ds_read_b64_tr_b16 a[10:11], v2 offset:55680\n"
"v_mul_f32 v100, v18, v92\n"
"v_mfma_f32_16x16x32_bf16 v[68:71], a[24:27], v[240:243], v[68:71]\n"
"ds_read_b64_tr_b16 a[12:13], v6 offset:53632\n"
"v_mul_f32 v101, v18, v93\n"
"v_mfma_f32_16x16x32_bf16 v[76:79], a[24:27], v[248:251], v[76:79]\n"
"s_addk_i32 s93, 4096\n"
"ds_read_b64_tr_b16 a[14:15], v6 offset:55680\n"
"v_mul_f32 v102, v18, v94\n"
"v_mfma_f32_16x16x32_bf16 v[72:75], a[24:27], v[244:247], v[72:75]\n"
"s_waitcnt lgkmcnt(4)\n"
"ds_read_b64_tr_b16 a[16:17], v3 offset:49536\n"
"v_mul_f32 v103, v18, v95\n"
"v_mfma_f32_16x16x32_bf16 v[72:75], a[28:31], v[252:255], v[72:75]\n"
"ds_read_b64_tr_b16 a[18:19], v3 offset:51584\n"
"v_cvt_pk_bf16_f32 v88, v88, v89\n"
"v_mfma_f32_16x16x32_bf16 v[32:35], a[0:3], v[108:111], 0\n"
"buffer_store_dwordx4 a[76:79], v19, s[52:55], s93 offen offset:0 sc1\n"
"v_mfma_f32_16x16x32_bf16 v[32:35], a[4:7], a[180:183], v[32:35]\n"
"ds_read_b64_tr_b16 a[20:21], v7 offset:49536\n"
"v_cvt_pk_bf16_f32 v90, v90, v91\n"
"v_mfma_f32_16x16x32_bf16 v[36:39], a[4:7], a[184:187], 0\n"
"ds_read_b64_tr_b16 a[22:23], v7 offset:51584\n"
"v_accvgpr_write_b32 a106, v88\n"
"v_mfma_f32_16x16x32_bf16 v[36:39], a[0:3], v[112:115], v[36:39]\n"
"ds_read_b64_tr_b16 a[24:25], v3 offset:53632\n"
"v_accvgpr_write_b32 a107, v90\n"
"v_mfma_f32_16x16x32_bf16 v[44:47], a[0:3], v[120:123], 0\n"
"ds_read_b64_tr_b16 a[26:27], v3 offset:55680\n"
"v_mul_f32 v48, v15, v48\n"
"v_mfma_f32_16x16x32_bf16 v[40:43], a[0:3], v[116:119], 0\n"
"s_waitcnt lgkmcnt(5)\n"
"buffer_load_dwordx4 v12, s[36:39], s29 offen offset:1024 lds\n"
"v_mfma_f32_16x16x32_bf16 v[40:43], a[4:7], v[124:127], v[40:43]\n"
"ds_read_b64_tr_b16 a[28:29], v7 offset:53632\n"
"v_mul_f32 v49, v15, v49\n"
"v_mfma_f32_16x16x32_bf16 v[32:35], a[8:11], v[128:131], v[32:35]\n"
"ds_read_b64_tr_b16 a[30:31], v7 offset:55680\n"
"v_mul_f32 v50, v15, v50\n"
"v_mfma_f32_16x16x32_bf16 v[32:35], a[12:15], a[188:191], v[32:35]\n"
"ds_read_b64_tr_b16 a[0:1], v4 offset:49536\n"
"v_mul_f32 v51, v15, v51\n"
"v_mfma_f32_16x16x32_bf16 v[36:39], a[12:15], a[192:195], v[36:39]\n"
"ds_read_b64_tr_b16 a[2:3], v4 offset:51584\n"
"v_cvt_pk_bf16_f32 v48, v48, v49\n"
"v_mfma_f32_16x16x32_bf16 v[36:39], a[8:11], v[132:135], v[36:39]\n"
"ds_read_b64_tr_b16 a[4:5], v8 offset:49536\n"
"v_cvt_pk_bf16_f32 v50, v50, v51\n"
"v_mfma_f32_16x16x32_bf16 v[44:47], a[8:11], v[140:143], v[44:47]\n"
"ds_read_b64_tr_b16 a[6:7], v8 offset:51584\n"
"v_accvgpr_write_b32 a92, v48\n"
"v_mfma_f32_16x16x32_bf16 v[40:43], a[8:11], v[136:139], v[40:43]\n"
"s_waitcnt lgkmcnt(6)\n"
"ds_read_b64_tr_b16 a[8:9], v4 offset:53632\n"
"v_accvgpr_write_b32 a93, v50\n"
"v_mfma_f32_16x16x32_bf16 v[40:43], a[12:15], a[196:199], v[40:43]\n"
"ds_read_b64_tr_b16 a[10:11], v4 offset:55680\n"
"v_mul_f32 v52, v16, v52\n"
"v_mfma_f32_16x16x32_bf16 v[80:83], a[16:19], v[108:111], 0\n"
"ds_read_b64_tr_b16 a[12:13], v8 offset:53632\n"
"v_mul_f32 v53, v16, v53\n"
"v_mfma_f32_16x16x32_bf16 v[80:83], a[20:23], a[180:183], v[80:83]\n"
"ds_read_b64_tr_b16 a[14:15], v8 offset:55680\n"
"v_mul_f32 v54, v16, v54\n"
"v_mfma_f32_16x16x32_bf16 v[84:87], a[20:23], a[184:187], 0\n"
"ds_write_b128 v23, v[76:79] offset:1024\n"
"v_mfma_f32_16x16x32_bf16 v[84:87], a[16:19], v[112:115], v[84:87]\n"
"v_mul_f32 v55, v16, v55\n"
"v_cvt_pk_bf16_f32 v52, v52, v53\n"
"v_mfma_f32_16x16x32_bf16 v[92:95], a[16:19], v[120:123], 0\n"
"s_addk_i32 s93, 4096\n"
"v_cvt_pk_bf16_f32 v54, v54, v55\n"
"v_accvgpr_write_b32 a100, v52\n"
"v_mfma_f32_16x16x32_bf16 v[88:91], a[16:19], v[116:119], 0\n"
"s_waitcnt lgkmcnt(4)\n"
"ds_read_b64_tr_b16 a[16:17], v5 offset:49536\n"
"v_accvgpr_write_b32 a101, v54\n"
"v_mfma_f32_16x16x32_bf16 v[88:91], a[20:23], v[124:127], v[88:91]\n"
"ds_read_b64_tr_b16 a[18:19], v5 offset:51584\n"
"v_mul_f32 v56, v17, v56\n"
"v_mfma_f32_16x16x32_bf16 v[80:83], a[24:27], v[128:131], v[80:83]\n"
"buffer_store_dwordx4 a[80:83], v19, s[52:55], s93 offen offset:0 sc1\n"
"v_mfma_f32_16x16x32_bf16 v[80:83], a[28:31], a[188:191], v[80:83]\n"
"ds_read_b64_tr_b16 a[20:21], v9 offset:49536\n"
"v_mul_f32 v57, v17, v57\n"
"v_mfma_f32_16x16x32_bf16 v[84:87], a[28:31], a[192:195], v[84:87]\n"
"ds_read_b64_tr_b16 a[22:23], v9 offset:51584\n"
"v_mul_f32 v58, v17, v58\n"
"v_mfma_f32_16x16x32_bf16 v[84:87], a[24:27], v[132:135], v[84:87]\n"
"v_mul_f32 v59, v17, v59\n"
"v_cvt_pk_bf16_f32 v56, v56, v57\n"
"v_mfma_f32_16x16x32_bf16 v[92:95], a[24:27], v[140:143], v[92:95]\n"
"buffer_load_dwordx4 v13, s[36:39], s29 offen offset:2048 lds\n"
"v_mfma_f32_16x16x32_bf16 v[88:91], a[24:27], v[136:139], v[88:91]\n"
"ds_read_b64_tr_b16 a[24:25], v5 offset:53632\n"
"v_cvt_pk_bf16_f32 v58, v58, v59\n"
"v_mfma_f32_16x16x32_bf16 v[88:91], a[28:31], a[196:199], v[88:91]\n"
"ds_read_b64_tr_b16 a[26:27], v5 offset:55680\n"
"v_accvgpr_write_b32 a108, v56\n"
"v_mfma_f32_16x16x32_bf16 v[48:51], a[0:3], v[108:111], 0\n"
"ds_read_b64_tr_b16 a[28:29], v9 offset:53632\n"
"v_accvgpr_write_b32 a109, v58\n"
"v_mfma_f32_16x16x32_bf16 v[48:51], a[4:7], a[180:183], v[48:51]\n"
"ds_read_b64_tr_b16 a[30:31], v9 offset:55680\n"
"v_mul_f32 v64, v15, v64\n"
"v_mfma_f32_16x16x32_bf16 v[52:55], a[4:7], a[184:187], 0\n"
"v_mul_f32 v65, v15, v65\n"
"v_mul_f32 v66, v15, v66\n"
"v_mfma_f32_16x16x32_bf16 v[52:55], a[0:3], v[112:115], v[52:55]\n"
"buffer_load_dwordx4 v14, s[36:39], s29 offen offset:3072 lds\n"
"v_mfma_f32_16x16x32_bf16 v[60:63], a[0:3], v[120:123], 0\n"
"v_mul_f32 v67, v15, v67\n"
"v_cvt_pk_bf16_f32 v64, v64, v65\n"
"v_mfma_f32_16x16x32_bf16 v[56:59], a[0:3], v[116:119], 0\n"
"s_waitcnt lgkmcnt(3)\n"
"v_cvt_pk_bf16_f32 v66, v66, v67\n"
"v_accvgpr_write_b32 a94, v64\n"
"v_mfma_f32_16x16x32_bf16 v[56:59], a[4:7], v[124:127], v[56:59]\n"
"v_accvgpr_write_b32 a95, v66\n"
"v_mul_f32 v68, v16, v68\n"
"v_mfma_f32_16x16x32_bf16 v[48:51], a[8:11], v[128:131], v[48:51]\n"
"v_mul_f32 v69, v16, v69\n"
"v_mul_f32 v70, v16, v70\n"
"v_mfma_f32_16x16x32_bf16 v[48:51], a[12:15], a[188:191], v[48:51]\n"
"v_mul_f32 v71, v16, v71\n"
"v_cvt_pk_bf16_f32 v68, v68, v69\n"
"v_mfma_f32_16x16x32_bf16 v[52:55], a[12:15], a[192:195], v[52:55]\n"
"v_cvt_pk_bf16_f32 v70, v70, v71\n"
"v_accvgpr_write_b32 a102, v68\n"
"v_mfma_f32_16x16x32_bf16 v[52:55], a[8:11], v[132:135], v[52:55]\n"
"v_accvgpr_write_b32 a103, v70\n"
"v_mul_f32 v72, v17, v72\n"
"v_mfma_f32_16x16x32_bf16 v[60:63], a[8:11], v[140:143], v[60:63]\n"
"s_addk_i32 s93, 4096\n"
"v_mul_f32 v73, v17, v73\n"
"v_mul_f32 v74, v17, v74\n"
"v_mfma_f32_16x16x32_bf16 v[56:59], a[8:11], v[136:139], v[56:59]\n"
"v_mul_f32 v75, v17, v75\n"
"v_cvt_pk_bf16_f32 v72, v72, v73\n"
"v_mfma_f32_16x16x32_bf16 v[56:59], a[12:15], a[196:199], v[56:59]\n"
"v_cvt_pk_bf16_f32 v74, v74, v75\n"
"v_accvgpr_write_b32 a110, v72\n"
"v_mfma_f32_16x16x32_bf16 v[64:67], a[16:19], v[108:111], 0\n"
"buffer_store_dwordx4 a[84:87], v19, s[52:55], s93 offen offset:0 sc1\n"
"v_mfma_f32_16x16x32_bf16 v[64:67], a[20:23], a[180:183], v[64:67]\n"
"s_waitcnt vmcnt(17) lgkmcnt(0)\n"
"v_accvgpr_write_b32 a111, v74\n"
"v_mfma_f32_16x16x32_bf16 v[68:71], a[20:23], a[184:187], 0\n"
"s_barrier\n"
"v_mfma_f32_16x16x32_bf16 v[68:71], a[16:19], v[112:115], v[68:71]\n"
"ds_read_b64_tr_b16 a[0:1], v2 offset:57792\n"
"v_mfma_f32_16x16x32_bf16 v[76:79], a[16:19], v[120:123], 0\n"
"s_addk_i32 m0, 8256\n"
"buffer_store_dwordx2 v[30:31], v29, s[52:55], 0 offen offset:0 sc1\n"
"v_mfma_f32_16x16x32_bf16 v[72:75], a[16:19], v[116:119], 0\n"
"ds_read_b64_tr_b16 a[2:3], v2 offset:59840\n"
"s_add_u32 s29, s68, 0\n"
"v_mfma_f32_16x16x32_bf16 v[72:75], a[20:23], v[124:127], v[72:75]\n"
"ds_read_b64_tr_b16 a[4:5], v6 offset:57792\n"
"v_mfma_f32_16x16x32_bf16 v[64:67], a[24:27], v[128:131], v[64:67]\n"
"ds_read_b64_tr_b16 a[6:7], v6 offset:59840\n"
"v_mfma_f32_16x16x32_bf16 v[64:67], a[28:31], a[188:191], v[64:67]\n"
"buffer_load_dwordx4 v11, s[36:39], s29 offen offset:0 lds\n"
"v_mfma_f32_16x16x32_bf16 v[68:71], a[28:31], a[192:195], v[68:71]\n"
"ds_read_b64_tr_b16 a[8:9], v2 offset:61888\n"
"v_mfma_f32_16x16x32_bf16 v[68:71], a[24:27], v[132:135], v[68:71]\n"
"ds_read_b64_tr_b16 a[10:11], v2 offset:63936\n"
"v_add_u32 v2, s27, v2\n"
"v_mfma_f32_16x16x32_bf16 v[76:79], a[24:27], v[140:143], v[76:79]\n"
"ds_read_b64_tr_b16 a[12:13], v6 offset:61888\n"
"v_mfma_f32_16x16x32_bf16 v[72:75], a[24:27], v[136:139], v[72:75]\n"
"s_waitcnt lgkmcnt(3)\n"
"ds_read_b64_tr_b16 a[14:15], v6 offset:63936\n"
"v_add_u32 v6, s27, v6\n"
"v_mfma_f32_16x16x32_bf16 v[72:75], a[28:31], a[196:199], v[72:75]\n"
"ds_read_b64_tr_b16 a[16:17], v3 offset:57792\n"
"v_mfma_f32_16x16x32_bf16 v[32:35], a[0:3], v[144:147], v[32:35]\n"
"ds_read_b64_tr_b16 a[18:19], v3 offset:59840\n"
"v_mfma_f32_16x16x32_bf16 v[32:35], a[4:7], a[200:203], v[32:35]\n"
"ds_read_b64_tr_b16 a[20:21], v7 offset:57792\n"
"v_mfma_f32_16x16x32_bf16 v[36:39], a[4:7], a[204:207], v[36:39]\n"
"ds_read_b64_tr_b16 a[22:23], v7 offset:59840\n"
"v_mfma_f32_16x16x32_bf16 v[36:39], a[0:3], v[148:151], v[36:39]\n"
"ds_read_b64_tr_b16 a[24:25], v3 offset:61888\n"
"v_mfma_f32_16x16x32_bf16 v[44:47], a[0:3], v[156:159], v[44:47]\n"
"s_addk_i32 s93, 4096\n"
"ds_read_b64_tr_b16 a[26:27], v3 offset:63936\n"
"v_add_u32 v3, s27, v3\n"
"v_mfma_f32_16x16x32_bf16 v[40:43], a[0:3], v[152:155], v[40:43]\n"
"s_waitcnt lgkmcnt(6)\n"
"ds_read_b64_tr_b16 a[28:29], v7 offset:61888\n"
"v_mfma_f32_16x16x32_bf16 v[40:43], a[4:7], v[160:163], v[40:43]\n"
"ds_read_b64_tr_b16 a[30:31], v7 offset:63936\n"
"v_add_u32 v7, s27, v7\n"
"v_mfma_f32_16x16x32_bf16 v[32:35], a[8:11], v[164:167], v[32:35]\n"
"buffer_store_dwordx4 a[88:91], v19, s[52:55], s93 offen offset:0 sc1\n"
"v_mfma_f32_16x16x32_bf16 v[32:35], a[12:15], a[208:211], v[32:35]\n"
"ds_read_b64_tr_b16 a[0:1], v4 offset:57792\n"
"v_mfma_f32_16x16x32_bf16 v[36:39], a[12:15], a[212:215], v[36:39]\n"
"ds_read_b64_tr_b16 a[2:3], v4 offset:59840\n"
"v_mfma_f32_16x16x32_bf16 v[36:39], a[8:11], v[168:171], v[36:39]\n"
"ds_read_b64_tr_b16 a[4:5], v8 offset:57792\n"
"v_mfma_f32_16x16x32_bf16 v[44:47], a[8:11], v[176:179], v[44:47]\n"
"buffer_load_dwordx4 v12, s[36:39], s29 offen offset:1024 lds\n"
"v_mfma_f32_16x16x32_bf16 v[40:43], a[8:11], v[172:175], v[40:43]\n"
"s_waitcnt lgkmcnt(4)\n"
"ds_read_b64_tr_b16 a[6:7], v8 offset:59840\n"
"v_mfma_f32_16x16x32_bf16 v[40:43], a[12:15], a[216:219], v[40:43]\n"
"ds_read_b64_tr_b16 a[8:9], v4 offset:61888\n"
"v_mfma_f32_16x16x32_bf16 v[80:83], a[16:19], v[144:147], v[80:83]\n"
"ds_read_b64_tr_b16 a[10:11], v4 offset:63936\n"
"v_add_u32 v4, s27, v4\n"
"v_mfma_f32_16x16x32_bf16 v[80:83], a[20:23], a[200:203], v[80:83]\n"
"ds_read_b64_tr_b16 a[12:13], v8 offset:61888\n"
"v_mfma_f32_16x16x32_bf16 v[84:87], a[20:23], a[204:207], v[84:87]\n"
"ds_read_b64_tr_b16 a[14:15], v8 offset:63936\n"
"v_add_u32 v8, s27, v8\n"
"v_mfma_f32_16x16x32_bf16 v[84:87], a[16:19], v[148:151], v[84:87]\n"
"ds_read_b128 v[104:107], v24 offset:0\n"
"v_mfma_f32_16x16x32_bf16 v[92:95], a[16:19], v[156:159], v[92:95]\n"
"buffer_load_dwordx4 v13, s[36:39], s29 offen offset:2048 lds\n"
"v_mfma_f32_16x16x32_bf16 v[88:91], a[16:19], v[152:155], v[88:91]\n"
"s_waitcnt lgkmcnt(5)\n"
"ds_read_b64_tr_b16 a[16:17], v5 offset:57792\n"
"v_mfma_f32_16x16x32_bf16 v[88:91], a[20:23], v[160:163], v[88:91]\n"
"ds_read_b64_tr_b16 a[18:19], v5 offset:59840\n"
"v_mfma_f32_16x16x32_bf16 v[80:83], a[24:27], v[164:167], v[80:83]\n"
"ds_read_b64_tr_b16 a[20:21], v9 offset:57792\n"
"v_mfma_f32_16x16x32_bf16 v[80:83], a[28:31], a[208:211], v[80:83]\n"
"s_waitcnt lgkmcnt(3)\n"
"ds_read_b64_tr_b16 a[22:23], v9 offset:59840\n"
"v_fmac_f32 v96, v18, v104\n"
"v_mfma_f32_16x16x32_bf16 v[84:87], a[28:31], a[212:215], v[84:87]\n"
"v_fmac_f32 v97, v18, v105\n"
"v_fmac_f32 v98, v18, v106\n"
"v_mfma_f32_16x16x32_bf16 v[84:87], a[24:27], v[168:171], v[84:87]\n"
"v_fmac_f32 v99, v18, v107\n"
"v_cvt_pk_bf16_f32 v96, v96, v97\n"
"v_mfma_f32_16x16x32_bf16 v[92:95], a[24:27], v[176:179], v[92:95]\n"
"s_addk_i32 s93, 4096\n"
"ds_read_b128 v[104:107], v24 offset:1024\n"
"v_cvt_pk_bf16_f32 v98, v98, v99\n"
"v_mfma_f32_16x16x32_bf16 v[88:91], a[24:27], v[172:175], v[88:91]\n"
"ds_read_b64_tr_b16 a[24:25], v5 offset:61888\n"
"v_accvgpr_write_b32 a112, v96\n"
"v_mfma_f32_16x16x32_bf16 v[88:91], a[28:31], a[216:219], v[88:91]\n"
"ds_read_b64_tr_b16 a[26:27], v5 offset:63936\n"
"v_add_u32 v5, s27, v5\n"
"v_mfma_f32_16x16x32_bf16 v[48:51], a[0:3], v[144:147], v[48:51]\n"
"buffer_store_dwordx4 a[92:95], v19, s[52:55], s93 offen offset:0 sc1\n"
"v_mfma_f32_16x16x32_bf16 v[48:51], a[4:7], a[200:203], v[48:51]\n"
"ds_read_b64_tr_b16 a[28:29], v9 offset:61888\n"
"v_accvgpr_write_b32 a113, v98\n"
"v_mfma_f32_16x16x32_bf16 v[52:55], a[4:7], a[204:207], v[52:55]\n"
"s_waitcnt lgkmcnt(3)\n"
"ds_read_b64_tr_b16 a[30:31], v9 offset:63936\n"
"v_add_u32 v9, s27, v9\n"
"v_mfma_f32_16x16x32_bf16 v[52:55], a[0:3], v[148:151], v[52:55]\n"
"v_fmac_f32 v100, v18, v104\n"
"v_fmac_f32 v101, v18, v105\n"
"v_mfma_f32_16x16x32_bf16 v[60:63], a[0:3], v[156:159], v[60:63]\n"
"buffer_load_dwordx4 v14, s[36:39], s29 offen offset:3072 lds\n"
"v_mfma_f32_16x16x32_bf16 v[56:59], a[0:3], v[152:155], v[56:59]\n"
"v_fmac_f32 v102, v18, v106\n"
"v_fmac_f32 v103, v18, v107\n"
"v_mfma_f32_16x16x32_bf16 v[56:59], a[4:7], v[160:163], v[56:59]\n"
"v_cvt_pk_bf16_f32 v100, v100, v101\n"
"v_cvt_pk_bf16_f32 v102, v102, v103\n"
"v_mfma_f32_16x16x32_bf16 v[48:51], a[8:11], v[164:167], v[48:51]\n"
"v_accvgpr_write_b32 a114, v100\n"
"v_accvgpr_write_b32 a115, v102\n"
"v_mfma_f32_16x16x32_bf16 v[48:51], a[12:15], a[208:211], v[48:51]\n"
"v_mfma_f32_16x16x32_bf16 v[52:55], a[12:15], a[212:215], v[52:55]\n"
"v_mfma_f32_16x16x32_bf16 v[52:55], a[8:11], v[168:171], v[52:55]\n"
"v_mfma_f32_16x16x32_bf16 v[60:63], a[8:11], v[176:179], v[60:63]\n"
"v_mfma_f32_16x16x32_bf16 v[56:59], a[8:11], v[172:175], v[56:59]\n"
"v_mfma_f32_16x16x32_bf16 v[56:59], a[12:15], a[216:219], v[56:59]\n"
"v_mfma_f32_16x16x32_bf16 v[64:67], a[16:19], v[144:147], v[64:67]\n"
"v_mfma_f32_16x16x32_bf16 v[64:67], a[20:23], a[200:203], v[64:67]\n"
"s_waitcnt lgkmcnt(0)\n"
"v_mfma_f32_16x16x32_bf16 v[68:71], a[20:23], a[204:207], v[68:71]\n"
"s_barrier\n"
"v_mfma_f32_16x16x32_bf16 v[68:71], a[16:19], v[148:151], v[68:71]\n"
"ds_read_b64_tr_b16 a[0:1], v2 offset:57792\n"
"v_mfma_f32_16x16x32_bf16 v[76:79], a[16:19], v[156:159], v[76:79]\n"
"s_addk_i32 s93, 4096\n"
"ds_read_b64_tr_b16 a[2:3], v2 offset:59840\n"
"v_mfma_f32_16x16x32_bf16 v[72:75], a[16:19], v[152:155], v[72:75]\n"
"ds_read_b64_tr_b16 a[4:5], v6 offset:57792\n"
"v_mfma_f32_16x16x32_bf16 v[72:75], a[20:23], v[160:163], v[72:75]\n"
"ds_read_b64_tr_b16 a[6:7], v6 offset:59840\n"
"v_mfma_f32_16x16x32_bf16 v[64:67], a[24:27], v[164:167], v[64:67]\n"
"buffer_store_dwordx4 a[96:99], v19, s[52:55], s93 offen offset:0 sc1\n"
"v_mfma_f32_16x16x32_bf16 v[64:67], a[28:31], a[208:211], v[64:67]\n"
"s_addk_i32 m0, 8256\n"
"ds_read_b64_tr_b16 a[8:9], v2 offset:61888\n"
"s_add_u32 s29, s69, 0\n"
"v_mfma_f32_16x16x32_bf16 v[68:71], a[28:31], a[212:215], v[68:71]\n"
"ds_read_b64_tr_b16 a[10:11], v2 offset:63936\n"
"v_subrev_u32 v2, s27, v2\n"
"v_mfma_f32_16x16x32_bf16 v[68:71], a[24:27], v[168:171], v[68:71]\n"
"ds_read_b64_tr_b16 a[12:13], v6 offset:61888\n"
"v_mfma_f32_16x16x32_bf16 v[76:79], a[24:27], v[176:179], v[76:79]\n"
"buffer_load_dwordx4 v11, s[36:39], s29 offen offset:0 lds\n"
"v_mfma_f32_16x16x32_bf16 v[72:75], a[24:27], v[172:175], v[72:75]\n"
"s_waitcnt lgkmcnt(3)\n"
"ds_read_b64_tr_b16 a[14:15], v6 offset:63936\n"
"v_subrev_u32 v6, s27, v6\n"
"v_mfma_f32_16x16x32_bf16 v[72:75], a[28:31], a[216:219], v[72:75]\n"
"ds_read_b64_tr_b16 a[16:17], v3 offset:57792\n"
"v_mfma_f32_16x16x32_bf16 v[32:35], a[0:3], v[180:183], v[32:35]\n"
"ds_read_b64_tr_b16 a[18:19], v3 offset:59840\n"
"v_mfma_f32_16x16x32_bf16 v[32:35], a[4:7], a[220:223], v[32:35]\n"
"ds_read_b64_tr_b16 a[20:21], v7 offset:57792\n"
"v_mfma_f32_16x16x32_bf16 v[36:39], a[4:7], a[224:227], v[36:39]\n"
"ds_read_b64_tr_b16 a[22:23], v7 offset:59840\n"
"v_mfma_f32_16x16x32_bf16 v[36:39], a[0:3], v[184:187], v[36:39]\n"
"ds_read_b64_tr_b16 a[24:25], v3 offset:61888\n"
"v_mfma_f32_16x16x32_bf16 v[44:47], a[0:3], v[192:195], v[44:47]\n"
"ds_read_b64_tr_b16 a[26:27], v3 offset:63936\n"
"v_subrev_u32 v3, s27, v3\n"
"v_mfma_f32_16x16x32_bf16 v[40:43], a[0:3], v[188:191], v[40:43]\n"
"s_waitcnt lgkmcnt(6)\n"
"buffer_load_dwordx4 v12, s[36:39], s29 offen offset:1024 lds\n"
"v_mfma_f32_16x16x32_bf16 v[40:43], a[4:7], v[196:199], v[40:43]\n"
"ds_read_b64_tr_b16 a[28:29], v7 offset:61888\n"
"v_mfma_f32_16x16x32_bf16 v[32:35], a[8:11], v[200:203], v[32:35]\n"
"ds_read_b64_tr_b16 a[30:31], v7 offset:63936\n"
"v_subrev_u32 v7, s27, v7\n"
"v_mfma_f32_16x16x32_bf16 v[32:35], a[12:15], a[228:231], v[32:35]\n"
"ds_read_b64_tr_b16 a[0:1], v4 offset:57792\n"
"v_mfma_f32_16x16x32_bf16 v[36:39], a[12:15], a[232:235], v[36:39]\n"
"ds_read_b64_tr_b16 a[2:3], v4 offset:59840\n"
"v_mfma_f32_16x16x32_bf16 v[36:39], a[8:11], v[204:207], v[36:39]\n"
"ds_read_b64_tr_b16 a[4:5], v8 offset:57792\n"
"v_mfma_f32_16x16x32_bf16 v[44:47], a[8:11], v[212:215], v[44:47]\n"
"s_addk_i32 s93, 4096\n"
"ds_read_b64_tr_b16 a[6:7], v8 offset:59840\n"
"v_mfma_f32_16x16x32_bf16 v[40:43], a[8:11], v[208:211], v[40:43]\n"
"s_waitcnt lgkmcnt(6)\n"
"ds_read_b64_tr_b16 a[8:9], v4 offset:61888\n"
"v_mfma_f32_16x16x32_bf16 v[40:43], a[12:15], a[236:239], v[40:43]\n"
"ds_read_b64_tr_b16 a[10:11], v4 offset:63936\n"
"v_subrev_u32 v4, s27, v4\n"
"v_mfma_f32_16x16x32_bf16 v[80:83], a[16:19], v[180:183], v[80:83]\n"
"buffer_store_dwordx4 a[100:103], v19, s[52:55], s93 offen offset:0 sc1\n"
"v_mfma_f32_16x16x32_bf16 v[80:83], a[20:23], a[220:223], v[80:83]\n"
"ds_read_b64_tr_b16 a[12:13], v8 offset:61888\n"
"v_mfma_f32_16x16x32_bf16 v[84:87], a[20:23], a[224:227], v[84:87]\n"
"ds_read_b64_tr_b16 a[14:15], v8 offset:63936\n"
"v_subrev_u32 v8, s27, v8\n"
"v_mfma_f32_16x16x32_bf16 v[84:87], a[16:19], v[184:187], v[84:87]\n"
"v_mfma_f32_16x16x32_bf16 v[92:95], a[16:19], v[192:195], v[92:95]\n"
"buffer_load_dwordx4 v13, s[36:39], s29 offen offset:2048 lds\n"
"v_mfma_f32_16x16x32_bf16 v[88:91], a[16:19], v[188:191], v[88:91]\n"
"s_waitcnt lgkmcnt(3)\n"
"ds_read_b64_tr_b16 a[16:17], v5 offset:57792\n"
"v_mfma_f32_16x16x32_bf16 v[88:91], a[20:23], v[196:199], v[88:91]\n"
"ds_read_b64_tr_b16 a[18:19], v5 offset:59840\n"
"v_mfma_f32_16x16x32_bf16 v[80:83], a[24:27], v[200:203], v[80:83]\n"
"ds_read_b64_tr_b16 a[20:21], v9 offset:57792\n"
"v_mfma_f32_16x16x32_bf16 v[80:83], a[28:31], a[228:231], v[80:83]\n"
"ds_read_b64_tr_b16 a[22:23], v9 offset:59840\n"
"v_mfma_f32_16x16x32_bf16 v[84:87], a[28:31], a[232:235], v[84:87]\n"
"PG12_%=:\n"
"s_cbranch_execz PGX%=\n"
"v_mfma_f32_16x16x32_bf16 v[84:87], a[24:27], v[204:207], v[84:87]\n"
"v_mfma_f32_16x16x32_bf16 v[92:95], a[24:27], v[212:215], v[92:95]\n"
"v_mfma_f32_16x16x32_bf16 v[88:91], a[24:27], v[208:211], v[88:91]\n"
"ds_read_b64_tr_b16 a[24:25], v5 offset:61888\n"
"v_mfma_f32_16x16x32_bf16 v[88:91], a[28:31], a[236:239], v[88:91]\n"
"ds_read_b64_tr_b16 a[26:27], v5 offset:63936\n"
"v_subrev_u32 v5, s27, v5\n"
"v_mfma_f32_16x16x32_bf16 v[48:51], a[0:3], v[180:183], v[48:51]\n"
"ds_read_b64_tr_b16 a[28:29], v9 offset:61888\n"
"v_mfma_f32_16x16x32_bf16 v[48:51], a[4:7], a[220:223], v[48:51]\n"
"ds_read_b64_tr_b16 a[30:31], v9 offset:63936\n"
"v_subrev_u32 v9, s27, v9\n"
"v_mfma_f32_16x16x32_bf16 v[52:55], a[4:7], a[224:227], v[52:55]\n"
"v_mfma_f32_16x16x32_bf16 v[52:55], a[0:3], v[184:187], v[52:55]\n"
"buffer_load_dwordx4 v14, s[36:39], s29 offen offset:3072 lds\n"
"v_mfma_f32_16x16x32_bf16 v[60:63], a[0:3], v[192:195], v[60:63]\n"
"s_addk_i32 s93, 4096\n"
"v_mfma_f32_16x16x32_bf16 v[56:59], a[0:3], v[188:191], v[56:59]\n"
"s_waitcnt lgkmcnt(3)\n"
"v_mfma_f32_16x16x32_bf16 v[56:59], a[4:7], v[196:199], v[56:59]\n"
"v_mfma_f32_16x16x32_bf16 v[48:51], a[8:11], v[200:203], v[48:51]\n"
"buffer_store_dwordx4 a[104:107], v19, s[52:55], s93 offen offset:0 sc1\n"
"v_mfma_f32_16x16x32_bf16 v[48:51], a[12:15], a[228:231], v[48:51]\n"
"v_mfma_f32_16x16x32_bf16 v[52:55], a[12:15], a[232:235], v[52:55]\n"
"v_mfma_f32_16x16x32_bf16 v[52:55], a[8:11], v[204:207], v[52:55]\n"
"v_mfma_f32_16x16x32_bf16 v[60:63], a[8:11], v[212:215], v[60:63]\n"
"v_mfma_f32_16x16x32_bf16 v[56:59], a[8:11], v[208:211], v[56:59]\n"
"v_mfma_f32_16x16x32_bf16 v[56:59], a[12:15], a[236:239], v[56:59]\n"
"v_mfma_f32_16x16x32_bf16 v[64:67], a[16:19], v[180:183], v[64:67]\n"
"v_mfma_f32_16x16x32_bf16 v[64:67], a[20:23], a[220:223], v[64:67]\n"
"s_waitcnt lgkmcnt(0)\n"
"v_mfma_f32_16x16x32_bf16 v[68:71], a[20:23], a[224:227], v[68:71]\n"
"s_barrier\n"
"v_mfma_f32_16x16x32_bf16 v[68:71], a[16:19], v[184:187], v[68:71]\n"
"s_addk_i32 m0, 8256\n"
"ds_read_b64_tr_b16 a[0:1], v2 offset:33024\n"
"s_add_u32 s29, s67, 57344\n"
"v_mfma_f32_16x16x32_bf16 v[76:79], a[16:19], v[192:195], v[76:79]\n"
"ds_read_b64_tr_b16 a[2:3], v2 offset:35072\n"
"v_mfma_f32_16x16x32_bf16 v[72:75], a[16:19], v[188:191], v[72:75]\n"
"ds_read_b64_tr_b16 a[4:5], v6 offset:33024\n"
"v_mfma_f32_16x16x32_bf16 v[72:75], a[20:23], v[196:199], v[72:75]\n"
"ds_read_b64_tr_b16 a[6:7], v6 offset:35072\n"
"v_mfma_f32_16x16x32_bf16 v[64:67], a[24:27], v[200:203], v[64:67]\n"
"buffer_load_dwordx4 v11, s[36:39], s29 offen offset:0 lds\n"
"v_mfma_f32_16x16x32_bf16 v[64:67], a[28:31], a[228:231], v[64:67]\n"
"ds_read_b64_tr_b16 a[8:9], v2 offset:37120\n"
"v_mfma_f32_16x16x32_bf16 v[68:71], a[28:31], a[232:235], v[68:71]\n"
"ds_read_b64_tr_b16 a[10:11], v2 offset:39168\n"
"v_lshlrev_b32 v2, 4, v1\n"
"v_mfma_f32_16x16x32_bf16 v[68:71], a[24:27], v[204:207], v[68:71]\n"
"s_buffer_load_dwordx8 s[40:47], s[52:55], s95 offset:0 glc\n"
"ds_read_b64_tr_b16 a[12:13], v6 offset:37120\n"
"v_add_u32 v26, s26, v2\n"
"v_mfma_f32_16x16x32_bf16 v[76:79], a[24:27], v[212:215], v[76:79]\n"
"s_addk_i32 s93, 4096\n"
"ds_read_b64_tr_b16 a[14:15], v6 offset:39168\n"
"v_mfma_f32_16x16x32_bf16 v[72:75], a[24:27], v[208:211], v[72:75]\n"
"s_waitcnt lgkmcnt(4)\n"
"ds_read_b64_tr_b16 a[16:17], v3 offset:33024\n"
"v_mfma_f32_16x16x32_bf16 v[72:75], a[28:31], a[236:239], v[72:75]\n"
"ds_read_b64_tr_b16 a[18:19], v3 offset:35072\n"
"v_mfma_f32_16x16x32_bf16 v[32:35], a[0:3], v[216:219], v[32:35]\n"
"buffer_store_dwordx4 a[108:111], v19, s[52:55], s93 offen offset:0 sc1\n"
"v_mfma_f32_16x16x32_bf16 v[32:35], a[4:7], a[240:243], v[32:35]\n"
"ds_read_b64_tr_b16 a[20:21], v7 offset:33024\n"
"v_mfma_f32_16x16x32_bf16 v[36:39], a[4:7], a[244:247], v[36:39]\n"
"ds_read_b64_tr_b16 a[22:23], v7 offset:35072\n"
"v_mfma_f32_16x16x32_bf16 v[36:39], a[0:3], v[220:223], v[36:39]\n"
"ds_read_b64_tr_b16 a[24:25], v3 offset:37120\n"
"v_mfma_f32_16x16x32_bf16 v[44:47], a[0:3], v[228:231], v[44:47]\n"
"ds_read_b64_tr_b16 a[26:27], v3 offset:39168\n"
"v_mfma_f32_16x16x32_bf16 v[40:43], a[0:3], v[224:227], v[40:43]\n"
"s_waitcnt lgkmcnt(6)\n"
"buffer_load_dwordx4 v12, s[36:39], s29 offen offset:1024 lds\n"
"v_mfma_f32_16x16x32_bf16 v[40:43], a[4:7], v[232:235], v[40:43]\n"
"ds_read_b64_tr_b16 a[28:29], v7 offset:37120\n"
"v_mfma_f32_16x16x32_bf16 v[32:35], a[8:11], v[236:239], v[32:35]\n"
"ds_read_b64_tr_b16 a[30:31], v7 offset:39168\n"
"v_mfma_f32_16x16x32_bf16 v[32:35], a[12:15], a[248:251], v[32:35]\n"
"ds_read_b64_tr_b16 a[0:1], v4 offset:33024\n"
"v_mfma_f32_16x16x32_bf16 v[36:39], a[12:15], a[252:255], v[36:39]\n"
"ds_read_b64_tr_b16 a[2:3], v4 offset:35072\n"
"v_mfma_f32_16x16x32_bf16 v[36:39], a[8:11], v[240:243], v[36:39]\n"
"ds_read_b64_tr_b16 a[4:5], v8 offset:33024\n"
"v_mfma_f32_16x16x32_bf16 v[44:47], a[8:11], v[248:251], v[44:47]\n"
"ds_read_b64_tr_b16 a[6:7], v8 offset:35072\n"
"v_mfma_f32_16x16x32_bf16 v[40:43], a[8:11], v[244:247], v[40:43]\n"
"s_waitcnt lgkmcnt(8)\n"
"ds_read_b64_tr_b16 a[8:9], v4 offset:37120\n"
"v_mul_f32 v32, v15, v32\n"
"v_mfma_f32_16x16x32_bf16 v[40:43], a[12:15], v[252:255], v[40:43]\n"
"ds_read_b64_tr_b16 a[10:11], v4 offset:39168\n"
"v_mul_f32 v33, v15, v33\n"
"v_mfma_f32_16x16x32_bf16 v[80:83], a[16:19], v[216:219], v[80:83]\n"
"ds_read_b64_tr_b16 a[12:13], v8 offset:37120\n"
"v_mul_f32 v96, v18, v44\n"
"v_mfma_f32_16x16x32_bf16 v[80:83], a[20:23], a[240:243], v[80:83]\n"
"ds_read_b64_tr_b16 a[14:15], v8 offset:39168\n"
"v_mul_f32 v97, v18, v45\n"
"v_mfma_f32_16x16x32_bf16 v[84:87], a[20:23], a[244:247], v[84:87]\n"
"v_mul_f32 v98, v18, v46\n"
"v_mul_f32 v99, v18, v47\n"
"v_mfma_f32_16x16x32_bf16 v[84:87], a[16:19], v[220:223], v[84:87]\n"
"v_mul_f32 v34, v15, v34\n"
"v_mul_f32 v35, v15, v35\n"
"v_mfma_f32_16x16x32_bf16 v[92:95], a[16:19], v[228:231], v[92:95]\n"
"s_addk_i32 s93, 4096\n"
"v_cvt_pk_bf16_f32 v32, v32, v33\n"
"v_cvt_pk_bf16_f32 v34, v34, v35\n"
"v_mfma_f32_16x16x32_bf16 v[88:91], a[16:19], v[224:227], v[88:91]\n"
"s_waitcnt lgkmcnt(8)\n"
"ds_read_b64_tr_b16 a[16:17], v5 offset:33024\n"
"v_accvgpr_write_b32 a116, v32\n"
"v_mfma_f32_16x16x32_bf16 v[88:91], a[20:23], v[232:235], v[88:91]\n"
"ds_read_b64_tr_b16 a[18:19], v5 offset:35072\n"
"v_accvgpr_write_b32 a117, v34\n"
"v_mfma_f32_16x16x32_bf16 v[80:83], a[24:27], v[236:239], v[80:83]\n"
"buffer_store_dwordx4 a[112:115], v19, s[52:55], s93 offen offset:0 sc1\n"
"v_mfma_f32_16x16x32_bf16 v[80:83], a[28:31], a[248:251], v[80:83]\n"
"ds_read_b64_tr_b16 a[20:21], v9 offset:33024\n"
"v_mul_f32 v36, v16, v36\n"
"v_mfma_f32_16x16x32_bf16 v[84:87], a[28:31], a[252:255], v[84:87]\n"
"ds_read_b64_tr_b16 a[22:23], v9 offset:35072\n"
"v_mul_f32 v37, v16, v37\n"
"v_mfma_f32_16x16x32_bf16 v[84:87], a[24:27], v[240:243], v[84:87]\n"
"v_mul_f32 v38, v16, v38\n"
"v_mul_f32 v39, v16, v39\n"
"v_mfma_f32_16x16x32_bf16 v[92:95], a[24:27], v[248:251], v[92:95]\n"
"buffer_load_dwordx4 v13, s[36:39], s29 offen offset:2048 lds\n"
"v_mfma_f32_16x16x32_bf16 v[88:91], a[24:27], v[244:247], v[88:91]\n"
"s_waitcnt lgkmcnt(8)\n"
"ds_read_b64_tr_b16 a[24:25], v5 offset:37120\n"
"v_mul_f32 v80, v15, v80\n"
"v_mfma_f32_16x16x32_bf16 v[88:91], a[28:31], v[252:255], v[88:91]\n"
"ds_read_b64_tr_b16 a[26:27], v5 offset:39168\n"
"v_mul_f32 v81, v15, v81\n"
"v_mfma_f32_16x16x32_bf16 v[48:51], a[0:3], v[216:219], v[48:51]\n"
"ds_read_b64_tr_b16 a[28:29], v9 offset:37120\n"
"v_mul_f32 v82, v15, v82\n"
"v_mfma_f32_16x16x32_bf16 v[48:51], a[4:7], a[240:243], v[48:51]\n"
"ds_read_b64_tr_b16 a[30:31], v9 offset:39168\n"
"v_mul_f32 v83, v15, v83\n"
"v_mfma_f32_16x16x32_bf16 v[52:55], a[4:7], a[244:247], v[52:55]\n"
"v_cvt_pk_bf16_f32 v80, v80, v81\n"
"v_cvt_pk_bf16_f32 v82, v82, v83\n"
"v_mfma_f32_16x16x32_bf16 v[52:55], a[0:3], v[220:223], v[52:55]\n"
"buffer_load_dwordx4 v14, s[36:39], s29 offen offset:3072 lds\n"
"v_mfma_f32_16x16x32_bf16 v[60:63], a[0:3], v[228:231], v[60:63]\n"
"v_accvgpr_write_b32 a118, v80\n"
"v_accvgpr_write_b32 a119, v82\n"
"v_mfma_f32_16x16x32_bf16 v[56:59], a[0:3], v[224:227], v[56:59]\n"
"s_waitcnt lgkmcnt(8)\n"
"v_mul_f32 v84, v16, v84\n"
"v_mul_f32 v85, v16, v85\n"
"v_mfma_f32_16x16x32_bf16 v[56:59], a[4:7], v[232:235], v[56:59]\n"
"v_mul_f32 v86, v16, v86\n"
"v_mul_f32 v87, v16, v87\n"
"v_mfma_f32_16x16x32_bf16 v[48:51], a[8:11], v[236:239], v[48:51]\n"
"v_cvt_pk_bf16_f32 v36, v36, v37\n"
"v_cvt_pk_bf16_f32 v38, v38, v39\n"
"v_mfma_f32_16x16x32_bf16 v[48:51], a[12:15], a[248:251], v[48:51]\n"
"v_cvt_pk_bf16_f32 v84, v84, v85\n"
"v_cvt_pk_bf16_f32 v86, v86, v87\n"
"v_mfma_f32_16x16x32_bf16 v[52:55], a[12:15], a[252:255], v[52:55]\n"
"v_accvgpr_write_b32 a124, v36\n"
"v_accvgpr_write_b32 a125, v38\n"
"v_mfma_f32_16x16x32_bf16 v[52:55], a[8:11], v[240:243], v[52:55]\n"
"v_accvgpr_write_b32 a126, v84\n"
"v_accvgpr_write_b32 a127, v86\n"
"v_mfma_f32_16x16x32_bf16 v[60:63], a[8:11], v[248:251], v[60:63]\n"
"v_mul_f32 v48, v15, v48\n"
"v_mul_f32 v49, v15, v49\n"
"v_mfma_f32_16x16x32_bf16 v[56:59], a[8:11], v[244:247], v[56:59]\n"
"s_waitcnt lgkmcnt(4)\n"
"v_mul_f32 v50, v15, v50\n"
"v_mul_f32 v51, v15, v51\n"
"v_mfma_f32_16x16x32_bf16 v[56:59], a[12:15], v[252:255], v[56:59]\n"
"v_cvt_pk_bf16_f32 v48, v48, v49\n"
"v_cvt_pk_bf16_f32 v50, v50, v51\n"
"v_mfma_f32_16x16x32_bf16 v[64:67], a[16:19], v[216:219], v[64:67]\n"
"ds_write_b128 v23, v[60:63] offset:0\n"
"v_mfma_f32_16x16x32_bf16 v[64:67], a[20:23], a[240:243], v[64:67]\n"
"s_waitcnt vmcnt(20) lgkmcnt(0)\n"
"v_accvgpr_write_b32 a120, v48\n"
"v_accvgpr_write_b32 a121, v50\n"
"v_mfma_f32_16x16x32_bf16 v[68:71], a[20:23], a[244:247], v[68:71]\n"
"s_barrier\n"
"v_mfma_f32_16x16x32_bf16 v[68:71], a[16:19], v[220:223], v[68:71]\n"
"s_add_u32 m0, s63, 33024\n"
"ds_read_b64_tr_b16 a[0:1], v21 offset:0\n"
"v_mul_f32 v52, v16, v52\n"
"v_mfma_f32_16x16x32_bf16 v[76:79], a[16:19], v[228:231], v[76:79]\n"
"s_add_u32 s29, s68, 57344\n"
"buffer_store_dwordx2 v[30:31], v29, s[52:55], 0 offen offset:128 sc1\n"
"v_mfma_f32_16x16x32_bf16 v[72:75], a[16:19], v[224:227], v[72:75]\n"
"ds_read_b64_tr_b16 a[2:3], v21 offset:4096\n"
"v_mul_f32 v53, v16, v53\n"
"v_mfma_f32_16x16x32_bf16 v[72:75], a[20:23], v[232:235], v[72:75]\n"
"s_xor_b64 s[40:41], s[40:41], s[88:89]\n"
"ds_read_b64_tr_b16 a[4:5], v22 offset:0\n"
"v_mul_f32 v54, v16, v54\n"
"v_mfma_f32_16x16x32_bf16 v[64:67], a[24:27], v[236:239], v[64:67]\n"
"s_xor_b64 s[42:43], s[42:43], s[88:89]\n"
"ds_read_b64_tr_b16 a[6:7], v22 offset:4096\n"
"v_mul_f32 v55, v16, v55\n"
"v_mfma_f32_16x16x32_bf16 v[64:67], a[28:31], a[248:251], v[64:67]\n"
"s_xor_b64 s[44:45], s[44:45], s[88:89]\n"
"buffer_load_dwordx4 v11, s[36:39], s29 offen offset:0 lds\n"
"v_mfma_f32_16x16x32_bf16 v[68:71], a[28:31], a[252:255], v[68:71]\n"
"s_xor_b64 s[46:47], s[46:47], s[88:89]\n"
"ds_read_b64_tr_b16 a[8:9], v21 offset:32\n"
"v_cvt_pk_bf16_f32 v52, v52, v53\n"
"v_mfma_f32_16x16x32_bf16 v[68:71], a[24:27], v[240:243], v[68:71]\n"
"s_or_b64 s[40:41], s[40:41], s[42:43]\n"
"ds_read_b64_tr_b16 a[10:11], v21 offset:4128\n"
"v_mul_f32 v64, v15, v64\n"
"v_mfma_f32_16x16x32_bf16 v[76:79], a[24:27], v[248:251], v[76:79]\n"
"s_or_b64 s[44:45], s[44:45], s[46:47]\n"
"buffer_load_dwordx4 v12, s[36:39], s29 offen offset:1024 lds\n"
"v_mfma_f32_16x16x32_bf16 v[72:75], a[24:27], v[244:247], v[72:75]\n"
"s_waitcnt lgkmcnt(2)\n"
"ds_read_b64_tr_b16 a[12:13], v22 offset:32\n"
"v_mul_f32 v65, v15, v65\n"
"v_mfma_f32_16x16x32_bf16 v[72:75], a[28:31], v[252:255], v[72:75]\n"
"s_add_u32 s90, s57, 0\n"
"ds_read_b64_tr_b16 a[14:15], v22 offset:4128\n"
"v_mul_f32 v66, v15, v66\n"
"v_mfma_f32_16x16x32_bf16 v[144:147], a[0:3], a[32:35], 0\n"
"s_or_b64 s[40:41], s[40:41], s[44:45]\n"
"s_cbranch_scc1 POLL0%=\n"
"BACK0%=:\n"
"v_mul_f32 v67, v15, v67\n"
"v_mfma_f32_16x16x32_bf16 v[144:147], a[4:7], a[36:39], v[144:147]\n"
"ds_read_b64_tr_b16 a[16:17], v21 offset:64\n"
"v_cvt_pk_bf16_f32 v64, v64, v65\n"
"v_mfma_f32_16x16x32_bf16 v[148:151], a[4:7], a[44:47], 0\n"
"buffer_load_dwordx4 a[144:147], v19, s[52:55], s90 offen offset:0 sc1\n"
"v_mfma_f32_16x16x32_bf16 v[148:151], a[0:3], a[40:43], v[148:151]\n"
"ds_read_b64_tr_b16 a[18:19], v21 offset:4160\n"
"v_cvt_pk_bf16_f32 v66, v66, v67\n"
"v_mfma_f32_16x16x32_bf16 v[156:159], a[0:3], a[56:59], 0\n"
"ds_read_b64_tr_b16 a[20:21], v22 offset:64\n"
"v_accvgpr_write_b32 a122, v64\n"
"v_mfma_f32_16x16x32_bf16 v[152:155], a[0:3], a[48:51], 0\n"
"s_waitcnt lgkmcnt(3)\n"
"ds_read_b64_tr_b16 a[22:23], v22 offset:4160\n"
"v_accvgpr_write_b32 a123, v66\n"
"v_mfma_f32_16x16x32_bf16 v[152:155], a[4:7], a[52:55], v[152:155]\n"
"buffer_load_dwordx4 v13, s[36:39], s29 offen offset:2048 lds\n"
"v_mfma_f32_16x16x32_bf16 v[160:163], a[8:11], a[32:35], 0\n"
"ds_read_b64_tr_b16 a[24:25], v21 offset:96\n"
"v_mul_f32 v68, v16, v68\n"
"v_mfma_f32_16x16x32_bf16 v[160:163], a[12:15], a[36:39], v[160:163]\n"
"ds_read_b64_tr_b16 a[26:27], v21 offset:4192\n"
"v_mul_f32 v69, v16, v69\n"
"v_mfma_f32_16x16x32_bf16 v[164:167], a[12:15], a[44:47], 0\n"
"ds_read_b64_tr_b16 a[28:29], v22 offset:96\n"
"v_mul_f32 v70, v16, v70\n"
"v_mfma_f32_16x16x32_bf16 v[164:167], a[8:11], a[40:43], v[164:167]\n"
"ds_read_b64_tr_b16 a[30:31], v22 offset:4192\n"
"v_mul_f32 v71, v16, v71\n"
"v_mfma_f32_16x16x32_bf16 v[172:175], a[8:11], a[56:59], 0\n"
"buffer_load_dwordx4 v14, s[36:39], s29 offen offset:3072 lds\n"
"v_mfma_f32_16x16x32_bf16 v[168:171], a[8:11], a[48:51], 0\n"
"s_waitcnt lgkmcnt(4)\n"
"ds_read_b64_tr_b16 a[0:1], v21 offset:128\n"
"v_cvt_pk_bf16_f32 v54, v54, v55\n"
"v_mfma_f32_16x16x32_bf16 v[168:171], a[12:15], a[52:55], v[168:171]\n"
"s_addk_i32 s90, 4096\n"
"ds_read_b64_tr_b16 a[2:3], v21 offset:4224\n"
"v_cvt_pk_bf16_f32 v68, v68, v69\n"
"v_mfma_f32_16x16x32_bf16 v[176:179], a[16:19], a[32:35], 0\n"
"ds_read_b64_tr_b16 a[4:5], v22 offset:128\n"
"v_cvt_pk_bf16_f32 v70, v70, v71\n"
"v_mfma_f32_16x16x32_bf16 v[176:179], a[20:23], a[36:39], v[176:179]\n"
"ds_read_b64_tr_b16 a[6:7], v22 offset:4224\n"
"v_accvgpr_write_b32 a128, v52\n"
"v_mfma_f32_16x16x32_bf16 v[180:183], a[20:23], a[44:47], 0\n"
"buffer_load_dwordx4 a[148:151], v19, s[52:55], s90 offen offset:0 sc1\n"
"v_mfma_f32_16x16x32_bf16 v[180:183], a[16:19], a[40:43], v[180:183]\n"
"ds_read_b64_tr_b16 a[8:9], v21 offset:160\n"
"v_accvgpr_write_b32 a129, v54\n"
"v_mfma_f32_16x16x32_bf16 v[188:191], a[16:19], a[56:59], 0\n"
"ds_read_b64_tr_b16 a[10:11], v21 offset:4256\n"
"v_accvgpr_write_b32 a130, v68\n"
"v_mfma_f32_16x16x32_bf16 v[184:187], a[16:19], a[48:51], 0\n"
"s_waitcnt lgkmcnt(5)\n"
"ds_read_b64_tr_b16 a[12:13], v22 offset:160\n"
"v_accvgpr_write_b32 a131, v70\n"
"v_mfma_f32_16x16x32_bf16 v[184:187], a[20:23], a[52:55], v[184:187]\n"
"ds_read_b64_tr_b16 a[14:15], v22 offset:4256\n"
"v_mul_f32 v100, v18, v92\n"
"v_mfma_f32_16x16x32_bf16 v[192:195], a[24:27], a[32:35], 0\n"
"ds_write_b128 v23, v[76:79] offset:1024\n"
"v_mfma_f32_16x16x32_bf16 v[192:195], a[28:31], a[36:39], v[192:195]\n"
"ds_read_b64_tr_b16 a[16:17], v21 offset:192\n"
"v_mul_f32 v101, v18, v93\n"
"v_mfma_f32_16x16x32_bf16 v[196:199], a[28:31], a[44:47], 0\n"
"ds_read_b64_tr_b16 a[18:19], v21 offset:4288\n"
"v_mul_f32 v102, v18, v94\n"
"v_mfma_f32_16x16x32_bf16 v[196:199], a[24:27], a[40:43], v[196:199]\n"
"ds_read_b64_tr_b16 a[20:21], v22 offset:192\n"
"v_mul_f32 v103, v18, v95\n"
"v_mfma_f32_16x16x32_bf16 v[204:207], a[24:27], a[56:59], 0\n"
"ds_read_b64_tr_b16 a[22:23], v22 offset:4288\n"
"v_mul_f32 v40, v17, v40\n"
"v_mfma_f32_16x16x32_bf16 v[200:203], a[24:27], a[48:51], 0\n"
"s_waitcnt lgkmcnt(6)\n"
"v_mul_f32 v41, v17, v41\n"
"v_mul_f32 v42, v17, v42\n"
"v_mfma_f32_16x16x32_bf16 v[200:203], a[28:31], a[52:55], v[200:203]\n"
"s_addk_i32 s90, 4096\n"
"v_mul_f32 v43, v17, v43\n"
"v_mul_f32 v88, v17, v88\n"
"v_mfma_f32_16x16x32_bf16 v[208:211], a[0:3], a[32:35], 0\n"
"v_mul_f32 v89, v17, v89\n"
"v_mul_f32 v90, v17, v90\n"
"v_mfma_f32_16x16x32_bf16 v[208:211], a[4:7], a[36:39], v[208:211]\n"
"v_mul_f32 v91, v17, v91\n"
"v_cvt_pk_bf16_f32 v40, v40, v41\n"
"v_mfma_f32_16x16x32_bf16 v[212:215], a[4:7], a[44:47], 0\n"
"buffer_load_dwordx4 a[152:155], v19, s[52:55], s90 offen offset:0 sc1\n"
"v_mfma_f32_16x16x32_bf16 v[212:215], a[0:3], a[40:43], v[212:215]\n"
"v_cvt_pk_bf16_f32 v42, v42, v43\n"
"v_cvt_pk_bf16_f32 v88, v88, v89\n"
"v_mfma_f32_16x16x32_bf16 v[220:223], a[0:3], a[56:59], 0\n"
"v_cvt_pk_bf16_f32 v90, v90, v91\n"
"v_mul_f32 v56, v17, v56\n"
"v_mfma_f32_16x16x32_bf16 v[216:219], a[0:3], a[48:51], 0\n"
"s_waitcnt lgkmcnt(0)\n"
"v_mul_f32 v57, v17, v57\n"
"v_mul_f32 v58, v17, v58\n"
"v_mfma_f32_16x16x32_bf16 v[216:219], a[4:7], a[52:55], v[216:219]\n"
"v_mul_f32 v59, v17, v59\n"
"v_mul_f32 v72, v17, v72\n"
"v_mfma_f32_16x16x32_bf16 v[224:227], a[8:11], a[32:35], 0\n"
"v_mul_f32 v73, v17, v73\n"
"v_mul_f32 v74, v17, v74\n"
"v_mfma_f32_16x16x32_bf16 v[224:227], a[12:15], a[36:39], v[224:227]\n"
"v_mul_f32 v75, v17, v75\n"
"v_accvgpr_write_b32 a132, v40\n"
"v_mfma_f32_16x16x32_bf16 v[228:231], a[12:15], a[44:47], 0\n"
"s_barrier\n"
"v_mfma_f32_16x16x32_bf16 v[228:231], a[8:11], a[40:43], v[228:231]\n"
"ds_read_b64_tr_b16 a[24:25], v21 offset:8256\n"
"v_accvgpr_write_b32 a133, v42\n"
"v_mfma_f32_16x16x32_bf16 v[236:239], a[8:11], a[56:59], 0\n"
"ds_read_b64_tr_b16 a[26:27], v21 offset:12352\n"
"v_accvgpr_write_b32 a134, v88\n"
"v_mfma_f32_16x16x32_bf16 v[232:235], a[8:11], a[48:51], 0\n"
"ds_read_b64_tr_b16 a[28:29], v22 offset:8256\n"
"v_accvgpr_write_b32 a135, v90\n"
"v_mfma_f32_16x16x32_bf16 v[232:235], a[12:15], a[52:55], v[232:235]\n"
"s_addk_i32 s90, 4096\n"
"ds_read_b64_tr_b16 a[30:31], v22 offset:12352\n"
"v_cvt_pk_bf16_f32 v56, v56, v57\n"
"v_mfma_f32_16x16x32_bf16 v[240:243], a[16:19], a[32:35], 0\n"
"ds_read_b64_tr_b16 a[0:1], v21 offset:8288\n"
"v_cvt_pk_bf16_f32 v58, v58, v59\n"
"v_mfma_f32_16x16x32_bf16 v[240:243], a[20:23], a[36:39], v[240:243]\n"
"ds_read_b64_tr_b16 a[2:3], v21 offset:12384\n"
"v_cvt_pk_bf16_f32 v72, v72, v73\n"
"v_mfma_f32_16x16x32_bf16 v[244:247], a[20:23], a[44:47], 0\n"
"buffer_load_dwordx4 a[156:159], v19, s[52:55], s90 offen offset:0 sc1\n"
"v_mfma_f32_16x16x32_bf16 v[244:247], a[16:19], a[40:43], v[244:247]\n"
"s_add_u32 s28, s58, 0\n"
"ds_read_b64_tr_b16 a[4:5], v22 offset:8288\n"
"v_cvt_pk_bf16_f32 v74, v74, v75\n"
"v_mfma_f32_16x16x32_bf16 v[252:255], a[16:19], a[56:59], 0\n"
"ds_read_b64_tr_b16 a[6:7], v22 offset:12384\n"
"v_accvgpr_write_b32 a136, v56\n"
"v_mfma_f32_16x16x32_bf16 v[248:251], a[16:19], a[48:51], 0\n"
"s_waitcnt lgkmcnt(4)\n"
"ds_read_b64_tr_b16 a[8:9], v21 offset:8320\n"
"v_accvgpr_write_b32 a137, v58\n"
"v_mfma_f32_16x16x32_bf16 v[248:251], a[20:23], a[52:55], v[248:251]\n"
"buffer_load_dwordx4 a[172:175], v19, s[52:55], s28 offen offset:0 sc1\n"
"v_mfma_f32_16x16x32_bf16 v[144:147], a[24:27], a[60:63], v[144:147]\n"
"ds_read_b64_tr_b16 a[10:11], v21 offset:12416\n"
"v_accvgpr_write_b32 a138, v72\n"
"v_mfma_f32_16x16x32_bf16 v[144:147], a[28:31], a[64:67], v[144:147]\n"
"s_add_u32 s30, s59, 0\n"
"ds_read_b64_tr_b16 a[12:13], v22 offset:8320\n"
"v_accvgpr_write_b32 a139, v74\n"
"v_mfma_f32_16x16x32_bf16 v[148:151], a[28:31], a[72:75], v[148:151]\n"
"ds_read_b64_tr_b16 a[14:15], v22 offset:12416\n"
"v_bfe_u32 v4, v1, 1, 2\n"
"v_mfma_f32_16x16x32_bf16 v[148:151], a[24:27], a[68:71], v[148:151]\n"
"ds_read_b64_tr_b16 a[16:17], v21 offset:8352\n"
"v_lshrrev_b32 v8, 4, v1\n"
"v_mfma_f32_16x16x32_bf16 v[156:159], a[24:27], a[84:87], v[156:159]\n"
"buffer_load_dwordx4 v[128:131], v19, s[52:55], s30 offen offset:0 sc1\n"
"v_mfma_f32_16x16x32_bf16 v[152:155], a[24:27], a[76:79], v[152:155]\n"
"s_waitcnt lgkmcnt(4)\n"
"ds_read_b64_tr_b16 a[18:19], v21 offset:12448\n"
"v_xor_b32 v4, v4, v25\n"
"v_mfma_f32_16x16x32_bf16 v[152:155], a[28:31], a[80:83], v[152:155]\n"
"s_addk_i32 s90, 4096\n"
"ds_read_b64_tr_b16 a[20:21], v22 offset:8352\n"
"v_lshl_add_u32 v27, v8, 4, v27\n"
"v_mfma_f32_16x16x32_bf16 v[160:163], a[0:3], a[60:63], v[160:163]\n"
"ds_read_b64_tr_b16 a[22:23], v22 offset:12448\n"
"v_lshlrev_b32 v4, 4, v4\n"
"v_mfma_f32_16x16x32_bf16 v[160:163], a[4:7], a[64:67], v[160:163]\n"
"ds_read_b64_tr_b16 a[24:25], v21 offset:8384\n"
"v_lshl_add_u32 v4, v10, 6, v4\n"
"v_mfma_f32_16x16x32_bf16 v[164:167], a[4:7], a[72:75], v[164:167]\n"
"buffer_load_dwordx4 a[160:163], v19, s[52:55], s90 offen offset:0 sc1\n"
"v_mfma_f32_16x16x32_bf16 v[164:167], a[0:3], a[68:71], v[164:167]\n"
"s_addk_i32 s28, 4096\n"
"ds_read_b64_tr_b16 a[26:27], v21 offset:12480\n"
"v_and_b32 v8, 1, v8\n"
"v_mfma_f32_16x16x32_bf16 v[172:175], a[0:3], a[84:87], v[172:175]\n"
"ds_read_b64_tr_b16 a[28:29], v22 offset:8384\n"
"v_lshl_add_u32 v4, v8, 3, v4\n"
"v_mfma_f32_16x16x32_bf16 v[168:171], a[0:3], a[76:79], v[168:171]\n"
"s_waitcnt lgkmcnt(5)\n"
"ds_read_b64_tr_b16 a[30:31], v22 offset:12480\n"
"v_and_b32 v8, 3, v1\n"
"v_mfma_f32_16x16x32_bf16 v[168:171], a[4:7], a[80:83], v[168:171]\n"
"buffer_load_dwordx4 a[176:179], v19, s[52:55], s28 offen offset:0 sc1\n"
"v_mfma_f32_16x16x32_bf16 v[176:179], a[8:11], a[60:63], v[176:179]\n"
"ds_read_b128 v[104:107], v24 offset:0\n"
"v_bfe_u32 v5, v1, 3, 2\n"
"v_mfma_f32_16x16x32_bf16 v[176:179], a[12:15], a[64:67], v[176:179]\n"
"s_addk_i32 s30, 4096\n"
"ds_read_b64_tr_b16 a[0:1], v21 offset:8416\n"
"v_lshrrev_b32 v10, 2, v1\n"
"v_mfma_f32_16x16x32_bf16 v[180:183], a[12:15], a[72:75], v[180:183]\n"
"ds_read_b64_tr_b16 a[2:3], v21 offset:12512\n"
"v_xor_b32 v5, v5, v8\n"
"v_mfma_f32_16x16x32_bf16 v[180:183], a[8:11], a[68:71], v[180:183]\n"
"ds_read_b64_tr_b16 a[4:5], v22 offset:8416\n"
"v_lshlrev_b32 v5, 4, v5\n"
"v_mfma_f32_16x16x32_bf16 v[188:191], a[8:11], a[84:87], v[188:191]\n"
"buffer_load_dwordx4 v[132:135], v19, s[52:55], s30 offen offset:0 sc1\n"
"v_mfma_f32_16x16x32_bf16 v[184:187], a[8:11], a[76:79], v[184:187]\n"
"s_waitcnt lgkmcnt(4)\n"
"ds_read_b64_tr_b16 a[6:7], v22 offset:12512\n"
"v_mul_u32_u24 v28, 144, v10\n"
"v_mfma_f32_16x16x32_bf16 v[184:187], a[12:15], a[80:83], v[184:187]\n"
"s_addk_i32 s90, 4096\n"
"ds_read_b64_tr_b16 a[8:9], v21 offset:8448\n"
"v_mul_u32_u24 v3, 1792, v10\n"
"v_mfma_f32_16x16x32_bf16 v[192:195], a[16:19], a[60:63], v[192:195]\n"
"s_waitcnt lgkmcnt(5)\n"
"ds_read_b64_tr_b16 a[10:11], v21 offset:12544\n"
"v_fmac_f32 v96, v18, v104\n"
"v_mfma_f32_16x16x32_bf16 v[192:195], a[20:23], a[64:67], v[192:195]\n"
"ds_read_b64_tr_b16 a[12:13], v22 offset:8448\n"
"v_fmac_f32 v97, v18, v105\n"
"v_mfma_f32_16x16x32_bf16 v[196:199], a[20:23], a[72:75], v[196:199]\n"
"buffer_load_dwordx4 a[164:167], v19, s[52:55], s90 offen offset:0 sc1\n"
"v_mfma_f32_16x16x32_bf16 v[196:199], a[16:19], a[68:71], v[196:199]\n"
"s_addk_i32 s28, 4096\n"
"ds_read_b64_tr_b16 a[14:15], v22 offset:12544\n"
"v_fmac_f32 v98, v18, v106\n"
"v_mfma_f32_16x16x32_bf16 v[204:207], a[16:19], a[84:87], v[204:207]\n"
"v_fmac_f32 v99, v18, v107\n"
"v_cvt_pk_bf16_f32 v96, v96, v97\n"
"v_mfma_f32_16x16x32_bf16 v[200:203], a[16:19], a[76:79], v[200:203]\n"
"ds_read_b128 v[104:107], v24 offset:1024\n"
"v_cvt_pk_bf16_f32 v98, v98, v99\n"
"v_mfma_f32_16x16x32_bf16 v[200:203], a[20:23], a[80:83], v[200:203]\n"
"buffer_load_dwordx4 v[108:111], v19, s[52:55], s28 offen offset:0 sc1\n"
"v_mfma_f32_16x16x32_bf16 v[208:211], a[24:27], a[60:63], v[208:211]\n"
"v_accvgpr_write_b32 a140, v96\n"
"v_accvgpr_write_b32 a141, v98\n"
"v_mfma_f32_16x16x32_bf16 v[208:211], a[28:31], a[64:67], v[208:211]\n"
"s_addk_i32 s30, 4096\n"
"v_xor_b32 v25, 32, v4\n"
"v_lshl_add_u32 v5, v10, 6, v5\n"
"v_mfma_f32_16x16x32_bf16 v[212:215], a[28:31], a[72:75], v[212:215]\n"
"v_lshl_add_u32 v28, v8, 5, v28\n"
"v_lshl_add_u32 v3, v8, 4, v3\n"
"v_mfma_f32_16x16x32_bf16 v[212:215], a[24:27], a[68:71], v[212:215]\n"
"s_waitcnt lgkmcnt(0)\n"
"v_fmac_f32 v100, v18, v104\n"
"v_fmac_f32 v101, v18, v105\n"
"v_mfma_f32_16x16x32_bf16 v[220:223], a[24:27], a[84:87], v[220:223]\n"
"buffer_load_dwordx4 v[136:139], v19, s[52:55], s30 offen offset:0 sc1\n"
"v_mfma_f32_16x16x32_bf16 v[216:219], a[24:27], a[76:79], v[216:219]\n"
"v_fmac_f32 v102, v18, v106\n"
"v_fmac_f32 v103, v18, v107\n"
"v_mfma_f32_16x16x32_bf16 v[216:219], a[28:31], a[80:83], v[216:219]\n"
"s_addk_i32 s90, 4096\n"
"v_cvt_pk_bf16_f32 v100, v100, v101\n"
"v_cvt_pk_bf16_f32 v102, v102, v103\n"
"v_mfma_f32_16x16x32_bf16 v[224:227], a[0:3], a[60:63], v[224:227]\n"
"v_accvgpr_write_b32 a142, v100\n"
"v_accvgpr_write_b32 a143, v102\n"
"v_mfma_f32_16x16x32_bf16 v[224:227], a[4:7], a[64:67], v[224:227]\n"
"s_waitcnt vmcnt(19)\n"
"v_add_u32 v4, s85, v4\n"
"v_add_u32 v25, s85, v25\n"
"v_mfma_f32_16x16x32_bf16 v[228:231], a[4:7], a[72:75], v[228:231]\n"
"s_barrier\n"
"v_mfma_f32_16x16x32_bf16 v[228:231], a[0:3], a[68:71], v[228:231]\n"
"buffer_load_dwordx4 a[168:171], v19, s[52:55], s90 offen offset:0 sc1\n"
"v_mfma_f32_16x16x32_bf16 v[236:239], a[0:3], a[84:87], v[236:239]\n"
"ds_read_b64_tr_b16 a[16:17], v21 offset:16512\n"
"v_add_u32 v5, s85, v5\n"
"v_mfma_f32_16x16x32_bf16 v[232:235], a[0:3], a[76:79], v[232:235]\n"
"ds_read_b64_tr_b16 a[18:19], v21 offset:20608\n"
"v_xor_b32 v8, 2, v8\n"
"v_mfma_f32_16x16x32_bf16 v[232:235], a[4:7], a[80:83], v[232:235]\n"
"s_addk_i32 s28, 4096\n"
"ds_read_b64_tr_b16 a[20:21], v22 offset:16512\n"
"v_add_u32 v3, s82, v3\n"
"v_mfma_f32_16x16x32_bf16 v[240:243], a[8:11], a[60:63], v[240:243]\n"
"s_add_u32 s85, s62, 66048\n"
"ds_read_b64_tr_b16 a[22:23], v22 offset:20608\n"
"v_add_u32 v28, s85, v28\n"
"v_mfma_f32_16x16x32_bf16 v[240:243], a[12:15], a[64:67], v[240:243]\n"
"buffer_store_dwordx2 v[30:31], v29, s[52:55], 0 offen offset:256 sc1\n"
"v_mfma_f32_16x16x32_bf16 v[244:247], a[12:15], a[72:75], v[244:247]\n"
"buffer_load_dwordx4 v[112:115], v19, s[52:55], s28 offen offset:0 sc1\n"
"v_mfma_f32_16x16x32_bf16 v[244:247], a[8:11], a[68:71], v[244:247]\n"
"ds_read_b64_tr_b16 a[24:25], v21 offset:16544\n"
"v_add_u32 v27, s85, v27\n"
"v_mfma_f32_16x16x32_bf16 v[252:255], a[8:11], a[84:87], v[252:255]\n"
"s_mul_i32 s85, s61, -8256\n"
"ds_read_b64_tr_b16 a[26:27], v21 offset:20640\n"
"v_and_b32 v10, 15, v1\n"
"v_mfma_f32_16x16x32_bf16 v[248:251], a[8:11], a[76:79], v[248:251]\n"
"s_waitcnt lgkmcnt(2)\n"
"ds_read_b64_tr_b16 a[28:29], v22 offset:16544\n"
"s_addk_i32 s85, 4128\n"
"v_mfma_f32_16x16x32_bf16 v[248:251], a[12:15], a[80:83], v[248:251]\n"
"s_addk_i32 s93, 4096\n"
"ds_read_b64_tr_b16 a[30:31], v22 offset:20640\n"
"v_add_u32 v9, s85, v28\n"
"v_mfma_f32_16x16x32_bf16 v[144:147], a[16:19], a[88:91], v[144:147]\n"
"s_cmp_eq_u32 s91, 0\n"
"s_cselect_b32 s85, 2, 0\n"
"ds_read_b64_tr_b16 a[0:1], v21 offset:16576\n"
"v_and_b32 v8, s85, v8\n"
"v_mfma_f32_16x16x32_bf16 v[144:147], a[20:23], a[92:95], v[144:147]\n"
"s_mov_b32 s85, 0x3f80\n"
"ds_read_b64_tr_b16 a[2:3], v21 offset:20672\n"
"v_lshl_add_u32 v3, v8, 28, v3\n"
"v_mfma_f32_16x16x32_bf16 v[148:151], a[20:23], a[100:103], v[148:151]\n"
"s_buffer_load_dwordx8 s[40:47], s[52:55], s95 offset:128 glc\n"
"buffer_store_dwordx4 a[116:119], v19, s[52:55], s93 offen offset:0 sc1\n"
"v_mfma_f32_16x16x32_bf16 v[148:151], a[16:19], a[96:99], v[148:151]\n"
"s_addk_i32 s28, 4096\n"
"ds_read_b64_tr_b16 a[4:5], v22 offset:16576\n"
"v_lshrrev_b32 v8, 2, v1\n"
"v_mfma_f32_16x16x32_bf16 v[156:159], a[16:19], a[112:115], v[156:159]\n"
"ds_read_b64_tr_b16 a[6:7], v22 offset:20672\n"
"v_and_b32 v8, 12, v8\n"
"v_mfma_f32_16x16x32_bf16 v[152:155], a[16:19], a[104:107], v[152:155]\n"
"s_waitcnt lgkmcnt(4)\n"
"ds_read_b64_tr_b16 a[8:9], v21 offset:16608\n"
"v_sub_u32 v10, v10, v8\n"
"v_mfma_f32_16x16x32_bf16 v[152:155], a[20:23], a[108:111], v[152:155]\n"
"buffer_load_dwordx4 v[116:119], v19, s[52:55], s28 offen offset:0 sc1\n"
"v_mfma_f32_16x16x32_bf16 v[160:163], a[24:27], a[88:91], v[160:163]\n"
"ds_read_b64_tr_b16 a[10:11], v21 offset:20704\n"
"v_mfma_f32_16x16x32_bf16 v[160:163], a[28:31], a[92:95], v[160:163]\n"
"s_addk_i32 s30, 4096\n"
"ds_read_b64_tr_b16 a[12:13], v22 offset:16608\n"
"v_mfma_f32_16x16x32_bf16 v[164:167], a[28:31], a[100:103], v[164:167]\n"
"ds_read_b64_tr_b16 a[14:15], v22 offset:20704\n"
"v_mfma_f32_16x16x32_bf16 v[164:167], a[24:27], a[96:99], v[164:167]\n"
"ds_read_b64_tr_b16 a[16:17], v21 offset:16640\n"
"v_mfma_f32_16x16x32_bf16 v[172:175], a[24:27], a[112:115], v[172:175]\n"
"buffer_load_dwordx4 v[140:143], v19, s[52:55], s30 offen offset:0 sc1\n"
"v_mfma_f32_16x16x32_bf16 v[168:171], a[24:27], a[104:107], v[168:171]\n"
"s_waitcnt lgkmcnt(5)\n"
"ds_read_b64_tr_b16 a[18:19], v21 offset:20736\n"
"v_mfma_f32_16x16x32_bf16 v[168:171], a[28:31], a[108:111], v[168:171]\n"
"s_addk_i32 s93, 4096\n"
"ds_read_b64_tr_b16 a[20:21], v22 offset:16640\n"
"v_mfma_f32_16x16x32_bf16 v[176:179], a[0:3], a[88:91], v[176:179]\n"
"ds_read_b64_tr_b16 a[22:23], v22 offset:20736\n"
"v_mfma_f32_16x16x32_bf16 v[176:179], a[4:7], a[92:95], v[176:179]\n"
"ds_read_b64_tr_b16 a[24:25], v21 offset:16672\n"
"v_mfma_f32_16x16x32_bf16 v[180:183], a[4:7], a[100:103], v[180:183]\n"
"buffer_store_dwordx4 a[120:123], v19, s[52:55], s93 offen offset:0 sc1\n"
"v_mfma_f32_16x16x32_bf16 v[180:183], a[0:3], a[96:99], v[180:183]\n"
"s_addk_i32 s28, 4096\n"
"ds_read_b64_tr_b16 a[26:27], v21 offset:20768\n"
"v_mfma_f32_16x16x32_bf16 v[188:191], a[0:3], a[112:115], v[188:191]\n"
"ds_read_b64_tr_b16 a[28:29], v22 offset:16672\n"
"v_mfma_f32_16x16x32_bf16 v[184:187], a[0:3], a[104:107], v[184:187]\n"
"s_waitcnt lgkmcnt(7)\n"
"ds_read_b64_tr_b16 a[30:31], v22 offset:20768\n"
"v_mfma_f32_16x16x32_bf16 v[184:187], a[4:7], a[108:111], v[184:187]\n"
"buffer_load_dwordx4 v[120:123], v19, s[52:55], s28 offen offset:0 sc1\n"
"v_mfma_f32_16x16x32_bf16 v[192:195], a[8:11], a[88:91], v[192:195]\n"
"ds_read_b64_tr_b16 a[0:1], v21 offset:16704\n"
"v_mfma_f32_16x16x32_bf16 v[192:195], a[12:15], a[92:95], v[192:195]\n"
"s_addk_i32 s30, 4096\n"
"PG13_%=:\n"
"s_cbranch_execz PGX%=\n"
"ds_read_b64_tr_b16 a[2:3], v21 offset:20800\n"
"v_mfma_f32_16x16x32_bf16 v[196:199], a[12:15], a[100:103], v[196:199]\n"
"ds_read_b64_tr_b16 a[4:5], v22 offset:16704\n"
"v_mfma_f32_16x16x32_bf16 v[196:199], a[8:11], a[96:99], v[196:199]\n"
"ds_read_b64_tr_b16 a[6:7], v22 offset:20800\n"
"v_mfma_f32_16x16x32_bf16 v[204:207], a[8:11], a[112:115], v[204:207]\n"
"buffer_load_dwordx4 a[180:183], v19, s[52:55], s30 offen offset:0 sc1\n"
"v_mfma_f32_16x16x32_bf16 v[200:203], a[8:11], a[104:107], v[200:203]\n"
"s_waitcnt lgkmcnt(8)\n"
"v_mfma_f32_16x16x32_bf16 v[200:203], a[12:15], a[108:111], v[200:203]\n"
"s_addk_i32 s93, 4096\n"
"v_mfma_f32_16x16x32_bf16 v[208:211], a[16:19], a[88:91], v[208:211]\n"
"v_mfma_f32_16x16x32_bf16 v[208:211], a[20:23], a[92:95], v[208:211]\n"
"v_mfma_f32_16x16x32_bf16 v[212:215], a[20:23], a[100:103], v[212:215]\n"
"buffer_store_dwordx4 a[124:127], v19, s[52:55], s93 offen offset:0 sc1\n"
"v_mfma_f32_16x16x32_bf16 v[212:215], a[16:19], a[96:99], v[212:215]\n"
"s_addk_i32 s28, 4096\n"
"v_mfma_f32_16x16x32_bf16 v[220:223], a[16:19], a[112:115], v[220:223]\n"
"v_mfma_f32_16x16x32_bf16 v[216:219], a[16:19], a[104:107], v[216:219]\n"
"s_waitcnt lgkmcnt(4)\n"
"v_mfma_f32_16x16x32_bf16 v[216:219], a[20:23], a[108:111], v[216:219]\n"
"buffer_load_dwordx4 v[124:127], v19, s[52:55], s28 offen offset:0 sc1\n"
"v_mfma_f32_16x16x32_bf16 v[224:227], a[24:27], a[88:91], v[224:227]\n"
"v_mfma_f32_16x16x32_bf16 v[224:227], a[28:31], a[92:95], v[224:227]\n"
"s_waitcnt lgkmcnt(0)\n"
"s_addk_i32 s30, 4096\n"
"v_mfma_f32_16x16x32_bf16 v[228:231], a[28:31], a[100:103], v[228:231]\n"
"s_barrier\n"
"v_mfma_f32_16x16x32_bf16 v[228:231], a[24:27], a[96:99], v[228:231]\n"
"s_xor_b64 s[40:41], s[40:41], s[88:89]\n"
"ds_read_b64_tr_b16 a[8:9], v21 offset:24768\n"
"s_xor_b64 s[42:43], s[42:43], s[88:89]\n"
"v_mfma_f32_16x16x32_bf16 v[236:239], a[24:27], a[112:115], v[236:239]\n"
"s_xor_b64 s[44:45], s[44:45], s[88:89]\n"
"buffer_load_dwordx4 a[184:187], v19, s[52:55], s30 offen offset:0 sc1\n"
"v_mfma_f32_16x16x32_bf16 v[232:235], a[24:27], a[104:107], v[232:235]\n"
"ds_read_b64_tr_b16 a[10:11], v21 offset:28864\n"
"s_xor_b64 s[46:47], s[46:47], s[88:89]\n"
"v_mfma_f32_16x16x32_bf16 v[232:235], a[28:31], a[108:111], v[232:235]\n"
"s_addk_i32 s93, 4096\n"
"ds_read_b64_tr_b16 a[12:13], v22 offset:24768\n"
"s_or_b64 s[40:41], s[40:41], s[42:43]\n"
"v_mfma_f32_16x16x32_bf16 v[240:243], a[0:3], a[88:91], v[240:243]\n"
"s_or_b64 s[44:45], s[44:45], s[46:47]\n"
"ds_read_b64_tr_b16 a[14:15], v22 offset:28864\n"
"v_mfma_f32_16x16x32_bf16 v[240:243], a[4:7], a[92:95], v[240:243]\n"
"s_or_b64 s[40:41], s[40:41], s[44:45]\n"
"s_cbranch_scc1 POLL1%=\n"
"BACK1%=:\n"
"v_mfma_f32_16x16x32_bf16 v[244:247], a[4:7], a[100:103], v[244:247]\n"
"buffer_store_dwordx4 a[128:131], v19, s[52:55], s93 offen offset:0 sc1\n"
"v_mfma_f32_16x16x32_bf16 v[244:247], a[0:3], a[96:99], v[244:247]\n"
"s_addk_i32 s30, 4096\n"
"ds_read_b64_tr_b16 a[16:17], v21 offset:24800\n"
"v_mfma_f32_16x16x32_bf16 v[252:255], a[0:3], a[112:115], v[252:255]\n"
"ds_read_b64_tr_b16 a[18:19], v21 offset:28896\n"
"v_mfma_f32_16x16x32_bf16 v[248:251], a[0:3], a[104:107], v[248:251]\n"
"s_waitcnt lgkmcnt(2)\n"
"ds_read_b64_tr_b16 a[20:21], v22 offset:24800\n"
"v_mfma_f32_16x16x32_bf16 v[248:251], a[4:7], a[108:111], v[248:251]\n"
"ds_read_b64_tr_b16 a[22:23], v22 offset:28896\n"
"v_mfma_f32_16x16x32_bf16 v[144:147], a[8:11], a[116:119], v[144:147]\n"
"buffer_load_dwordx4 a[188:191], v19, s[52:55], s30 offen offset:0 sc1\n"
"v_mfma_f32_16x16x32_bf16 v[144:147], a[12:15], a[120:123], v[144:147]\n"
"s_add_u32 m0, s63, 0\n"
"ds_read_b64_tr_b16 a[24:25], v21 offset:24832\n"
"s_add_u32 s29, s69, 57344\n"
"v_mfma_f32_16x16x32_bf16 v[148:151], a[12:15], a[128:131], v[148:151]\n"
"ds_read_b64_tr_b16 a[26:27], v21 offset:28928\n"
"v_mfma_f32_16x16x32_bf16 v[148:151], a[8:11], a[124:127], v[148:151]\n"
"ds_read_b64_tr_b16 a[28:29], v22 offset:24832\n"
"v_mfma_f32_16x16x32_bf16 v[156:159], a[8:11], a[140:143], v[156:159]\n"
"buffer_load_dwordx4 v11, s[36:39], s29 offen offset:0 lds\n"
"v_mfma_f32_16x16x32_bf16 v[152:155], a[8:11], a[132:135], v[152:155]\n"
"s_waitcnt lgkmcnt(3)\n"
"ds_read_b64_tr_b16 a[30:31], v22 offset:28928\n"
"v_mfma_f32_16x16x32_bf16 v[152:155], a[12:15], a[136:139], v[152:155]\n"
"s_addk_i32 s93, 4096\n"
"ds_read_b64_tr_b16 a[0:1], v21 offset:24864\n"
"v_mfma_f32_16x16x32_bf16 v[160:163], a[16:19], a[116:119], v[160:163]\n"
"ds_read_b64_tr_b16 a[2:3], v21 offset:28960\n"
"v_mfma_f32_16x16x32_bf16 v[160:163], a[20:23], a[120:123], v[160:163]\n"
"ds_read_b64_tr_b16 a[4:5], v22 offset:24864\n"
"v_mfma_f32_16x16x32_bf16 v[164:167], a[20:23], a[128:131], v[164:167]\n"
"buffer_store_dwordx4 a[132:135], v19, s[52:55], s93 offen offset:0 sc1\n"
"v_mfma_f32_16x16x32_bf16 v[164:167], a[16:19], a[124:127], v[164:167]\n"
"ds_read_b64_tr_b16 a[6:7], v22 offset:28960\n"
"v_mfma_f32_16x16x32_bf16 v[172:175], a[16:19], a[140:143], v[172:175]\n"
"ds_read_b64_tr_b16 a[8:9], v21 offset:24896\n"
"v_mfma_f32_16x16x32_bf16 v[168:171], a[16:19], a[132:135], v[168:171]\n"
"s_waitcnt lgkmcnt(5)\n"
"ds_read_b64_tr_b16 a[10:11], v21 offset:28992\n"
"v_mfma_f32_16x16x32_bf16 v[168:171], a[20:23], a[136:139], v[168:171]\n"
"buffer_load_dwordx4 v12, s[36:39], s29 offen offset:1024 lds\n"
"v_mfma_f32_16x16x32_bf16 v[176:179], a[24:27], a[116:119], v[176:179]\n"
"ds_read_b64_tr_b16 a[12:13], v22 offset:24896\n"
"v_mfma_f32_16x16x32_bf16 v[176:179], a[28:31], a[120:123], v[176:179]\n"
"ds_read_b64_tr_b16 a[14:15], v22 offset:28992\n"
"v_mfma_f32_16x16x32_bf16 v[180:183], a[28:31], a[128:131], v[180:183]\n"
"ds_read_b64_tr_b16 a[16:17], v21 offset:24928\n"
"v_mfma_f32_16x16x32_bf16 v[180:183], a[24:27], a[124:127], v[180:183]\n"
"ds_read_b64_tr_b16 a[18:19], v21 offset:29024\n"
"v_mfma_f32_16x16x32_bf16 v[188:191], a[24:27], a[140:143], v[188:191]\n"
"buffer_load_dwordx4 v13, s[36:39], s29 offen offset:2048 lds\n"
"v_mfma_f32_16x16x32_bf16 v[184:187], a[24:27], a[132:135], v[184:187]\n"
"s_waitcnt lgkmcnt(4)\n"
"ds_read_b64_tr_b16 a[20:21], v22 offset:24928\n"
"v_mfma_f32_16x16x32_bf16 v[184:187], a[28:31], a[136:139], v[184:187]\n"
"s_addk_i32 s93, 4096\n"
"ds_read_b64_tr_b16 a[22:23], v22 offset:29024\n"
"v_mfma_f32_16x16x32_bf16 v[192:195], a[0:3], a[116:119], v[192:195]\n"
"ds_read_b64_tr_b16 a[24:25], v21 offset:24960\n"
"v_mfma_f32_16x16x32_bf16 v[192:195], a[4:7], a[120:123], v[192:195]\n"
"ds_read_b64_tr_b16 a[26:27], v21 offset:29056\n"
"v_mfma_f32_16x16x32_bf16 v[196:199], a[4:7], a[128:131], v[196:199]\n"
"buffer_store_dwordx4 a[136:139], v19, s[52:55], s93 offen offset:0 sc1\n"
"v_mfma_f32_16x16x32_bf16 v[196:199], a[0:3], a[124:127], v[196:199]\n"
"ds_read_b64_tr_b16 a[28:29], v22 offset:24960\n"
"v_mfma_f32_16x16x32_bf16 v[204:207], a[0:3], a[140:143], v[204:207]\n"
"ds_read_b64_tr_b16 a[30:31], v22 offset:29056\n"
"v_mfma_f32_16x16x32_bf16 v[200:203], a[0:3], a[132:135], v[200:203]\n"
"s_waitcnt lgkmcnt(5)\n"
"v_mfma_f32_16x16x32_bf16 v[200:203], a[4:7], a[136:139], v[200:203]\n"
"buffer_load_dwordx4 v14, s[36:39], s29 offen offset:3072 lds\n"
"v_mfma_f32_16x16x32_bf16 v[208:211], a[8:11], a[116:119], v[208:211]\n"
"v_mfma_f32_16x16x32_bf16 v[208:211], a[12:15], a[120:123], v[208:211]\n"
"s_addk_i32 m0, 8256\n"
"s_add_u32 s29, s67, 114688\n"
"v_mfma_f32_16x16x32_bf16 v[212:215], a[12:15], a[128:131], v[212:215]\n"
"v_mfma_f32_16x16x32_bf16 v[212:215], a[8:11], a[124:127], v[212:215]\n"
"v_mfma_f32_16x16x32_bf16 v[220:223], a[8:11], a[140:143], v[220:223]\n"
"buffer_load_dwordx4 v11, s[36:39], s29 offen offset:0 lds\n"
"v_mfma_f32_16x16x32_bf16 v[216:219], a[8:11], a[132:135], v[216:219]\n"
"s_waitcnt lgkmcnt(0)\n"
"v_mfma_f32_16x16x32_bf16 v[216:219], a[12:15], a[136:139], v[216:219]\n"
"s_addk_i32 s93, 4096\n"
"v_mfma_f32_16x16x32_bf16 v[224:227], a[16:19], a[116:119], v[224:227]\n"
"v_mfma_f32_16x16x32_bf16 v[224:227], a[20:23], a[120:123], v[224:227]\n"
"v_mfma_f32_16x16x32_bf16 v[228:231], a[20:23], a[128:131], v[228:231]\n"
"s_barrier\n"
"v_mfma_f32_16x16x32_bf16 v[228:231], a[16:19], a[124:127], v[228:231]\n"
"buffer_store_dwordx4 a[140:143], v19, s[52:55], s93 offen offset:0 sc1\n"
"v_mfma_f32_16x16x32_bf16 v[236:239], a[16:19], a[140:143], v[236:239]\n"
"s_add_u32 s93, s70, 0\n"
"ds_read_b64_tr_b16 a[0:1], v21 offset:41280\n"
"v_mfma_f32_16x16x32_bf16 v[232:235], a[16:19], a[132:135], v[232:235]\n"
"ds_read_b64_tr_b16 a[2:3], v21 offset:45376\n"
"v_mfma_f32_16x16x32_bf16 v[232:235], a[20:23], a[136:139], v[232:235]\n"
"ds_read_b64_tr_b16 a[4:5], v22 offset:41280\n"
"v_mfma_f32_16x16x32_bf16 v[240:243], a[24:27], a[116:119], v[240:243]\n"
"ds_read_b64_tr_b16 a[6:7], v22 offset:45376\n"
"v_mfma_f32_16x16x32_bf16 v[240:243], a[28:31], a[120:123], v[240:243]\n"
"buffer_load_dwordx4 v12, s[36:39], s29 offen offset:1024 lds\n"
"v_mfma_f32_16x16x32_bf16 v[244:247], a[28:31], a[128:131], v[244:247]\n"
"ds_read_b64_tr_b16 a[8:9], v21 offset:41312\n"
"v_mfma_f32_16x16x32_bf16 v[244:247], a[24:27], a[124:127], v[244:247]\n"
"ds_read_b64_tr_b16 a[10:11], v21 offset:45408\n"
"v_mfma_f32_16x16x32_bf16 v[252:255], a[24:27], a[140:143], v[252:255]\n"
"buffer_load_dwordx4 v13, s[36:39], s29 offen offset:2048 lds\n"
"v_mfma_f32_16x16x32_bf16 v[248:251], a[24:27], a[132:135], v[248:251]\n"
"s_waitcnt vmcnt(23)\n"
"ds_read_b64_tr_b16 a[12:13], v22 offset:41312\n"
"s_waitcnt lgkmcnt(3)\n"
"v_mfma_f32_16x16x32_bf16 v[248:251], a[28:31], a[136:139], v[248:251]\n"
"s_addk_i32 s90, 4096\n"
"ds_read_b64_tr_b16 a[14:15], v22 offset:45408\n"
"v_mfma_f32_16x16x32_bf16 v[144:147], a[0:3], a[144:147], v[144:147]\n"
"ds_read_b64_tr_b16 a[16:17], v21 offset:41344\n"
"v_mfma_f32_16x16x32_bf16 v[144:147], a[4:7], a[148:151], v[144:147]\n"
"ds_read_b64_tr_b16 a[18:19], v21 offset:45440\n"
"v_mfma_f32_16x16x32_bf16 v[148:151], a[4:7], a[156:159], v[148:151]\n"
"buffer_load_dwordx4 a[192:195], v19, s[52:55], s90 offen offset:0 sc1\n"
"v_mfma_f32_16x16x32_bf16 v[148:151], a[0:3], a[152:155], v[148:151]\n"
"ds_read_b64_tr_b16 a[20:21], v22 offset:41344\n"
"v_mfma_f32_16x16x32_bf16 v[156:159], a[0:3], a[168:171], v[156:159]\n"
"ds_read_b64_tr_b16 a[22:23], v22 offset:45440\n"
"v_mfma_f32_16x16x32_bf16 v[152:155], a[0:3], a[160:163], v[152:155]\n"
"s_waitcnt lgkmcnt(4)\n"
"ds_read_b64_tr_b16 a[24:25], v21 offset:41376\n"
"v_mfma_f32_16x16x32_bf16 v[152:155], a[4:7], a[164:167], v[152:155]\n"
"buffer_load_dwordx4 v14, s[36:39], s29 offen offset:3072 lds\n"
"v_mfma_f32_16x16x32_bf16 v[160:163], a[8:11], a[144:147], v[160:163]\n"
"ds_read_b64_tr_b16 a[26:27], v21 offset:45472\n"
"v_mfma_f32_16x16x32_bf16 v[160:163], a[12:15], a[148:151], v[160:163]\n"
"s_addk_i32 m0, 8256\n"
"ds_read_b64_tr_b16 a[28:29], v22 offset:41376\n"
"s_add_u32 s29, s68, 114688\n"
"v_mfma_f32_16x16x32_bf16 v[164:167], a[12:15], a[156:159], v[164:167]\n"
"ds_read_b64_tr_b16 a[30:31], v22 offset:45472\n"
"v_mfma_f32_16x16x32_bf16 v[164:167], a[8:11], a[152:155], v[164:167]\n"
"ds_read_b64_tr_b16 a[0:1], v21 offset:41408\n"
"v_mfma_f32_16x16x32_bf16 v[172:175], a[8:11], a[168:171], v[172:175]\n"
"buffer_load_dwordx4 v11, s[36:39], s29 offen offset:0 lds\n"
"v_mfma_f32_16x16x32_bf16 v[168:171], a[8:11], a[160:163], v[168:171]\n"
"s_waitcnt lgkmcnt(4)\n"
"ds_read_b64_tr_b16 a[2:3], v21 offset:45504\n"
"v_mfma_f32_16x16x32_bf16 v[168:171], a[12:15], a[164:167], v[168:171]\n"
"s_addk_i32 s90, 4096\n"
"ds_read_b64_tr_b16 a[4:5], v22 offset:41408\n"
"v_mfma_f32_16x16x32_bf16 v[176:179], a[16:19], a[144:147], v[176:179]\n"
"ds_read_b64_tr_b16 a[6:7], v22 offset:45504\n"
"v_mfma_f32_16x16x32_bf16 v[176:179], a[20:23], a[148:151], v[176:179]\n"
"ds_read_b64_tr_b16 a[8:9], v21 offset:41440\n"
"v_mfma_f32_16x16x32_bf16 v[180:183], a[20:23], a[156:159], v[180:183]\n"
"buffer_load_dwordx4 a[196:199], v19, s[52:55], s90 offen offset:0 sc1\n"
"v_mfma_f32_16x16x32_bf16 v[180:183], a[16:19], a[152:155], v[180:183]\n"
"ds_read_b64_tr_b16 a[10:11], v21 offset:45536\n"
"v_mfma_f32_16x16x32_bf16 v[188:191], a[16:19], a[168:171], v[188:191]\n"
"ds_read_b64_tr_b16 a[12:13], v22 offset:41440\n"
"v_mfma_f32_16x16x32_bf16 v[184:187], a[16:19], a[160:163], v[184:187]\n"
"s_waitcnt lgkmcnt(5)\n"
"ds_read_b64_tr_b16 a[14:15], v22 offset:45536\n"
"v_mfma_f32_16x16x32_bf16 v[184:187], a[20:23], a[164:167], v[184:187]\n"
"buffer_load_dwordx4 v12, s[36:39], s29 offen offset:1024 lds\n"
"v_mfma_f32_16x16x32_bf16 v[192:195], a[24:27], a[144:147], v[192:195]\n"
"ds_read_b64_tr_b16 a[16:17], v21 offset:41472\n"
"v_mfma_f32_16x16x32_bf16 v[192:195], a[28:31], a[148:151], v[192:195]\n"
"ds_read_b64_tr_b16 a[18:19], v21 offset:45568\n"
"v_mfma_f32_16x16x32_bf16 v[196:199], a[28:31], a[156:159], v[196:199]\n"
"ds_read_b64_tr_b16 a[20:21], v22 offset:41472\n"
"v_mfma_f32_16x16x32_bf16 v[196:199], a[24:27], a[152:155], v[196:199]\n"
"ds_read_b64_tr_b16 a[22:23], v22 offset:45568\n"
"v_mfma_f32_16x16x32_bf16 v[204:207], a[24:27], a[168:171], v[204:207]\n"
"buffer_load_dwordx4 v13, s[36:39], s29 offen offset:2048 lds\n"
"v_mfma_f32_16x16x32_bf16 v[200:203], a[24:27], a[160:163], v[200:203]\n"
"s_waitcnt lgkmcnt(4)\n"
"v_mfma_f32_16x16x32_bf16 v[200:203], a[28:31], a[164:167], v[200:203]\n"
"s_addk_i32 s90, 4096\n"
"v_mfma_f32_16x16x32_bf16 v[208:211], a[0:3], a[144:147], v[208:211]\n"
"v_mfma_f32_16x16x32_bf16 v[208:211], a[4:7], a[148:151], v[208:211]\n"
"v_mfma_f32_16x16x32_bf16 v[212:215], a[4:7], a[156:159], v[212:215]\n"
"buffer_load_dwordx4 a[200:203], v19, s[52:55], s90 offen offset:0 sc1\n"
"v_mfma_f32_16x16x32_bf16 v[212:215], a[0:3], a[152:155], v[212:215]\n"
"v_mfma_f32_16x16x32_bf16 v[220:223], a[0:3], a[168:171], v[220:223]\n"
"v_mfma_f32_16x16x32_bf16 v[216:219], a[0:3], a[160:163], v[216:219]\n"
"v_mfma_f32_16x16x32_bf16 v[216:219], a[4:7], a[164:167], v[216:219]\n"
"buffer_load_dwordx4 v14, s[36:39], s29 offen offset:3072 lds\n"
"v_mfma_f32_16x16x32_bf16 v[224:227], a[8:11], a[144:147], v[224:227]\n"
"v_mfma_f32_16x16x32_bf16 v[224:227], a[12:15], a[148:151], v[224:227]\n"
"s_waitcnt lgkmcnt(0)\n"
"s_addk_i32 m0, 8256\n"
"s_add_u32 s29, s69, 114688\n"
"v_mfma_f32_16x16x32_bf16 v[228:231], a[12:15], a[156:159], v[228:231]\n"
"s_barrier\n"
"v_mfma_f32_16x16x32_bf16 v[228:231], a[8:11], a[152:155], v[228:231]\n"
"ds_read_b64_tr_b16 a[24:25], v21 offset:49536\n"
"v_mfma_f32_16x16x32_bf16 v[236:239], a[8:11], a[168:171], v[236:239]\n"
"buffer_load_dwordx4 v11, s[36:39], s29 offen offset:0 lds\n"
"v_mfma_f32_16x16x32_bf16 v[232:235], a[8:11], a[160:163], v[232:235]\n"
"ds_read_b64_tr_b16 a[26:27], v21 offset:53632\n"
"v_mfma_f32_16x16x32_bf16 v[232:235], a[12:15], a[164:167], v[232:235]\n"
"s_addk_i32 s90, 4096\n"
"ds_read_b64_tr_b16 a[28:29], v22 offset:49536\n"
"v_mfma_f32_16x16x32_bf16 v[240:243], a[16:19], a[144:147], v[240:243]\n"
"ds_read_b64_tr_b16 a[30:31], v22 offset:53632\n"
"v_mfma_f32_16x16x32_bf16 v[240:243], a[20:23], a[148:151], v[240:243]\n"
"ds_read_b64_tr_b16 a[0:1], v21 offset:49568\n"
"v_mfma_f32_16x16x32_bf16 v[244:247], a[20:23], a[156:159], v[244:247]\n"
"buffer_load_dwordx4 a[204:207], v19, s[52:55], s90 offen offset:0 sc1\n"
"v_mfma_f32_16x16x32_bf16 v[244:247], a[16:19], a[152:155], v[244:247]\n"
"s_buffer_load_dwordx8 s[40:47], s[52:55], s95 offset:256 glc\n"
"ds_read_b64_tr_b16 a[2:3], v21 offset:53664\n"
"s_addk_i32 s28, 4096\n"
"v_mfma_f32_16x16x32_bf16 v[252:255], a[16:19], a[168:171], v[252:255]\n"
"ds_read_b64_tr_b16 a[4:5], v22 offset:49568\n"
"v_mfma_f32_16x16x32_bf16 v[248:251], a[16:19], a[160:163], v[248:251]\n"
"s_waitcnt vmcnt(23)\n"
"ds_read_b64_tr_b16 a[6:7], v22 offset:53664\n"
"s_waitcnt lgkmcnt(4)\n"
"v_mfma_f32_16x16x32_bf16 v[248:251], a[20:23], a[164:167], v[248:251]\n"
"buffer_load_dwordx4 a[220:223], v19, s[52:55], s28 offen offset:0 sc1\n"
"v_mfma_f32_16x16x32_bf16 v[144:147], a[24:27], a[172:175], v[144:147]\n"
"ds_read_b64_tr_b16 a[8:9], v21 offset:49600\n"
"v_mfma_f32_16x16x32_bf16 v[144:147], a[28:31], a[176:179], v[144:147]\n"
"s_addk_i32 s30, 4096\n"
"ds_read_b64_tr_b16 a[10:11], v21 offset:53696\n"
"v_mfma_f32_16x16x32_bf16 v[148:151], a[28:31], v[112:115], v[148:151]\n"
"ds_read_b64_tr_b16 a[12:13], v22 offset:49600\n"
"v_mfma_f32_16x16x32_bf16 v[148:151], a[24:27], v[108:111], v[148:151]\n"
"ds_read_b64_tr_b16 a[14:15], v22 offset:53696\n"
"v_mfma_f32_16x16x32_bf16 v[156:159], a[24:27], v[124:127], v[156:159]\n"
"buffer_load_dwordx4 a[248:251], v19, s[52:55], s30 offen offset:0 sc1\n"
"v_mfma_f32_16x16x32_bf16 v[152:155], a[24:27], v[116:119], v[152:155]\n"
"s_waitcnt lgkmcnt(4)\n"
"ds_read_b64_tr_b16 a[16:17], v21 offset:49632\n"
"v_mfma_f32_16x16x32_bf16 v[152:155], a[28:31], v[120:123], v[152:155]\n"
"s_addk_i32 s90, 4096\n"
"ds_read_b64_tr_b16 a[18:19], v21 offset:53728\n"
"v_mfma_f32_16x16x32_bf16 v[160:163], a[0:3], a[172:175], v[160:163]\n"
"ds_read_b64_tr_b16 a[20:21], v22 offset:49632\n"
"v_mfma_f32_16x16x32_bf16 v[160:163], a[4:7], a[176:179], v[160:163]\n"
"ds_read_b64_tr_b16 a[22:23], v22 offset:53728\n"
"v_mfma_f32_16x16x32_bf16 v[164:167], a[4:7], v[112:115], v[164:167]\n"
"buffer_load_dwordx4 a[208:211], v19, s[52:55], s90 offen offset:0 sc1\n"
"v_mfma_f32_16x16x32_bf16 v[164:167], a[0:3], v[108:111], v[164:167]\n"
"s_addk_i32 s28, 4096\n"
"ds_read_b64_tr_b16 a[24:25], v21 offset:49664\n"
"v_mfma_f32_16x16x32_bf16 v[172:175], a[0:3], v[124:127], v[172:175]\n"
"ds_read_b64_tr_b16 a[26:27], v21 offset:53760\n"
"v_mfma_f32_16x16x32_bf16 v[168:171], a[0:3], v[116:119], v[168:171]\n"
"s_waitcnt lgkmcnt(6)\n"
"ds_read_b64_tr_b16 a[28:29], v22 offset:49664\n"
"v_mfma_f32_16x16x32_bf16 v[168:171], a[4:7], v[120:123], v[168:171]\n"
"buffer_load_dwordx4 a[224:227], v19, s[52:55], s28 offen offset:0 sc1\n"
"v_mfma_f32_16x16x32_bf16 v[176:179], a[8:11], a[172:175], v[176:179]\n"
"ds_read_b64_tr_b16 a[30:31], v22 offset:53760\n"
"v_mfma_f32_16x16x32_bf16 v[176:179], a[12:15], a[176:179], v[176:179]\n"
"s_addk_i32 s30, 4096\n"
"ds_read_b64_tr_b16 a[0:1], v21 offset:49696\n"
"v_mfma_f32_16x16x32_bf16 v[180:183], a[12:15], v[112:115], v[180:183]\n"
"ds_read_b64_tr_b16 a[2:3], v21 offset:53792\n"
"v_mfma_f32_16x16x32_bf16 v[180:183], a[8:11], v[108:111], v[180:183]\n"
"ds_read_b64_tr_b16 a[4:5], v22 offset:49696\n"
"v_mfma_f32_16x16x32_bf16 v[188:191], a[8:11], v[124:127], v[188:191]\n"
"buffer_load_dwordx4 a[252:255], v19, s[52:55], s30 offen offset:0 sc1\n"
"v_mfma_f32_16x16x32_bf16 v[184:187], a[8:11], v[116:119], v[184:187]\n"
"s_waitcnt lgkmcnt(7)\n"
"ds_read_b64_tr_b16 a[6:7], v22 offset:53792\n"
"v_mfma_f32_16x16x32_bf16 v[184:187], a[12:15], v[120:123], v[184:187]\n"
"s_addk_i32 s90, 4096\n"
"ds_read_b64_tr_b16 a[8:9], v21 offset:49728\n"
"v_mfma_f32_16x16x32_bf16 v[192:195], a[16:19], a[172:175], v[192:195]\n"
"ds_read_b64_tr_b16 a[10:11], v21 offset:53824\n"
"v_mfma_f32_16x16x32_bf16 v[192:195], a[20:23], a[176:179], v[192:195]\n"
"ds_read_b64_tr_b16 a[12:13], v22 offset:49728\n"
"v_mfma_f32_16x16x32_bf16 v[196:199], a[20:23], v[112:115], v[196:199]\n"
"buffer_load_dwordx4 a[212:215], v19, s[52:55], s90 offen offset:0 sc1\n"
"v_mfma_f32_16x16x32_bf16 v[196:199], a[16:19], v[108:111], v[196:199]\n"
"s_addk_i32 s28, 4096\n"
"ds_read_b64_tr_b16 a[14:15], v22 offset:53824\n"
"v_mfma_f32_16x16x32_bf16 v[204:207], a[16:19], v[124:127], v[204:207]\n"
"v_mfma_f32_16x16x32_bf16 v[200:203], a[16:19], v[116:119], v[200:203]\n"
"s_waitcnt lgkmcnt(8)\n"
"v_mfma_f32_16x16x32_bf16 v[200:203], a[20:23], v[120:123], v[200:203]\n"
"buffer_load_dwordx4 a[228:231], v19, s[52:55], s28 offen offset:0 sc1\n"
"v_mfma_f32_16x16x32_bf16 v[208:211], a[24:27], a[172:175], v[208:211]\n"
"v_mfma_f32_16x16x32_bf16 v[208:211], a[28:31], a[176:179], v[208:211]\n"
"s_addk_i32 s30, 4096\n"
"v_mfma_f32_16x16x32_bf16 v[212:215], a[28:31], v[112:115], v[212:215]\n"
"v_mfma_f32_16x16x32_bf16 v[212:215], a[24:27], v[108:111], v[212:215]\n"
"v_mfma_f32_16x16x32_bf16 v[220:223], a[24:27], v[124:127], v[220:223]\n"
"buffer_load_dwordx4 a[32:35], v19, s[52:55], s30 offen offset:0 sc1\n"
"v_mfma_f32_16x16x32_bf16 v[216:219], a[24:27], v[116:119], v[216:219]\n"
"s_waitcnt lgkmcnt(4)\n"
"v_mfma_f32_16x16x32_bf16 v[216:219], a[28:31], v[120:123], v[216:219]\n"
"s_addk_i32 s90, 4096\n"
"v_mfma_f32_16x16x32_bf16 v[224:227], a[0:3], a[172:175], v[224:227]\n"
"v_mfma_f32_16x16x32_bf16 v[224:227], a[4:7], a[176:179], v[224:227]\n"
"s_waitcnt vmcnt(20) lgkmcnt(0)\n"
"v_mfma_f32_16x16x32_bf16 v[228:231], a[4:7], v[112:115], v[228:231]\n"
"s_barrier\n"
"v_mfma_f32_16x16x32_bf16 v[228:231], a[0:3], v[108:111], v[228:231]\n"
"s_xor_b64 s[40:41], s[40:41], s[88:89]\n"
"buffer_load_dwordx4 a[216:219], v19, s[52:55], s90 offen offset:0 sc1\n"
"v_mfma_f32_16x16x32_bf16 v[236:239], a[0:3], v[124:127], v[236:239]\n"
"s_xor_b64 s[42:43], s[42:43], s[88:89]\n"
"ds_read_b64_tr_b16 a[16:17], v21 offset:57792\n"
"s_xor_b64 s[44:45], s[44:45], s[88:89]\n"
"v_mfma_f32_16x16x32_bf16 v[232:235], a[0:3], v[116:119], v[232:235]\n"
"ds_read_b64_tr_b16 a[18:19], v21 offset:61888\n"
"s_addk_i32 s28, 4096\n"
"v_mfma_f32_16x16x32_bf16 v[232:235], a[4:7], v[120:123], v[232:235]\n"
"s_xor_b64 s[46:47], s[46:47], s[88:89]\n"
"ds_read_b64_tr_b16 a[20:21], v22 offset:57792\n"
"s_or_b64 s[40:41], s[40:41], s[42:43]\n"
"v_mfma_f32_16x16x32_bf16 v[240:243], a[8:11], a[172:175], v[240:243]\n"
"s_or_b64 s[44:45], s[44:45], s[46:47]\n"
"ds_read_b64_tr_b16 a[22:23], v22 offset:61888\n"
"v_mfma_f32_16x16x32_bf16 v[240:243], a[12:15], a[176:179], v[240:243]\n"
"buffer_store_dwordx2 v[30:31], v29, s[52:55], 0 offen offset:384 sc1\n"
"v_mfma_f32_16x16x32_bf16 v[244:247], a[12:15], v[112:115], v[244:247]\n"
"buffer_load_dwordx4 a[232:235], v19, s[52:55], s28 offen offset:0 sc1\n"
"v_mfma_f32_16x16x32_bf16 v[244:247], a[8:11], v[108:111], v[244:247]\n"
"s_or_b64 s[40:41], s[40:41], s[44:45]\n"
"s_cbranch_scc1 POLL2%=\n"
"BACK2%=:\n"
"v_lshlrev_b32 v29, 22, v1\n"
"v_mfma_f32_16x16x32_bf16 v[252:255], a[8:11], v[124:127], v[252:255]\n"
"ds_read_b64_tr_b16 a[24:25], v21 offset:57824\n"
"v_mfma_f32_16x16x32_bf16 v[248:251], a[8:11], v[116:119], v[248:251]\n"
"ds_read_b64_tr_b16 a[26:27], v21 offset:61920\n"
"s_waitcnt lgkmcnt(2)\n"
"v_mfma_f32_16x16x32_bf16 v[248:251], a[12:15], v[120:123], v[248:251]\n"
"s_addk_i32 s90, 4096\n"
"ds_read_b64_tr_b16 a[28:29], v22 offset:57824\n"
"v_mfma_f32_16x16x32_bf16 v[144:147], a[16:19], v[128:131], v[144:147]\n"
"ds_read_b64_tr_b16 a[30:31], v22 offset:61920\n"
"v_mfma_f32_16x16x32_bf16 v[144:147], a[20:23], v[132:135], v[144:147]\n"
"ds_read_b64_tr_b16 a[0:1], v21 offset:57856\n"
"v_mfma_f32_16x16x32_bf16 v[148:151], a[20:23], v[140:143], v[148:151]\n"
"buffer_load_dwordx4 v[32:35], v19, s[52:55], s90 offen offset:0 sc1\n"
"v_mfma_f32_16x16x32_bf16 v[148:151], a[16:19], v[136:139], v[148:151]\n"
"s_addk_i32 s28, 4096\n"
"ds_read_b64_tr_b16 a[2:3], v21 offset:61952\n"
"v_mfma_f32_16x16x32_bf16 v[156:159], a[16:19], a[188:191], v[156:159]\n"
"ds_read_b64_tr_b16 a[4:5], v22 offset:57856\n"
"v_mfma_f32_16x16x32_bf16 v[152:155], a[16:19], a[180:183], v[152:155]\n"
"s_waitcnt lgkmcnt(3)\n"
"ds_read_b64_tr_b16 a[6:7], v22 offset:61952\n"
"v_mfma_f32_16x16x32_bf16 v[152:155], a[20:23], a[184:187], v[152:155]\n"
"buffer_load_dwordx4 a[236:239], v19, s[52:55], s28 offen offset:0 sc1\n"
"v_mfma_f32_16x16x32_bf16 v[160:163], a[24:27], v[128:131], v[160:163]\n"
"ds_read_b64_tr_b16 a[8:9], v21 offset:57888\n"
"v_mfma_f32_16x16x32_bf16 v[160:163], a[28:31], v[132:135], v[160:163]\n"
"s_addk_i32 s30, 4096\n"
"ds_read_b64_tr_b16 a[10:11], v21 offset:61984\n"
"v_mfma_f32_16x16x32_bf16 v[164:167], a[28:31], v[140:143], v[164:167]\n"
"ds_read_b64_tr_b16 a[12:13], v22 offset:57888\n"
"v_mfma_f32_16x16x32_bf16 v[164:167], a[24:27], v[136:139], v[164:167]\n"
"ds_read_b64_tr_b16 a[14:15], v22 offset:61984\n"
"v_mfma_f32_16x16x32_bf16 v[172:175], a[24:27], a[188:191], v[172:175]\n"
"buffer_load_dwordx4 a[36:39], v19, s[52:55], s30 offen offset:0 sc1\n"
"v_mfma_f32_16x16x32_bf16 v[168:171], a[24:27], a[180:183], v[168:171]\n"
"s_waitcnt lgkmcnt(4)\n"
"ds_read_b64_tr_b16 a[16:17], v21 offset:57920\n"
"v_mfma_f32_16x16x32_bf16 v[168:171], a[28:31], a[184:187], v[168:171]\n"
"s_addk_i32 s90, 4096\n"
"ds_read_b64_tr_b16 a[18:19], v21 offset:62016\n"
"v_mfma_f32_16x16x32_bf16 v[176:179], a[0:3], v[128:131], v[176:179]\n"
"ds_read_b64_tr_b16 a[20:21], v22 offset:57920\n"
"v_mfma_f32_16x16x32_bf16 v[176:179], a[4:7], v[132:135], v[176:179]\n"
"ds_read_b64_tr_b16 a[22:23], v22 offset:62016\n"
"v_mfma_f32_16x16x32_bf16 v[180:183], a[4:7], v[140:143], v[180:183]\n"
"buffer_load_dwordx4 v[36:39], v19, s[52:55], s90 offen offset:0 sc1\n"
"v_mfma_f32_16x16x32_bf16 v[180:183], a[0:3], v[136:139], v[180:183]\n"
"s_addk_i32 s28, 4096\n"
"ds_read_b64_tr_b16 a[24:25], v21 offset:57952\n"
"v_mfma_f32_16x16x32_bf16 v[188:191], a[0:3], a[188:191], v[188:191]\n"
"ds_read_b64_tr_b16 a[26:27], v21 offset:62048\n"
"v_mfma_f32_16x16x32_bf16 v[184:187], a[0:3], a[180:183], v[184:187]\n"
"s_waitcnt lgkmcnt(5)\n"
"ds_read_b64_tr_b16 a[28:29], v22 offset:57952\n"
"v_mfma_f32_16x16x32_bf16 v[184:187], a[4:7], a[184:187], v[184:187]\n"
"buffer_load_dwordx4 a[240:243], v19, s[52:55], s28 offen offset:0 sc1\n"
"v_mfma_f32_16x16x32_bf16 v[192:195], a[8:11], v[128:131], v[192:195]\n"
"ds_read_b64_tr_b16 a[30:31], v22 offset:62048\n"
"v_mfma_f32_16x16x32_bf16 v[192:195], a[12:15], v[132:135], v[192:195]\n"
"s_addk_i32 s30, 4096\n"
"ds_read_b64_tr_b16 a[0:1], v21 offset:57984\n"
"v_mfma_f32_16x16x32_bf16 v[196:199], a[12:15], v[140:143], v[196:199]\n"
"ds_read_b64_tr_b16 a[2:3], v21 offset:62080\n"
"v_add_u32 v21, s27, v21\n"
"v_mfma_f32_16x16x32_bf16 v[196:199], a[8:11], v[136:139], v[196:199]\n"
"ds_read_b64_tr_b16 a[4:5], v22 offset:57984\n"
"v_mfma_f32_16x16x32_bf16 v[204:207], a[8:11], a[188:191], v[204:207]\n"
"buffer_load_dwordx4 a[40:43], v19, s[52:55], s30 offen offset:0 sc1\n"
"v_mfma_f32_16x16x32_bf16 v[200:203], a[8:11], a[180:183], v[200:203]\n"
"s_waitcnt lgkmcnt(4)\n"
"ds_read_b64_tr_b16 a[6:7], v22 offset:62080\n"
"v_add_u32 v22, s27, v22\n"
"v_mfma_f32_16x16x32_bf16 v[200:203], a[12:15], a[184:187], v[200:203]\n"
"s_addk_i32 s90, 4096\n"
"v_mfma_f32_16x16x32_bf16 v[208:211], a[16:19], v[128:131], v[208:211]\n"
"v_mfma_f32_16x16x32_bf16 v[208:211], a[20:23], v[132:135], v[208:211]\n"
"v_mfma_f32_16x16x32_bf16 v[212:215], a[20:23], v[140:143], v[212:215]\n"
"buffer_load_dwordx4 v[40:43], v19, s[52:55], s90 offen offset:0 sc1\n"
"v_mfma_f32_16x16x32_bf16 v[212:215], a[16:19], v[136:139], v[212:215]\n"
"s_addk_i32 s28, 4096\n"
"v_mfma_f32_16x16x32_bf16 v[220:223], a[16:19], a[188:191], v[220:223]\n"
"v_mfma_f32_16x16x32_bf16 v[216:219], a[16:19], a[180:183], v[216:219]\n"
"s_waitcnt lgkmcnt(0)\n"
"v_mfma_f32_16x16x32_bf16 v[216:219], a[20:23], a[184:187], v[216:219]\n"
"buffer_load_dwordx4 a[244:247], v19, s[52:55], s28 offen offset:0 sc1\n"
"v_mfma_f32_16x16x32_bf16 v[224:227], a[24:27], v[128:131], v[224:227]\n"
"v_mfma_f32_16x16x32_bf16 v[224:227], a[28:31], v[132:135], v[224:227]\n"
"s_addk_i32 s30, 4096\n"
"v_mfma_f32_16x16x32_bf16 v[228:231], a[28:31], v[140:143], v[228:231]\n"
"s_barrier\n"
"v_mfma_f32_16x16x32_bf16 v[228:231], a[24:27], v[136:139], v[228:231]\n"
"ds_read_b64_tr_b16 a[8:9], v21 offset:57792\n"
"v_mfma_f32_16x16x32_bf16 v[236:239], a[24:27], a[188:191], v[236:239]\n"
"buffer_load_dwordx4 a[44:47], v19, s[52:55], s30 offen offset:0 sc1\n"
"v_mfma_f32_16x16x32_bf16 v[232:235], a[24:27], a[180:183], v[232:235]\n"
"ds_read_b64_tr_b16 a[10:11], v21 offset:61888\n"
"v_mfma_f32_16x16x32_bf16 v[232:235], a[28:31], a[184:187], v[232:235]\n"
"s_addk_i32 s90, 4096\n"
"ds_read_b64_tr_b16 a[12:13], v22 offset:57792\n"
"v_mfma_f32_16x16x32_bf16 v[240:243], a[0:3], v[128:131], v[240:243]\n"
"ds_read_b64_tr_b16 a[14:15], v22 offset:61888\n"
"v_mfma_f32_16x16x32_bf16 v[240:243], a[4:7], v[132:135], v[240:243]\n"
"ds_read_b64_tr_b16 a[16:17], v21 offset:57824\n"
"v_mfma_f32_16x16x32_bf16 v[244:247], a[4:7], v[140:143], v[244:247]\n"
"buffer_load_dwordx4 v[44:47], v19, s[52:55], s90 offen offset:0 sc1\n"
"v_mfma_f32_16x16x32_bf16 v[244:247], a[0:3], v[136:139], v[244:247]\n"
"s_addk_i32 s30, 4096\n"
"ds_read_b64_tr_b16 a[18:19], v21 offset:61920\n"
"v_mfma_f32_16x16x32_bf16 v[252:255], a[0:3], a[188:191], v[252:255]\n"
"ds_read_b64_tr_b16 a[20:21], v22 offset:57824\n"
"v_mfma_f32_16x16x32_bf16 v[248:251], a[0:3], a[180:183], v[248:251]\n"
"s_waitcnt vmcnt(12)\n"
"ds_read_b64_tr_b16 a[22:23], v22 offset:61920\n"
"s_waitcnt lgkmcnt(4)\n"
"v_mfma_f32_16x16x32_bf16 v[248:251], a[4:7], a[184:187], v[248:251]\n"
"buffer_load_dwordx4 a[48:51], v19, s[52:55], s30 offen offset:0 sc1\n"
"v_mfma_f32_16x16x32_bf16 v[144:147], a[8:11], a[192:195], v[144:147]\n"
"ds_read_b64_tr_b16 a[24:25], v21 offset:57856\n"
"v_mfma_f32_16x16x32_bf16 v[144:147], a[12:15], a[196:199], v[144:147]\n"
"s_addk_i32 s28, 4096\n"
"ds_read_b64_tr_b16 a[26:27], v21 offset:61952\n"
"v_mfma_f32_16x16x32_bf16 v[148:151], a[12:15], a[204:207], v[148:151]\n"
"ds_read_b64_tr_b16 a[28:29], v22 offset:57856\n"
"v_mfma_f32_16x16x32_bf16 v[148:151], a[8:11], a[200:203], v[148:151]\n"
"ds_read_b64_tr_b16 a[30:31], v22 offset:61952\n"
"v_mfma_f32_16x16x32_bf16 v[156:159], a[8:11], a[216:219], v[156:159]\n"
"buffer_load_dwordx4 v[60:63], v19, s[52:55], s28 offen offset:0 sc1\n"
"v_mfma_f32_16x16x32_bf16 v[152:155], a[8:11], a[208:211], v[152:155]\n"
"s_waitcnt lgkmcnt(4)\n"
"ds_read_b64_tr_b16 a[0:1], v21 offset:57888\n"
"v_mfma_f32_16x16x32_bf16 v[152:155], a[12:15], a[212:215], v[152:155]\n"
"s_addk_i32 s90, 4096\n"
"ds_read_b64_tr_b16 a[2:3], v21 offset:61984\n"
"v_mfma_f32_16x16x32_bf16 v[160:163], a[16:19], a[192:195], v[160:163]\n"
"ds_read_b64_tr_b16 a[4:5], v22 offset:57888\n"
"v_mfma_f32_16x16x32_bf16 v[160:163], a[20:23], a[196:199], v[160:163]\n"
"ds_read_b64_tr_b16 a[6:7], v22 offset:61984\n"
"v_mfma_f32_16x16x32_bf16 v[164:167], a[20:23], a[204:207], v[164:167]\n"
"buffer_load_dwordx4 v[48:51], v19, s[52:55], s90 offen offset:0 sc1\n"
"v_mfma_f32_16x16x32_bf16 v[164:167], a[16:19], a[200:203], v[164:167]\n"
"s_addk_i32 s28, 4096\n"
"ds_read_b64_tr_b16 a[8:9], v21 offset:57920\n"
"v_mfma_f32_16x16x32_bf16 v[172:175], a[16:19], a[216:219], v[172:175]\n"
"ds_read_b64_tr_b16 a[10:11], v21 offset:62016\n"
"v_mfma_f32_16x16x32_bf16 v[168:171], a[16:19], a[208:211], v[168:171]\n"
"s_waitcnt lgkmcnt(5)\n"
"ds_read_b64_tr_b16 a[12:13], v22 offset:57920\n"
"v_mfma_f32_16x16x32_bf16 v[168:171], a[20:23], a[212:215], v[168:171]\n"
"buffer_load_dwordx4 v[64:67], v19, s[52:55], s28 offen offset:0 sc1\n"
"v_mfma_f32_16x16x32_bf16 v[176:179], a[24:27], a[192:195], v[176:179]\n"
"ds_read_b64_tr_b16 a[14:15], v22 offset:62016\n"
"v_mfma_f32_16x16x32_bf16 v[176:179], a[28:31], a[196:199], v[176:179]\n"
"ds_read_b64_tr_b16 a[16:17], v21 offset:57952\n"
"v_mfma_f32_16x16x32_bf16 v[180:183], a[28:31], a[204:207], v[180:183]\n"
"ds_read_b64_tr_b16 a[18:19], v21 offset:62048\n"
"v_mfma_f32_16x16x32_bf16 v[180:183], a[24:27], a[200:203], v[180:183]\n"
"ds_read_b64_tr_b16 a[20:21], v22 offset:57952\n"
"v_mfma_f32_16x16x32_bf16 v[188:191], a[24:27], a[216:219], v[188:191]\n"
"buffer_load_dwordx4 v12, s[36:39], s29 offen offset:1024 lds\n"
"v_mfma_f32_16x16x32_bf16 v[184:187], a[24:27], a[208:211], v[184:187]\n"
"s_waitcnt lgkmcnt(4)\n"
"ds_read_b64_tr_b16 a[22:23], v22 offset:62048\n"
"v_mfma_f32_16x16x32_bf16 v[184:187], a[28:31], a[212:215], v[184:187]\n"
"s_addk_i32 s90, 4096\n"
"ds_read_b64_tr_b16 a[24:25], v21 offset:57984\n"
"v_mfma_f32_16x16x32_bf16 v[192:195], a[0:3], a[192:195], v[192:195]\n"
"ds_read_b64_tr_b16 a[26:27], v21 offset:62080\n"
"v_subrev_u32 v21, s27, v21\n"
"v_mfma_f32_16x16x32_bf16 v[192:195], a[4:7], a[196:199], v[192:195]\n"
"ds_read_b64_tr_b16 a[28:29], v22 offset:57984\n"
"v_mfma_f32_16x16x32_bf16 v[196:199], a[4:7], a[204:207], v[196:199]\n"
"buffer_load_dwordx4 v[52:55], v19, s[52:55], s90 offen offset:0 sc1\n"
"v_mfma_f32_16x16x32_bf16 v[196:199], a[0:3], a[200:203], v[196:199]\n"
"s_addk_i32 s28, 4096\n"
"ds_read_b64_tr_b16 a[30:31], v22 offset:62080\n"
"v_subrev_u32 v22, s27, v22\n"
"v_mfma_f32_16x16x32_bf16 v[204:207], a[0:3], a[216:219], v[204:207]\n"
"v_mfma_f32_16x16x32_bf16 v[200:203], a[0:3], a[208:211], v[200:203]\n"
"s_waitcnt lgkmcnt(4)\n"
"v_mfma_f32_16x16x32_bf16 v[200:203], a[4:7], a[212:215], v[200:203]\n"
"buffer_load_dwordx4 v[68:71], v19, s[52:55], s28 offen offset:0 sc1\n"
"v_mfma_f32_16x16x32_bf16 v[208:211], a[8:11], a[192:195], v[208:211]\n"
"PG14_%=:\n"
"s_cbranch_execz PGX%=\n"
"v_mfma_f32_16x16x32_bf16 v[208:211], a[12:15], a[196:199], v[208:211]\n"
"v_mfma_f32_16x16x32_bf16 v[212:215], a[12:15], a[204:207], v[212:215]\n"
"v_mfma_f32_16x16x32_bf16 v[212:215], a[8:11], a[200:203], v[212:215]\n"
"v_mfma_f32_16x16x32_bf16 v[220:223], a[8:11], a[216:219], v[220:223]\n"
"buffer_load_dwordx4 v13, s[36:39], s29 offen offset:2048 lds\n"
"v_mfma_f32_16x16x32_bf16 v[216:219], a[8:11], a[208:211], v[216:219]\n"
"v_mfma_f32_16x16x32_bf16 v[216:219], a[12:15], a[212:215], v[216:219]\n"
"s_addk_i32 s90, 4096\n"
"v_mfma_f32_16x16x32_bf16 v[224:227], a[16:19], a[192:195], v[224:227]\n"
"v_mfma_f32_16x16x32_bf16 v[224:227], a[20:23], a[196:199], v[224:227]\n"
"s_waitcnt lgkmcnt(0)\n"
"v_mfma_f32_16x16x32_bf16 v[228:231], a[20:23], a[204:207], v[228:231]\n"
"s_barrier\n"
"v_mfma_f32_16x16x32_bf16 v[228:231], a[16:19], a[200:203], v[228:231]\n"
"s_addk_i32 s28, 4096\n"
"buffer_load_dwordx4 v[56:59], v19, s[52:55], s90 offen offset:0 sc1\n"
"v_mfma_f32_16x16x32_bf16 v[236:239], a[16:19], a[216:219], v[236:239]\n"
"ds_read_b64_tr_b16 a[0:1], v21 offset:33024\n"
"v_mfma_f32_16x16x32_bf16 v[232:235], a[16:19], a[208:211], v[232:235]\n"
"ds_read_b64_tr_b16 a[2:3], v21 offset:37120\n"
"v_mfma_f32_16x16x32_bf16 v[232:235], a[20:23], a[212:215], v[232:235]\n"
"ds_read_b64_tr_b16 a[4:5], v22 offset:33024\n"
"v_mfma_f32_16x16x32_bf16 v[240:243], a[24:27], a[192:195], v[240:243]\n"
"ds_read_b64_tr_b16 a[6:7], v22 offset:37120\n"
"v_mfma_f32_16x16x32_bf16 v[240:243], a[28:31], a[196:199], v[240:243]\n"
"buffer_load_dwordx4 v[72:75], v19, s[52:55], s28 offen offset:0 sc1\n"
"v_mfma_f32_16x16x32_bf16 v[244:247], a[28:31], a[204:207], v[244:247]\n"
"ds_read_b64_tr_b16 a[8:9], v21 offset:33056\n"
"v_mfma_f32_16x16x32_bf16 v[244:247], a[24:27], a[200:203], v[244:247]\n"
"ds_read_b64_tr_b16 a[10:11], v21 offset:37152\n"
"v_mfma_f32_16x16x32_bf16 v[252:255], a[24:27], a[216:219], v[252:255]\n"
"buffer_load_dwordx4 v14, s[36:39], s29 offen offset:3072 lds\n"
"v_mfma_f32_16x16x32_bf16 v[248:251], a[24:27], a[208:211], v[248:251]\n"
"s_waitcnt vmcnt(13)\n"
"ds_read_b64_tr_b16 a[12:13], v22 offset:33056\n"
"s_waitcnt lgkmcnt(3)\n"
"v_mfma_f32_16x16x32_bf16 v[248:251], a[28:31], a[212:215], v[248:251]\n"
"s_addk_i32 s30, 4096\n"
"ds_read_b64_tr_b16 a[14:15], v22 offset:37152\n"
"v_mfma_f32_16x16x32_bf16 v[144:147], a[0:3], a[220:223], v[144:147]\n"
"s_buffer_load_dwordx8 s[40:47], s[52:55], s95 offset:384 glc\n"
"ds_read_b64_tr_b16 a[16:17], v21 offset:33088\n"
"v_mfma_f32_16x16x32_bf16 v[144:147], a[4:7], a[224:227], v[144:147]\n"
"buffer_load_dwordx4 v[88:91], v19, s[52:55], s30 offen offset:0 sc1\n"
"v_mfma_f32_16x16x32_bf16 v[148:151], a[4:7], a[232:235], v[148:151]\n"
"ds_read_b64_tr_b16 a[18:19], v21 offset:37184\n"
"v_mfma_f32_16x16x32_bf16 v[148:151], a[0:3], a[228:231], v[148:151]\n"
"s_addk_i32 s28, 4096\n"
"ds_read_b64_tr_b16 a[20:21], v22 offset:33088\n"
"v_mfma_f32_16x16x32_bf16 v[156:159], a[0:3], a[244:247], v[156:159]\n"
"ds_read_b64_tr_b16 a[22:23], v22 offset:37184\n"
"v_mfma_f32_16x16x32_bf16 v[152:155], a[0:3], a[236:239], v[152:155]\n"
"s_waitcnt lgkmcnt(4)\n"
"ds_read_b64_tr_b16 a[24:25], v21 offset:33120\n"
"v_mfma_f32_16x16x32_bf16 v[152:155], a[4:7], a[240:243], v[152:155]\n"
"buffer_load_dwordx4 v[76:79], v19, s[52:55], s28 offen offset:0 sc1\n"
"v_mfma_f32_16x16x32_bf16 v[160:163], a[8:11], a[220:223], v[160:163]\n"
"s_addk_i32 s30, 4096\n"
"ds_read_b64_tr_b16 a[26:27], v21 offset:37216\n"
"v_mfma_f32_16x16x32_bf16 v[160:163], a[12:15], a[224:227], v[160:163]\n"
"ds_read_b64_tr_b16 a[28:29], v22 offset:33120\n"
"v_mfma_f32_16x16x32_bf16 v[164:167], a[12:15], a[232:235], v[164:167]\n"
"ds_read_b64_tr_b16 a[30:31], v22 offset:37216\n"
"v_mfma_f32_16x16x32_bf16 v[164:167], a[8:11], a[228:231], v[164:167]\n"
"buffer_load_dwordx4 v[92:95], v19, s[52:55], s30 offen offset:0 sc1\n"
"v_mfma_f32_16x16x32_bf16 v[172:175], a[8:11], a[244:247], v[172:175]\n"
"ds_read_b64_tr_b16 a[0:1], v21 offset:33152\n"
"v_mfma_f32_16x16x32_bf16 v[168:171], a[8:11], a[236:239], v[168:171]\n"
"s_waitcnt lgkmcnt(5)\n"
"ds_read_b64_tr_b16 a[2:3], v21 offset:37248\n"
"s_addk_i32 s30, 4096\n"
"v_mfma_f32_16x16x32_bf16 v[168:171], a[12:15], a[240:243], v[168:171]\n"
"ds_read_b64_tr_b16 a[4:5], v22 offset:33152\n"
"v_mfma_f32_16x16x32_bf16 v[176:179], a[16:19], a[220:223], v[176:179]\n"
"ds_read_b64_tr_b16 a[6:7], v22 offset:37248\n"
"v_mfma_f32_16x16x32_bf16 v[176:179], a[20:23], a[224:227], v[176:179]\n"
"buffer_load_dwordx4 a[52:55], v19, s[52:55], s30 offen offset:0 sc1\n"
"v_mfma_f32_16x16x32_bf16 v[180:183], a[20:23], a[232:235], v[180:183]\n"
"ds_read_b64_tr_b16 a[8:9], v21 offset:33184\n"
"v_mfma_f32_16x16x32_bf16 v[180:183], a[16:19], a[228:231], v[180:183]\n"
"s_addk_i32 s28, 4096\n"
"ds_read_b64_tr_b16 a[10:11], v21 offset:37280\n"
"v_mfma_f32_16x16x32_bf16 v[188:191], a[16:19], a[244:247], v[188:191]\n"
"ds_read_b64_tr_b16 a[12:13], v22 offset:33184\n"
"v_mfma_f32_16x16x32_bf16 v[184:187], a[16:19], a[236:239], v[184:187]\n"
"s_waitcnt lgkmcnt(7)\n"
"ds_read_b64_tr_b16 a[14:15], v22 offset:37280\n"
"v_mfma_f32_16x16x32_bf16 v[184:187], a[20:23], a[240:243], v[184:187]\n"
"buffer_load_dwordx4 v[80:83], v19, s[52:55], s28 offen offset:0 sc1\n"
"v_mfma_f32_16x16x32_bf16 v[192:195], a[24:27], a[220:223], v[192:195]\n"
"s_addk_i32 s30, 4096\n"
"v_mfma_f32_16x16x32_bf16 v[192:195], a[28:31], a[224:227], v[192:195]\n"
"v_mfma_f32_16x16x32_bf16 v[196:199], a[28:31], a[232:235], v[196:199]\n"
"v_mfma_f32_16x16x32_bf16 v[196:199], a[24:27], a[228:231], v[196:199]\n"
"buffer_load_dwordx4 a[56:59], v19, s[52:55], s30 offen offset:0 sc1\n"
"v_mfma_f32_16x16x32_bf16 v[204:207], a[24:27], a[244:247], v[204:207]\n"
"v_mfma_f32_16x16x32_bf16 v[200:203], a[24:27], a[236:239], v[200:203]\n"
"s_waitcnt lgkmcnt(4)\n"
"s_addk_i32 s30, 4096\n"
"v_mfma_f32_16x16x32_bf16 v[200:203], a[28:31], a[240:243], v[200:203]\n"
"v_mfma_f32_16x16x32_bf16 v[208:211], a[0:3], a[220:223], v[208:211]\n"
"v_mfma_f32_16x16x32_bf16 v[208:211], a[4:7], a[224:227], v[208:211]\n"
"s_waitcnt lgkmcnt(0)\n"
"buffer_load_dwordx4 a[144:147], v19, s[52:55], s30 offen offset:0 sc1\n"
"v_mfma_f32_16x16x32_bf16 v[212:215], a[4:7], a[232:235], v[212:215]\n"
"s_barrier\n"
"v_mfma_f32_16x16x32_bf16 v[212:215], a[0:3], a[228:231], v[212:215]\n"
"s_addk_i32 s28, 4096\n"
"ds_read_b64_tr_b16 a[16:17], v21 offset:0\n"
"s_xor_b64 s[40:41], s[40:41], s[88:89]\n"
"v_mfma_f32_16x16x32_bf16 v[220:223], a[0:3], a[244:247], v[220:223]\n"
"s_xor_b64 s[42:43], s[42:43], s[88:89]\n"
"ds_read_b64_tr_b16 a[18:19], v21 offset:4096\n"
"s_xor_b64 s[44:45], s[44:45], s[88:89]\n"
"v_mfma_f32_16x16x32_bf16 v[216:219], a[0:3], a[236:239], v[216:219]\n"
"buffer_load_dwordx4 v[84:87], v19, s[52:55], s28 offen offset:0 sc1\n"
"v_mfma_f32_16x16x32_bf16 v[216:219], a[4:7], a[240:243], v[216:219]\n"
"s_xor_b64 s[46:47], s[46:47], s[88:89]\n"
"ds_read_b64_tr_b16 a[20:21], v22 offset:0\n"
"s_or_b64 s[40:41], s[40:41], s[42:43]\n"
"v_mfma_f32_16x16x32_bf16 v[224:227], a[8:11], a[220:223], v[224:227]\n"
"s_addk_i32 s30, 4096\n"
"ds_read_b64_tr_b16 a[22:23], v22 offset:4096\n"
"s_or_b64 s[44:45], s[44:45], s[46:47]\n"
"v_mfma_f32_16x16x32_bf16 v[224:227], a[12:15], a[224:227], v[224:227]\n"
"s_or_b64 s[40:41], s[40:41], s[44:45]\n"
"s_cbranch_scc1 POLL3%=\n"
"BACK3%=:\n"
"v_cmp_eq_u32 vcc, 0, v10\n"
"v_mfma_f32_16x16x32_bf16 v[228:231], a[12:15], a[232:235], v[228:231]\n"
"s_mov_b64 s[40:41], s[14:15]\n"
"ds_read_b64_tr_b16 a[24:25], v21 offset:32\n"
"v_cndmask_b32 v8, 0, 1, vcc\n"
"v_mfma_f32_16x16x32_bf16 v[228:231], a[8:11], a[228:231], v[228:231]\n"
"s_and_b32 s41, s41, 0xffff\n"
"buffer_load_dwordx4 a[148:151], v19, s[52:55], s30 offen offset:0 sc1\n"
"v_mfma_f32_16x16x32_bf16 v[236:239], a[8:11], a[244:247], v[236:239]\n"
"s_mov_b32 s42, 399840\n"
"ds_read_b64_tr_b16 a[26:27], v21 offset:4128\n"
"v_mul_u32_u24 v8, s85, v8\n"
"v_mfma_f32_16x16x32_bf16 v[232:235], a[8:11], a[236:239], v[232:235]\n"
"s_waitcnt vmcnt(19)\n"
"ds_read_b64_tr_b16 a[28:29], v22 offset:32\n"
"v_cmp_eq_u32 vcc, 1, v10\n"
"v_mfma_f32_16x16x32_bf16 v[232:235], a[12:15], a[240:243], v[232:235]\n"
"s_waitcnt lgkmcnt(3)\n"
"ds_read_b64_tr_b16 a[30:31], v22 offset:4128\n"
"v_cndmask_b32 v8, v8, 1.0, vcc\n"
"v_mfma_f32_16x16x32_bf16 v[144:147], a[16:19], a[248:251], v[144:147]\n"
"s_addk_i32 s90, 4096\n"
"ds_read_b64_tr_b16 a[0:1], v21 offset:64\n"
"v_accvgpr_write_b32 a192, v8\n"
"v_mfma_f32_16x16x32_bf16 v[144:147], a[20:23], a[252:255], v[144:147]\n"
"s_mov_b32 s43, 0x20000\n"
"ds_read_b64_tr_b16 a[2:3], v21 offset:4160\n"
"v_cmp_eq_u32 vcc, 2, v10\n"
"v_mfma_f32_16x16x32_bf16 v[148:151], a[20:23], a[36:39], v[148:151]\n"
"buffer_load_dwordx4 a[156:159], v19, s[52:55], s90 offen offset:0 sc1\n"
"v_mfma_f32_16x16x32_bf16 v[148:151], a[16:19], a[32:35], v[148:151]\n"
"s_addk_i32 s30, 4096\n"
"ds_read_b64_tr_b16 a[4:5], v22 offset:64\n"
"v_cndmask_b32 v8, 0, 1, vcc\n"
"v_mfma_f32_16x16x32_bf16 v[156:159], a[16:19], a[48:51], v[156:159]\n"
"ds_read_b64_tr_b16 a[6:7], v22 offset:4160\n"
"v_mul_u32_u24 v8, s85, v8\n"
"v_mfma_f32_16x16x32_bf16 v[152:155], a[16:19], a[40:43], v[152:155]\n"
"s_waitcnt lgkmcnt(4)\n"
"ds_read_b64_tr_b16 a[8:9], v21 offset:96\n"
"v_cmp_eq_u32 vcc, 3, v10\n"
"v_mfma_f32_16x16x32_bf16 v[152:155], a[20:23], a[44:47], v[152:155]\n"
"s_add_u32 s85, s24, -2885120\n"
"buffer_load_dwordx4 a[152:155], v19, s[52:55], s30 offen offset:0 sc1\n"
"v_mfma_f32_16x16x32_bf16 v[160:163], a[24:27], a[248:251], v[160:163]\n"
"s_and_b32 s85, s85, 1536\n"
"ds_read_b64_tr_b16 a[10:11], v21 offset:4192\n"
"v_cndmask_b32 v8, v8, 1.0, vcc\n"
"v_mfma_f32_16x16x32_bf16 v[160:163], a[28:31], a[252:255], v[160:163]\n"
"s_addk_i32 m0, 16512\n"
"ds_read_b64_tr_b16 a[12:13], v22 offset:96\n"
"v_accvgpr_write_b32 a193, v8\n"
"v_mfma_f32_16x16x32_bf16 v[164:167], a[28:31], a[36:39], v[164:167]\n"
"s_add_u32 s29, s67, 172032\n"
"ds_read_b64_tr_b16 a[14:15], v22 offset:4192\n"
"v_lshlrev_b32 v0, 2, v1\n"
"v_mfma_f32_16x16x32_bf16 v[164:167], a[24:27], a[32:35], v[164:167]\n"
"s_add_u32 s73, s73, s85\n"
"ds_read_b64_tr_b16 a[16:17], v21 offset:128\n"
"s_add_u32 s73, s73, 2885632\n"
"v_mfma_f32_16x16x32_bf16 v[172:175], a[24:27], a[48:51], v[172:175]\n"
"s_add_u32 s85, s24, -2884608\n"
"buffer_load_dwordx4 v11, s[36:39], s29 offen offset:0 lds\n"
"v_mfma_f32_16x16x32_bf16 v[168:171], a[24:27], a[40:43], v[168:171]\n"
"s_waitcnt lgkmcnt(4)\n"
"ds_read_b64_tr_b16 a[18:19], v21 offset:4224\n"
"s_and_b32 s85, s85, 1536\n"
"v_mfma_f32_16x16x32_bf16 v[168:171], a[28:31], a[44:47], v[168:171]\n"
"s_addk_i32 s90, 4096\n"
"ds_read_b64_tr_b16 a[20:21], v22 offset:128\n"
"s_add_u32 s75, s75, s85\n"
"v_mfma_f32_16x16x32_bf16 v[176:179], a[0:3], a[248:251], v[176:179]\n"
"s_add_u32 s75, s75, 2885640\n"
"ds_read_b64_tr_b16 a[22:23], v22 offset:4224\n"
"s_add_u32 s85, s24, -2884096\n"
"v_mfma_f32_16x16x32_bf16 v[176:179], a[4:7], a[252:255], v[176:179]\n"
"s_and_b32 s85, s85, 1536\n"
"ds_read_b64_tr_b16 a[24:25], v21 offset:160\n"
"s_add_u32 s77, s77, s85\n"
"v_mfma_f32_16x16x32_bf16 v[180:183], a[4:7], a[36:39], v[180:183]\n"
"s_add_u32 s77, s77, 2885648\n"
"buffer_load_dwordx4 a[160:163], v19, s[52:55], s90 offen offset:0 sc1\n"
"v_mfma_f32_16x16x32_bf16 v[180:183], a[0:3], a[32:35], v[180:183]\n"
"ds_read_b64_tr_b16 a[26:27], v21 offset:4256\n"
"v_mfma_f32_16x16x32_bf16 v[188:191], a[0:3], a[48:51], v[188:191]\n"
"ds_read_b64_tr_b16 a[28:29], v22 offset:160\n"
"v_mfma_f32_16x16x32_bf16 v[184:187], a[0:3], a[40:43], v[184:187]\n"
"s_waitcnt lgkmcnt(5)\n"
"ds_read_b64_tr_b16 a[30:31], v22 offset:4256\n"
"v_mfma_f32_16x16x32_bf16 v[184:187], a[4:7], a[44:47], v[184:187]\n"
"buffer_load_dwordx4 v12, s[36:39], s29 offen offset:1024 lds\n"
"v_mfma_f32_16x16x32_bf16 v[192:195], a[8:11], a[248:251], v[192:195]\n"
"v_mfma_f32_16x16x32_bf16 v[192:195], a[12:15], a[252:255], v[192:195]\n"
"v_mfma_f32_16x16x32_bf16 v[196:199], a[12:15], a[36:39], v[196:199]\n"
"v_mfma_f32_16x16x32_bf16 v[196:199], a[8:11], a[32:35], v[196:199]\n"
"v_mfma_f32_16x16x32_bf16 v[204:207], a[8:11], a[48:51], v[204:207]\n"
"buffer_load_dwordx4 v13, s[36:39], s29 offen offset:2048 lds\n"
"v_mfma_f32_16x16x32_bf16 v[200:203], a[8:11], a[40:43], v[200:203]\n"
"s_waitcnt lgkmcnt(0)\n"
"v_mfma_f32_16x16x32_bf16 v[200:203], a[12:15], a[44:47], v[200:203]\n"
"s_addk_i32 s90, 4096\n"
"v_mfma_f32_16x16x32_bf16 v[208:211], a[16:19], a[248:251], v[208:211]\n"
"v_mfma_f32_16x16x32_bf16 v[208:211], a[20:23], a[252:255], v[208:211]\n"
"v_mfma_f32_16x16x32_bf16 v[212:215], a[20:23], a[36:39], v[212:215]\n"
"s_barrier\n"
"v_mfma_f32_16x16x32_bf16 v[212:215], a[16:19], a[32:35], v[212:215]\n"
"s_mov_b64 s[4:5], s[88:89]\n"
"buffer_load_dwordx4 a[164:167], v19, s[52:55], s90 offen offset:0 sc1\n"
"v_mfma_f32_16x16x32_bf16 v[220:223], a[16:19], a[48:51], v[220:223]\n"
"s_mov_b64 s[6:7], s[88:89]\n"
"ds_read_b64_tr_b16 a[0:1], v21 offset:8256\n"
"s_mov_b64 s[8:9], s[88:89]\n"
"v_mfma_f32_16x16x32_bf16 v[216:219], a[16:19], a[40:43], v[216:219]\n"
"ds_read_b64_tr_b16 a[2:3], v21 offset:12352\n"
"v_mfma_f32_16x16x32_bf16 v[216:219], a[20:23], a[44:47], v[216:219]\n"
"ds_read_b64_tr_b16 a[4:5], v22 offset:8256\n"
"v_mfma_f32_16x16x32_bf16 v[224:227], a[24:27], a[248:251], v[224:227]\n"
"ds_read_b64_tr_b16 a[6:7], v22 offset:12352\n"
"v_mfma_f32_16x16x32_bf16 v[224:227], a[28:31], a[252:255], v[224:227]\n"
"buffer_load_dwordx4 v14, s[36:39], s29 offen offset:3072 lds\n"
"v_mfma_f32_16x16x32_bf16 v[228:231], a[28:31], a[36:39], v[228:231]\n"
"s_addk_i32 m0, 8256\n"
"ds_read_b64_tr_b16 a[8:9], v21 offset:8288\n"
"s_add_u32 s29, s68, 172032\n"
"v_mfma_f32_16x16x32_bf16 v[228:231], a[24:27], a[32:35], v[228:231]\n"
"ds_read_b64_tr_b16 a[10:11], v21 offset:12384\n"
"v_mfma_f32_16x16x32_bf16 v[236:239], a[24:27], a[48:51], v[236:239]\n"
"buffer_load_dwordx4 v11, s[36:39], s29 offen offset:0 lds\n"
"v_mfma_f32_16x16x32_bf16 v[232:235], a[24:27], a[40:43], v[232:235]\n"
"s_waitcnt vmcnt(20)\n"
"ds_read_b64_tr_b16 a[12:13], v22 offset:8288\n"
"s_waitcnt lgkmcnt(3)\n"
"v_mfma_f32_16x16x32_bf16 v[232:235], a[28:31], a[44:47], v[232:235]\n"
"s_addk_i32 s90, 4096\n"
"ds_read_b64_tr_b16 a[14:15], v22 offset:12384\n"
"v_mfma_f32_16x16x32_bf16 v[144:147], a[0:3], v[32:35], v[144:147]\n"
"ds_read_b64_tr_b16 a[16:17], v21 offset:8320\n"
"v_mfma_f32_16x16x32_bf16 v[144:147], a[4:7], v[36:39], v[144:147]\n"
"ds_read_b64_tr_b16 a[18:19], v21 offset:12416\n"
"v_mfma_f32_16x16x32_bf16 v[148:151], a[4:7], v[44:47], v[148:151]\n"
"buffer_load_dwordx4 a[168:171], v19, s[52:55], s90 offen offset:0 sc1\n"
"v_mfma_f32_16x16x32_bf16 v[148:151], a[0:3], v[40:43], v[148:151]\n"
"ds_read_b64_tr_b16 a[20:21], v22 offset:8320\n"
"v_mfma_f32_16x16x32_bf16 v[156:159], a[0:3], v[56:59], v[156:159]\n"
"ds_read_b64_tr_b16 a[22:23], v22 offset:12416\n"
"v_mfma_f32_16x16x32_bf16 v[152:155], a[0:3], v[48:51], v[152:155]\n"
"s_waitcnt lgkmcnt(4)\n"
"ds_read_b64_tr_b16 a[24:25], v21 offset:8352\n"
"v_mfma_f32_16x16x32_bf16 v[152:155], a[4:7], v[52:55], v[152:155]\n"
"buffer_load_dwordx4 v12, s[36:39], s29 offen offset:1024 lds\n"
"v_mfma_f32_16x16x32_bf16 v[160:163], a[8:11], v[32:35], v[160:163]\n"
"ds_read_b64_tr_b16 a[26:27], v21 offset:12448\n"
"v_mfma_f32_16x16x32_bf16 v[160:163], a[12:15], v[36:39], v[160:163]\n"
"ds_read_b64_tr_b16 a[28:29], v22 offset:8352\n"
"v_mfma_f32_16x16x32_bf16 v[164:167], a[12:15], v[44:47], v[164:167]\n"
"ds_read_b64_tr_b16 a[30:31], v22 offset:12448\n"
"v_mfma_f32_16x16x32_bf16 v[164:167], a[8:11], v[40:43], v[164:167]\n"
"ds_read_b64_tr_b16 a[0:1], v21 offset:8384\n"
"v_mfma_f32_16x16x32_bf16 v[172:175], a[8:11], v[56:59], v[172:175]\n"
"buffer_load_dwordx4 v13, s[36:39], s29 offen offset:2048 lds\n"
"v_mfma_f32_16x16x32_bf16 v[168:171], a[8:11], v[48:51], v[168:171]\n"
"s_waitcnt lgkmcnt(4)\n"
"ds_read_b64_tr_b16 a[2:3], v21 offset:12480\n"
"v_mfma_f32_16x16x32_bf16 v[168:171], a[12:15], v[52:55], v[168:171]\n"
"s_addk_i32 s90, 4096\n"
"ds_read_b64_tr_b16 a[4:5], v22 offset:8384\n"
"v_mfma_f32_16x16x32_bf16 v[176:179], a[16:19], v[32:35], v[176:179]\n"
"ds_read_b64_tr_b16 a[6:7], v22 offset:12480\n"
"v_mfma_f32_16x16x32_bf16 v[176:179], a[20:23], v[36:39], v[176:179]\n"
"ds_read_b64_tr_b16 a[8:9], v21 offset:8416\n"
"v_mfma_f32_16x16x32_bf16 v[180:183], a[20:23], v[44:47], v[180:183]\n"
"buffer_load_dwordx4 a[172:175], v19, s[52:55], s90 offen offset:0 sc1\n"
"v_mfma_f32_16x16x32_bf16 v[180:183], a[16:19], v[40:43], v[180:183]\n"
"ds_read_b64_tr_b16 a[10:11], v21 offset:12512\n"
"v_mfma_f32_16x16x32_bf16 v[188:191], a[16:19], v[56:59], v[188:191]\n"
"ds_read_b64_tr_b16 a[12:13], v22 offset:8416\n"
"v_mfma_f32_16x16x32_bf16 v[184:187], a[16:19], v[48:51], v[184:187]\n"
"s_waitcnt lgkmcnt(5)\n"
"ds_read_b64_tr_b16 a[14:15], v22 offset:12512\n"
"v_mfma_f32_16x16x32_bf16 v[184:187], a[20:23], v[52:55], v[184:187]\n"
"buffer_load_dwordx4 v14, s[36:39], s29 offen offset:3072 lds\n"
"v_mfma_f32_16x16x32_bf16 v[192:195], a[24:27], v[32:35], v[192:195]\n"
"v_mfma_f32_16x16x32_bf16 v[192:195], a[28:31], v[36:39], v[192:195]\n"
"s_addk_i32 m0, 8256\n"
"s_add_u32 s29, s69, 172032\n"
"v_mfma_f32_16x16x32_bf16 v[196:199], a[28:31], v[44:47], v[196:199]\n"
"v_mfma_f32_16x16x32_bf16 v[196:199], a[24:27], v[40:43], v[196:199]\n"
"v_mfma_f32_16x16x32_bf16 v[204:207], a[24:27], v[56:59], v[204:207]\n"
"buffer_load_dwordx4 v11, s[36:39], s29 offen offset:0 lds\n"
"v_mfma_f32_16x16x32_bf16 v[200:203], a[24:27], v[48:51], v[200:203]\n"
"s_waitcnt lgkmcnt(0)\n"
"v_mfma_f32_16x16x32_bf16 v[200:203], a[28:31], v[52:55], v[200:203]\n"
"s_addk_i32 s90, 4096\n"
"v_mfma_f32_16x16x32_bf16 v[208:211], a[0:3], v[32:35], v[208:211]\n"
"v_mfma_f32_16x16x32_bf16 v[208:211], a[4:7], v[36:39], v[208:211]\n"
"v_mfma_f32_16x16x32_bf16 v[212:215], a[4:7], v[44:47], v[212:215]\n"
"s_barrier\n"
"v_mfma_f32_16x16x32_bf16 v[212:215], a[0:3], v[40:43], v[212:215]\n"
"buffer_load_dwordx4 a[176:179], v19, s[52:55], s90 offen offset:0 sc1\n"
"v_mfma_f32_16x16x32_bf16 v[220:223], a[0:3], v[56:59], v[220:223]\n"
"ds_read_b64_tr_b16 a[16:17], v21 offset:16512\n"
"v_mfma_f32_16x16x32_bf16 v[216:219], a[0:3], v[48:51], v[216:219]\n"
"ds_read_b64_tr_b16 a[18:19], v21 offset:20608\n"
"v_mfma_f32_16x16x32_bf16 v[216:219], a[4:7], v[52:55], v[216:219]\n"
"ds_read_b64_tr_b16 a[20:21], v22 offset:16512\n"
"v_mfma_f32_16x16x32_bf16 v[224:227], a[8:11], v[32:35], v[224:227]\n"
"ds_read_b64_tr_b16 a[22:23], v22 offset:20608\n"
"v_mfma_f32_16x16x32_bf16 v[224:227], a[12:15], v[36:39], v[224:227]\n"
"buffer_load_dwordx4 v12, s[36:39], s29 offen offset:1024 lds\n"
"v_mfma_f32_16x16x32_bf16 v[228:231], a[12:15], v[44:47], v[228:231]\n"
"ds_read_b64_tr_b16 a[24:25], v21 offset:16544\n"
"v_mfma_f32_16x16x32_bf16 v[228:231], a[8:11], v[40:43], v[228:231]\n"
"ds_read_b64_tr_b16 a[26:27], v21 offset:20640\n"
"v_mfma_f32_16x16x32_bf16 v[236:239], a[8:11], v[56:59], v[236:239]\n"
"buffer_load_dwordx4 v13, s[36:39], s29 offen offset:2048 lds\n"
"v_mfma_f32_16x16x32_bf16 v[232:235], a[8:11], v[48:51], v[232:235]\n"
"s_waitcnt vmcnt(19)\n"
"ds_read_b64_tr_b16 a[28:29], v22 offset:16544\n"
"s_waitcnt lgkmcnt(3)\n"
"v_mfma_f32_16x16x32_bf16 v[232:235], a[12:15], v[52:55], v[232:235]\n"
"s_addk_i32 s90, 4096\n"
"ds_read_b64_tr_b16 a[30:31], v22 offset:20640\n"
"v_mfma_f32_16x16x32_bf16 v[144:147], a[16:19], v[60:63], v[144:147]\n"
"ds_read_b64_tr_b16 a[0:1], v21 offset:16576\n"
"v_mfma_f32_16x16x32_bf16 v[144:147], a[20:23], v[64:67], v[144:147]\n"
"ds_read_b64_tr_b16 a[2:3], v21 offset:20672\n"
"v_mfma_f32_16x16x32_bf16 v[148:151], a[20:23], v[72:75], v[148:151]\n"
"buffer_load_dwordx4 v[108:111], v19, s[52:55], s90 offen offset:0 sc1\n"
"v_mfma_f32_16x16x32_bf16 v[148:151], a[16:19], v[68:71], v[148:151]\n"
"ds_read_b64_tr_b16 a[4:5], v22 offset:16576\n"
"v_mfma_f32_16x16x32_bf16 v[156:159], a[16:19], v[84:87], v[156:159]\n"
"ds_read_b64_tr_b16 a[6:7], v22 offset:20672\n"
"v_mfma_f32_16x16x32_bf16 v[152:155], a[16:19], v[76:79], v[152:155]\n"
"s_waitcnt lgkmcnt(4)\n"
"ds_read_b64_tr_b16 a[8:9], v21 offset:16608\n"
"v_mfma_f32_16x16x32_bf16 v[152:155], a[20:23], v[80:83], v[152:155]\n"
"buffer_load_dwordx4 v14, s[36:39], s29 offen offset:3072 lds\n"
"v_mfma_f32_16x16x32_bf16 v[160:163], a[24:27], v[60:63], v[160:163]\n"
"ds_read_b64_tr_b16 a[10:11], v21 offset:20704\n"
"v_mfma_f32_16x16x32_bf16 v[160:163], a[28:31], v[64:67], v[160:163]\n"
"s_addk_i32 s28, 4096\n"
"ds_read_b64_tr_b16 a[12:13], v22 offset:16608\n"
"v_mfma_f32_16x16x32_bf16 v[164:167], a[28:31], v[72:75], v[164:167]\n"
"ds_read_b64_tr_b16 a[14:15], v22 offset:20704\n"
"v_mfma_f32_16x16x32_bf16 v[164:167], a[24:27], v[68:71], v[164:167]\n"
"ds_read_b64_tr_b16 a[16:17], v21 offset:16640\n"
"v_mfma_f32_16x16x32_bf16 v[172:175], a[24:27], v[84:87], v[172:175]\n"
"buffer_load_dwordx4 v[112:115], v19, s[52:55], s28 offen offset:0 sc1\n"
"v_mfma_f32_16x16x32_bf16 v[168:171], a[24:27], v[76:79], v[168:171]\n"
"s_waitcnt lgkmcnt(4)\n"
"ds_read_b64_tr_b16 a[18:19], v21 offset:20736\n"
"s_addk_i32 s28, 4096\n"
"v_mfma_f32_16x16x32_bf16 v[168:171], a[28:31], v[80:83], v[168:171]\n"
"ds_read_b64_tr_b16 a[20:21], v22 offset:16640\n"
"v_mfma_f32_16x16x32_bf16 v[176:179], a[0:3], v[60:63], v[176:179]\n"
"ds_read_b64_tr_b16 a[22:23], v22 offset:20736\n"
"v_mfma_f32_16x16x32_bf16 v[176:179], a[4:7], v[64:67], v[176:179]\n"
"buffer_load_dwordx4 v[116:119], v19, s[52:55], s28 offen offset:0 sc1\n"
"v_mfma_f32_16x16x32_bf16 v[180:183], a[4:7], v[72:75], v[180:183]\n"
"ds_read_b64_tr_b16 a[24:25], v21 offset:16672\n"
"v_mfma_f32_16x16x32_bf16 v[180:183], a[0:3], v[68:71], v[180:183]\n"
"s_addk_i32 s28, 4096\n"
"ds_read_b64_tr_b16 a[26:27], v21 offset:20768\n"
"v_mfma_f32_16x16x32_bf16 v[188:191], a[0:3], v[84:87], v[188:191]\n"
"ds_read_b64_tr_b16 a[28:29], v22 offset:16672\n"
"v_mfma_f32_16x16x32_bf16 v[184:187], a[0:3], v[76:79], v[184:187]\n"
"s_waitcnt lgkmcnt(5)\n"
"ds_read_b64_tr_b16 a[30:31], v22 offset:20768\n"
"v_mfma_f32_16x16x32_bf16 v[184:187], a[4:7], v[80:83], v[184:187]\n"
"buffer_load_dwordx4 v[120:123], v19, s[52:55], s28 offen offset:0 sc1\n"
"v_mfma_f32_16x16x32_bf16 v[192:195], a[8:11], v[60:63], v[192:195]\n"
"s_addk_i32 s28, 4096\n"
"v_mfma_f32_16x16x32_bf16 v[192:195], a[12:15], v[64:67], v[192:195]\n"
"v_mfma_f32_16x16x32_bf16 v[196:199], a[12:15], v[72:75], v[196:199]\n"
"v_mfma_f32_16x16x32_bf16 v[196:199], a[8:11], v[68:71], v[196:199]\n"
"buffer_load_dwordx4 v[124:127], v19, s[52:55], s28 offen offset:0 sc1\n"
"v_mfma_f32_16x16x32_bf16 v[204:207], a[8:11], v[84:87], v[204:207]\n"
"v_mfma_f32_16x16x32_bf16 v[200:203], a[8:11], v[76:79], v[200:203]\n"
"s_waitcnt lgkmcnt(0)\n"
"s_addk_i32 s28, 4096\n"
"v_mfma_f32_16x16x32_bf16 v[200:203], a[12:15], v[80:83], v[200:203]\n"
"v_mfma_f32_16x16x32_bf16 v[208:211], a[16:19], v[60:63], v[208:211]\n"
"v_mfma_f32_16x16x32_bf16 v[208:211], a[20:23], v[64:67], v[208:211]\n"
"buffer_load_dwordx4 v[128:131], v19, s[52:55], s28 offen offset:0 sc1\n"
"v_mfma_f32_16x16x32_bf16 v[212:215], a[20:23], v[72:75], v[212:215]\n"
"s_barrier\n"
"v_mfma_f32_16x16x32_bf16 v[212:215], a[16:19], v[68:71], v[212:215]\n"
"s_addk_i32 s28, 4096\n"
"ds_read_b64_tr_b16 a[0:1], v21 offset:24768\n"
"v_mfma_f32_16x16x32_bf16 v[220:223], a[16:19], v[84:87], v[220:223]\n"
"ds_read_b64_tr_b16 a[2:3], v21 offset:28864\n"
"v_mfma_f32_16x16x32_bf16 v[216:219], a[16:19], v[76:79], v[216:219]\n"
"ds_read_b64_tr_b16 a[4:5], v22 offset:24768\n"
"v_mfma_f32_16x16x32_bf16 v[216:219], a[20:23], v[80:83], v[216:219]\n"
"ds_read_b64_tr_b16 a[6:7], v22 offset:28864\n"
"v_mfma_f32_16x16x32_bf16 v[224:227], a[24:27], v[60:63], v[224:227]\n"
"s_addk_i32 s30, 4096\n"
"buffer_load_dwordx4 v[132:135], v19, s[52:55], s28 offen offset:0 sc1\n"
"v_mfma_f32_16x16x32_bf16 v[224:227], a[28:31], v[64:67], v[224:227]\n"
"ds_read_b64_tr_b16 a[8:9], v21 offset:24800\n"
"v_mfma_f32_16x16x32_bf16 v[228:231], a[28:31], v[72:75], v[228:231]\n"
"ds_read_b64_tr_b16 a[10:11], v21 offset:28896\n"
"v_mfma_f32_16x16x32_bf16 v[228:231], a[24:27], v[68:71], v[228:231]\n"
"buffer_load_dwordx4 v[140:143], v19, s[52:55], s30 offen offset:0 sc1\n"
"v_mfma_f32_16x16x32_bf16 v[236:239], a[24:27], v[84:87], v[236:239]\n"
"ds_read_b64_tr_b16 a[12:13], v22 offset:24800\n"
"v_mfma_f32_16x16x32_bf16 v[232:235], a[24:27], v[76:79], v[232:235]\n"
"s_waitcnt vmcnt(25)\n"
"ds_read_b64_tr_b16 a[14:15], v22 offset:28896\n"
"s_waitcnt lgkmcnt(4)\n"
"v_mfma_f32_16x16x32_bf16 v[232:235], a[28:31], v[80:83], v[232:235]\n"
"s_addk_i32 s30, 4096\n"
"ds_read_b64_tr_b16 a[16:17], v21 offset:24832\n"
"v_mfma_f32_16x16x32_bf16 v[144:147], a[0:3], v[88:91], v[144:147]\n"
"ds_read_b64_tr_b16 a[18:19], v21 offset:28928\n"
"v_mfma_f32_16x16x32_bf16 v[144:147], a[4:7], v[92:95], v[144:147]\n"
"buffer_load_dwordx4 a[180:183], v19, s[52:55], s30 offen offset:0 sc1\n"
"v_mfma_f32_16x16x32_bf16 v[148:151], a[4:7], a[56:59], v[148:151]\n"
"s_addk_i32 s28, 4096\n"
"ds_read_b64_tr_b16 a[20:21], v22 offset:24832\n"
"v_mfma_f32_16x16x32_bf16 v[148:151], a[0:3], a[52:55], v[148:151]\n"
"ds_read_b64_tr_b16 a[22:23], v22 offset:28928\n"
"v_mfma_f32_16x16x32_bf16 v[156:159], a[0:3], a[152:155], v[156:159]\n"
"ds_read_b64_tr_b16 a[24:25], v21 offset:24864\n"
"v_mfma_f32_16x16x32_bf16 v[152:155], a[0:3], a[144:147], v[152:155]\n"
"s_waitcnt lgkmcnt(5)\n"
"buffer_load_dwordx4 v[136:139], v19, s[52:55], s28 offen offset:0 sc1\n"
"v_mfma_f32_16x16x32_bf16 v[152:155], a[4:7], a[148:151], v[152:155]\n"
"ds_read_b64_tr_b16 a[26:27], v21 offset:28960\n"
"v_mfma_f32_16x16x32_bf16 v[160:163], a[8:11], v[88:91], v[160:163]\n"
"s_addk_i32 s30, 4096\n"
"ds_read_b64_tr_b16 a[28:29], v22 offset:24864\n"
"v_mfma_f32_16x16x32_bf16 v[160:163], a[12:15], v[92:95], v[160:163]\n"
"ds_read_b64_tr_b16 a[30:31], v22 offset:28960\n"
"v_mfma_f32_16x16x32_bf16 v[164:167], a[12:15], a[56:59], v[164:167]\n"
"ds_read_b64_tr_b16 a[0:1], v21 offset:24896\n"
"v_mfma_f32_16x16x32_bf16 v[164:167], a[8:11], a[52:55], v[164:167]\n"
"buffer_load_dwordx4 a[184:187], v19, s[52:55], s30 offen offset:0 sc1\n"
"v_mfma_f32_16x16x32_bf16 v[172:175], a[8:11], a[152:155], v[172:175]\n"
"ds_read_b64_tr_b16 a[2:3], v21 offset:28992\n"
"v_mfma_f32_16x16x32_bf16 v[168:171], a[8:11], a[144:147], v[168:171]\n"
"s_waitcnt lgkmcnt(5)\n"
"ds_read_b64_tr_b16 a[4:5], v22 offset:24896\n"
"s_addk_i32 s30, 4096\n"
"v_mfma_f32_16x16x32_bf16 v[168:171], a[12:15], a[148:151], v[168:171]\n"
"ds_read_b64_tr_b16 a[6:7], v22 offset:28992\n"
"v_mfma_f32_16x16x32_bf16 v[176:179], a[16:19], v[88:91], v[176:179]\n"
"ds_read_b64_tr_b16 a[8:9], v21 offset:24928\n"
"v_mfma_f32_16x16x32_bf16 v[176:179], a[20:23], v[92:95], v[176:179]\n"
"buffer_load_dwordx4 a[188:191], v19, s[52:55], s30 offen offset:0 sc1\n"
"v_mfma_f32_16x16x32_bf16 v[180:183], a[20:23], a[56:59], v[180:183]\n"
"s_addk_i32 s30, 4096\n"
"ds_read_b64_tr_b16 a[10:11], v21 offset:29024\n"
"v_mfma_f32_16x16x32_bf16 v[180:183], a[16:19], a[52:55], v[180:183]\n"
"ds_read_b64_tr_b16 a[12:13], v22 offset:24928\n"
"v_mfma_f32_16x16x32_bf16 v[188:191], a[16:19], a[152:155], v[188:191]\n"
"ds_read_b64_tr_b16 a[14:15], v22 offset:29024\n"
"v_mfma_f32_16x16x32_bf16 v[184:187], a[16:19], a[144:147], v[184:187]\n"
"s_waitcnt lgkmcnt(5)\n"
"buffer_load_dwordx4 a[60:63], v19, s[52:55], s30 offen offset:0 sc1\n"
"v_mfma_f32_16x16x32_bf16 v[184:187], a[20:23], a[148:151], v[184:187]\n"
"v_mfma_f32_16x16x32_bf16 v[192:195], a[24:27], v[88:91], v[192:195]\n"
"s_addk_i32 s30, 4096\n"
"v_mfma_f32_16x16x32_bf16 v[192:195], a[28:31], v[92:95], v[192:195]\n"
"v_mfma_f32_16x16x32_bf16 v[196:199], a[28:31], a[56:59], v[196:199]\n"
"v_mfma_f32_16x16x32_bf16 v[196:199], a[24:27], a[52:55], v[196:199]\n"
"buffer_load_dwordx4 a[64:67], v19, s[52:55], s30 offen offset:0 sc1\n"
"v_mfma_f32_16x16x32_bf16 v[204:207], a[24:27], a[152:155], v[204:207]\n"
"s_addk_i32 s30, 4096\n"
"v_mfma_f32_16x16x32_bf16 v[200:203], a[24:27], a[144:147], v[200:203]\n"
"s_waitcnt lgkmcnt(0)\n"
"v_mfma_f32_16x16x32_bf16 v[200:203], a[28:31], a[148:151], v[200:203]\n"
"v_mfma_f32_16x16x32_bf16 v[208:211], a[0:3], v[88:91], v[208:211]\n"
"buffer_load_dwordx4 a[68:71], v19, s[52:55], s30 offen offset:0 sc1\n"
"v_mfma_f32_16x16x32_bf16 v[208:211], a[4:7], v[92:95], v[208:211]\n"
"s_waitcnt vmcnt(14)\n"
"v_mfma_f32_16x16x32_bf16 v[212:215], a[4:7], a[56:59], v[212:215]\n"
"s_barrier\n"
"v_mfma_f32_16x16x32_bf16 v[212:215], a[0:3], a[52:55], v[212:215]\n"
"ds_read_b64_tr_b16 a[16:17], v21 offset:41280\n"
"v_mfma_f32_16x16x32_bf16 v[220:223], a[0:3], a[152:155], v[220:223]\n"
"ds_read_b64_tr_b16 a[18:19], v21 offset:45376\n"
"v_mfma_f32_16x16x32_bf16 v[216:219], a[0:3], a[144:147], v[216:219]\n"
"ds_read_b64_tr_b16 a[20:21], v22 offset:41280\n"
"v_mfma_f32_16x16x32_bf16 v[216:219], a[4:7], a[148:151], v[216:219]\n"
"ds_read_b64_tr_b16 a[22:23], v22 offset:45376\n"
"v_mfma_f32_16x16x32_bf16 v[224:227], a[8:11], v[88:91], v[224:227]\n"
"ds_read_b64_tr_b16 a[24:25], v21 offset:49536\n"
"v_mfma_f32_16x16x32_bf16 v[224:227], a[12:15], v[92:95], v[224:227]\n"
"ds_read_b64_tr_b16 a[26:27], v21 offset:53632\n"
"v_mfma_f32_16x16x32_bf16 v[228:231], a[12:15], a[56:59], v[228:231]\n"
"ds_read_b64_tr_b16 a[28:29], v22 offset:49536\n"
"v_mfma_f32_16x16x32_bf16 v[228:231], a[8:11], a[52:55], v[228:231]\n"
"ds_read_b64_tr_b16 a[30:31], v22 offset:53632\n"
"v_mfma_f32_16x16x32_bf16 v[236:239], a[8:11], a[152:155], v[236:239]\n"
"ds_read_b64_tr_b16 a[0:1], v21 offset:57792\n"
"v_mfma_f32_16x16x32_bf16 v[232:235], a[8:11], a[144:147], v[232:235]\n"
"ds_read_b64_tr_b16 a[2:3], v21 offset:61888\n"
"s_waitcnt lgkmcnt(6)\n"
"v_mfma_f32_16x16x32_bf16 v[232:235], a[12:15], a[148:151], v[232:235]\n"
"ds_read_b64_tr_b16 a[4:5], v22 offset:57792\n"
"v_mfma_f32_16x16x32_bf16 v[144:147], a[16:19], a[156:159], v[144:147]\n"
"ds_read_b64_tr_b16 a[6:7], v22 offset:61888\n"
"v_mfma_f32_16x16x32_bf16 v[144:147], a[20:23], a[160:163], v[144:147]\n"
"ds_read_b64_tr_b16 a[8:9], v21 offset:41312\n"
"v_mfma_f32_16x16x32_bf16 v[148:151], a[20:23], a[168:171], v[148:151]\n"
"ds_read_b64_tr_b16 a[10:11], v21 offset:45408\n"
"v_mfma_f32_16x16x32_bf16 v[148:151], a[16:19], a[164:167], v[148:151]\n"
"ds_read_b64_tr_b16 a[12:13], v22 offset:41312\n"
"v_mfma_f32_16x16x32_bf16 v[156:159], a[16:19], v[108:111], v[156:159]\n"
"ds_read_b64_tr_b16 a[14:15], v22 offset:45408\n"
"v_mfma_f32_16x16x32_bf16 v[152:155], a[16:19], a[172:175], v[152:155]\n"
"s_waitcnt vmcnt(5)\n"
"ds_read_b64_tr_b16 a[16:17], v21 offset:49568\n"
"s_waitcnt lgkmcnt(7)\n"
"v_mfma_f32_16x16x32_bf16 v[152:155], a[20:23], a[176:179], v[152:155]\n"
"ds_read_b64_tr_b16 a[18:19], v21 offset:53664\n"
"v_mfma_f32_16x16x32_bf16 v[144:147], a[24:27], v[112:115], v[144:147]\n"
"ds_read_b64_tr_b16 a[20:21], v22 offset:49568\n"
"v_mfma_f32_16x16x32_bf16 v[144:147], a[28:31], v[116:119], v[144:147]\n"
"ds_read_b64_tr_b16 a[22:23], v22 offset:53664\n"
"v_mfma_f32_16x16x32_bf16 v[148:151], a[28:31], v[124:127], v[148:151]\n"
"v_mfma_f32_16x16x32_bf16 v[148:151], a[24:27], v[120:123], v[148:151]\n"
"v_mfma_f32_16x16x32_bf16 v[156:159], a[24:27], v[136:139], v[156:159]\n"
"v_mfma_f32_16x16x32_bf16 v[152:155], a[24:27], v[128:131], v[152:155]\n"
"s_waitcnt vmcnt(0)\n"
"ds_read_b64_tr_b16 a[24:25], v21 offset:57824\n"
"s_waitcnt lgkmcnt(4)\n"
"v_mfma_f32_16x16x32_bf16 v[152:155], a[28:31], v[132:135], v[152:155]\n"
"ds_read_b64_tr_b16 a[26:27], v21 offset:61920\n"
"v_mfma_f32_16x16x32_bf16 v[144:147], a[0:3], v[140:143], v[144:147]\n"
"ds_read_b64_tr_b16 a[28:29], v22 offset:57824\n"
"v_mfma_f32_16x16x32_bf16 v[144:147], a[4:7], a[180:183], v[144:147]\n"
"ds_read_b64_tr_b16 a[30:31], v22 offset:61920\n"
"v_mfma_f32_16x16x32_bf16 v[148:151], a[4:7], a[188:191], v[148:151]\n"
"PG15_%=:\n"
"s_cbranch_execz PGX%=\n"
"v_mfma_f32_16x16x32_bf16 v[148:151], a[0:3], a[184:187], v[148:151]\n"
"v_mfma_f32_16x16x32_bf16 v[156:159], a[0:3], a[68:71], v[156:159]\n"
"v_mfma_f32_16x16x32_bf16 v[152:155], a[0:3], a[60:63], v[152:155]\n"
"ds_read_b64_tr_b16 a[0:1], v21 offset:41344\n"
"v_mfma_f32_16x16x32_bf16 v[152:155], a[4:7], a[64:67], v[152:155]\n"
"ds_read_b64_tr_b16 a[2:3], v21 offset:45440\n"
"v_cvt_pk_bf16_f32 v144, v144, v145\n"
"v_mfma_f32_16x16x32_bf16 v[160:163], a[8:11], a[156:159], v[160:163]\n"
"ds_read_b64_tr_b16 a[4:5], v22 offset:41344\n"
"v_cvt_pk_bf16_f32 v145, v146, v147\n"
"v_mfma_f32_16x16x32_bf16 v[160:163], a[12:15], a[160:163], v[160:163]\n"
"ds_read_b64_tr_b16 a[6:7], v22 offset:45440\n"
"v_cvt_pk_bf16_f32 v146, v148, v149\n"
"v_mfma_f32_16x16x32_bf16 v[164:167], a[12:15], a[168:171], v[164:167]\n"
"v_cvt_pk_bf16_f32 v147, v150, v151\n"
"v_cvt_pk_bf16_f32 v152, v152, v153\n"
"v_mfma_f32_16x16x32_bf16 v[164:167], a[8:11], a[164:167], v[164:167]\n"
"buffer_store_dwordx4 v[144:147], v2, s[52:55], s93 offen offset:0 sc1\n"
"v_mfma_f32_16x16x32_bf16 v[172:175], a[8:11], v[108:111], v[172:175]\n"
"v_cvt_pk_bf16_f32 v153, v154, v155\n"
"v_cvt_pk_bf16_f32 v154, v156, v157\n"
"v_mfma_f32_16x16x32_bf16 v[168:171], a[8:11], a[172:175], v[168:171]\n"
"s_waitcnt lgkmcnt(3)\n"
"ds_read_b64_tr_b16 a[8:9], v21 offset:49600\n"
"v_cvt_pk_bf16_f32 v155, v158, v159\n"
"v_mfma_f32_16x16x32_bf16 v[168:171], a[12:15], a[176:179], v[168:171]\n"
"buffer_store_dwordx4 v[152:155], v2, s[52:55], s93 offen offset:1024 sc1\n"
"v_mfma_f32_16x16x32_bf16 v[160:163], a[16:19], v[112:115], v[160:163]\n"
"s_addk_i32 s93, 2048\n"
"ds_read_b64_tr_b16 a[10:11], v21 offset:53696\n"
"v_mfma_f32_16x16x32_bf16 v[160:163], a[20:23], v[116:119], v[160:163]\n"
"ds_read_b64_tr_b16 a[12:13], v22 offset:49600\n"
"v_mfma_f32_16x16x32_bf16 v[164:167], a[20:23], v[124:127], v[164:167]\n"
"ds_read_b64_tr_b16 a[14:15], v22 offset:53696\n"
"v_mfma_f32_16x16x32_bf16 v[164:167], a[16:19], v[120:123], v[164:167]\n"
"v_mfma_f32_16x16x32_bf16 v[172:175], a[16:19], v[136:139], v[172:175]\n"
"v_mfma_f32_16x16x32_bf16 v[168:171], a[16:19], v[128:131], v[168:171]\n"
"ds_read_b64_tr_b16 a[16:17], v21 offset:57856\n"
"v_mfma_f32_16x16x32_bf16 v[168:171], a[20:23], v[132:135], v[168:171]\n"
"ds_read_b64_tr_b16 a[18:19], v21 offset:61952\n"
"v_mfma_f32_16x16x32_bf16 v[160:163], a[24:27], v[140:143], v[160:163]\n"
"ds_read_b64_tr_b16 a[20:21], v22 offset:57856\n"
"v_mfma_f32_16x16x32_bf16 v[160:163], a[28:31], a[180:183], v[160:163]\n"
"ds_read_b64_tr_b16 a[22:23], v22 offset:61952\n"
"v_mfma_f32_16x16x32_bf16 v[164:167], a[28:31], a[188:191], v[164:167]\n"
"v_mfma_f32_16x16x32_bf16 v[164:167], a[24:27], a[184:187], v[164:167]\n"
"v_mfma_f32_16x16x32_bf16 v[172:175], a[24:27], a[68:71], v[172:175]\n"
"v_mfma_f32_16x16x32_bf16 v[168:171], a[24:27], a[60:63], v[168:171]\n"
"s_waitcnt lgkmcnt(3)\n"
"ds_read_b64_tr_b16 a[24:25], v21 offset:41376\n"
"v_mfma_f32_16x16x32_bf16 v[168:171], a[28:31], a[64:67], v[168:171]\n"
"ds_read_b64_tr_b16 a[26:27], v21 offset:45472\n"
"v_cvt_pk_bf16_f32 v160, v160, v161\n"
"v_mfma_f32_16x16x32_bf16 v[176:179], a[0:3], a[156:159], v[176:179]\n"
"ds_read_b64_tr_b16 a[28:29], v22 offset:41376\n"
"v_cvt_pk_bf16_f32 v161, v162, v163\n"
"v_mfma_f32_16x16x32_bf16 v[176:179], a[4:7], a[160:163], v[176:179]\n"
"ds_read_b64_tr_b16 a[30:31], v22 offset:45472\n"
"v_cvt_pk_bf16_f32 v162, v164, v165\n"
"v_mfma_f32_16x16x32_bf16 v[180:183], a[4:7], a[168:171], v[180:183]\n"
"v_cvt_pk_bf16_f32 v163, v166, v167\n"
"v_cvt_pk_bf16_f32 v168, v168, v169\n"
"v_mfma_f32_16x16x32_bf16 v[180:183], a[0:3], a[164:167], v[180:183]\n"
"buffer_store_dwordx4 v[160:163], v2, s[52:55], s93 offen offset:0 sc1\n"
"v_mfma_f32_16x16x32_bf16 v[188:191], a[0:3], v[108:111], v[188:191]\n"
"v_cvt_pk_bf16_f32 v169, v170, v171\n"
"v_cvt_pk_bf16_f32 v170, v172, v173\n"
"v_mfma_f32_16x16x32_bf16 v[184:187], a[0:3], a[172:175], v[184:187]\n"
"ds_read_b64_tr_b16 a[0:1], v21 offset:49632\n"
"v_cvt_pk_bf16_f32 v171, v174, v175\n"
"v_mfma_f32_16x16x32_bf16 v[184:187], a[4:7], a[176:179], v[184:187]\n"
"buffer_store_dwordx4 v[168:171], v2, s[52:55], s93 offen offset:1024 sc1\n"
"v_mfma_f32_16x16x32_bf16 v[176:179], a[8:11], v[112:115], v[176:179]\n"
"s_addk_i32 s93, 2048\n"
"ds_read_b64_tr_b16 a[2:3], v21 offset:53728\n"
"v_mfma_f32_16x16x32_bf16 v[176:179], a[12:15], v[116:119], v[176:179]\n"
"ds_read_b64_tr_b16 a[4:5], v22 offset:49632\n"
"v_mfma_f32_16x16x32_bf16 v[180:183], a[12:15], v[124:127], v[180:183]\n"
"ds_read_b64_tr_b16 a[6:7], v22 offset:53728\n"
"v_mfma_f32_16x16x32_bf16 v[180:183], a[8:11], v[120:123], v[180:183]\n"
"v_mfma_f32_16x16x32_bf16 v[188:191], a[8:11], v[136:139], v[188:191]\n"
"v_mfma_f32_16x16x32_bf16 v[184:187], a[8:11], v[128:131], v[184:187]\n"
"s_waitcnt lgkmcnt(3)\n"
"ds_read_b64_tr_b16 a[8:9], v21 offset:57888\n"
"v_mfma_f32_16x16x32_bf16 v[184:187], a[12:15], v[132:135], v[184:187]\n"
"ds_read_b64_tr_b16 a[10:11], v21 offset:61984\n"
"v_mfma_f32_16x16x32_bf16 v[176:179], a[16:19], v[140:143], v[176:179]\n"
"ds_read_b64_tr_b16 a[12:13], v22 offset:57888\n"
"v_mfma_f32_16x16x32_bf16 v[176:179], a[20:23], a[180:183], v[176:179]\n"
"ds_read_b64_tr_b16 a[14:15], v22 offset:61984\n"
"v_mfma_f32_16x16x32_bf16 v[180:183], a[20:23], a[188:191], v[180:183]\n"
"v_mfma_f32_16x16x32_bf16 v[180:183], a[16:19], a[184:187], v[180:183]\n"
"v_mfma_f32_16x16x32_bf16 v[188:191], a[16:19], a[68:71], v[188:191]\n"
"v_mfma_f32_16x16x32_bf16 v[184:187], a[16:19], a[60:63], v[184:187]\n"
"ds_read_b64_tr_b16 a[16:17], v21 offset:41408\n"
"v_mfma_f32_16x16x32_bf16 v[184:187], a[20:23], a[64:67], v[184:187]\n"
"ds_read_b64_tr_b16 a[18:19], v21 offset:45504\n"
"v_cvt_pk_bf16_f32 v176, v176, v177\n"
"v_mfma_f32_16x16x32_bf16 v[192:195], a[24:27], a[156:159], v[192:195]\n"
"ds_read_b64_tr_b16 a[20:21], v22 offset:41408\n"
"v_cvt_pk_bf16_f32 v177, v178, v179\n"
"v_mfma_f32_16x16x32_bf16 v[192:195], a[28:31], a[160:163], v[192:195]\n"
"ds_read_b64_tr_b16 a[22:23], v22 offset:45504\n"
"v_cvt_pk_bf16_f32 v178, v180, v181\n"
"v_mfma_f32_16x16x32_bf16 v[196:199], a[28:31], a[168:171], v[196:199]\n"
"v_cvt_pk_bf16_f32 v179, v182, v183\n"
"v_cvt_pk_bf16_f32 v184, v184, v185\n"
"v_mfma_f32_16x16x32_bf16 v[196:199], a[24:27], a[164:167], v[196:199]\n"
"buffer_store_dwordx4 v[176:179], v2, s[52:55], s93 offen offset:0 sc1\n"
"v_mfma_f32_16x16x32_bf16 v[204:207], a[24:27], v[108:111], v[204:207]\n"
"v_cvt_pk_bf16_f32 v185, v186, v187\n"
"v_cvt_pk_bf16_f32 v186, v188, v189\n"
"v_mfma_f32_16x16x32_bf16 v[200:203], a[24:27], a[172:175], v[200:203]\n"
"s_waitcnt lgkmcnt(3)\n"
"ds_read_b64_tr_b16 a[24:25], v21 offset:49664\n"
"v_cvt_pk_bf16_f32 v187, v190, v191\n"
"v_mfma_f32_16x16x32_bf16 v[200:203], a[28:31], a[176:179], v[200:203]\n"
"buffer_store_dwordx4 v[184:187], v2, s[52:55], s93 offen offset:1024 sc1\n"
"v_mfma_f32_16x16x32_bf16 v[192:195], a[0:3], v[112:115], v[192:195]\n"
"s_addk_i32 s93, 2048\n"
"ds_read_b64_tr_b16 a[26:27], v21 offset:53760\n"
"v_mfma_f32_16x16x32_bf16 v[192:195], a[4:7], v[116:119], v[192:195]\n"
"ds_read_b64_tr_b16 a[28:29], v22 offset:49664\n"
"v_mfma_f32_16x16x32_bf16 v[196:199], a[4:7], v[124:127], v[196:199]\n"
"ds_read_b64_tr_b16 a[30:31], v22 offset:53760\n"
"v_mfma_f32_16x16x32_bf16 v[196:199], a[0:3], v[120:123], v[196:199]\n"
"v_mfma_f32_16x16x32_bf16 v[204:207], a[0:3], v[136:139], v[204:207]\n"
"v_mfma_f32_16x16x32_bf16 v[200:203], a[0:3], v[128:131], v[200:203]\n"
"ds_read_b64_tr_b16 a[0:1], v21 offset:57920\n"
"v_mfma_f32_16x16x32_bf16 v[200:203], a[4:7], v[132:135], v[200:203]\n"
"ds_read_b64_tr_b16 a[2:3], v21 offset:62016\n"
"v_mfma_f32_16x16x32_bf16 v[192:195], a[8:11], v[140:143], v[192:195]\n"
"ds_read_b64_tr_b16 a[4:5], v22 offset:57920\n"
"v_mfma_f32_16x16x32_bf16 v[192:195], a[12:15], a[180:183], v[192:195]\n"
"ds_read_b64_tr_b16 a[6:7], v22 offset:62016\n"
"v_mfma_f32_16x16x32_bf16 v[196:199], a[12:15], a[188:191], v[196:199]\n"
"v_mfma_f32_16x16x32_bf16 v[196:199], a[8:11], a[184:187], v[196:199]\n"
"v_mfma_f32_16x16x32_bf16 v[204:207], a[8:11], a[68:71], v[204:207]\n"
"v_mfma_f32_16x16x32_bf16 v[200:203], a[8:11], a[60:63], v[200:203]\n"
"s_waitcnt lgkmcnt(3)\n"
"ds_read_b64_tr_b16 a[8:9], v21 offset:41440\n"
"v_mfma_f32_16x16x32_bf16 v[200:203], a[12:15], a[64:67], v[200:203]\n"
"ds_read_b64_tr_b16 a[10:11], v21 offset:45536\n"
"v_cvt_pk_bf16_f32 v192, v192, v193\n"
"v_mfma_f32_16x16x32_bf16 v[208:211], a[16:19], a[156:159], v[208:211]\n"
"ds_read_b64_tr_b16 a[12:13], v22 offset:41440\n"
"v_cvt_pk_bf16_f32 v193, v194, v195\n"
"v_mfma_f32_16x16x32_bf16 v[208:211], a[20:23], a[160:163], v[208:211]\n"
"ds_read_b64_tr_b16 a[14:15], v22 offset:45536\n"
"v_cvt_pk_bf16_f32 v194, v196, v197\n"
"v_mfma_f32_16x16x32_bf16 v[212:215], a[20:23], a[168:171], v[212:215]\n"
"v_cvt_pk_bf16_f32 v195, v198, v199\n"
"v_cvt_pk_bf16_f32 v200, v200, v201\n"
"v_mfma_f32_16x16x32_bf16 v[212:215], a[16:19], a[164:167], v[212:215]\n"
"buffer_store_dwordx4 v[192:195], v2, s[52:55], s93 offen offset:0 sc1\n"
"v_mfma_f32_16x16x32_bf16 v[220:223], a[16:19], v[108:111], v[220:223]\n"
"v_cvt_pk_bf16_f32 v201, v202, v203\n"
"v_cvt_pk_bf16_f32 v202, v204, v205\n"
"v_mfma_f32_16x16x32_bf16 v[216:219], a[16:19], a[172:175], v[216:219]\n"
"ds_read_b64_tr_b16 a[16:17], v21 offset:49696\n"
"v_cvt_pk_bf16_f32 v203, v206, v207\n"
"v_mfma_f32_16x16x32_bf16 v[216:219], a[20:23], a[176:179], v[216:219]\n"
"buffer_store_dwordx4 v[200:203], v2, s[52:55], s93 offen offset:1024 sc1\n"
"v_mfma_f32_16x16x32_bf16 v[208:211], a[24:27], v[112:115], v[208:211]\n"
"s_addk_i32 s93, 2048\n"
"ds_read_b64_tr_b16 a[18:19], v21 offset:53792\n"
"v_mfma_f32_16x16x32_bf16 v[208:211], a[28:31], v[116:119], v[208:211]\n"
"ds_read_b64_tr_b16 a[20:21], v22 offset:49696\n"
"v_mfma_f32_16x16x32_bf16 v[212:215], a[28:31], v[124:127], v[212:215]\n"
"ds_read_b64_tr_b16 a[22:23], v22 offset:53792\n"
"v_mfma_f32_16x16x32_bf16 v[212:215], a[24:27], v[120:123], v[212:215]\n"
"v_mfma_f32_16x16x32_bf16 v[220:223], a[24:27], v[136:139], v[220:223]\n"
"v_mfma_f32_16x16x32_bf16 v[216:219], a[24:27], v[128:131], v[216:219]\n"
"s_waitcnt lgkmcnt(3)\n"
"ds_read_b64_tr_b16 a[24:25], v21 offset:57952\n"
"v_mfma_f32_16x16x32_bf16 v[216:219], a[28:31], v[132:135], v[216:219]\n"
"ds_read_b64_tr_b16 a[26:27], v21 offset:62048\n"
"v_mfma_f32_16x16x32_bf16 v[208:211], a[0:3], v[140:143], v[208:211]\n"
"ds_read_b64_tr_b16 a[28:29], v22 offset:57952\n"
"v_mfma_f32_16x16x32_bf16 v[208:211], a[4:7], a[180:183], v[208:211]\n"
"ds_read_b64_tr_b16 a[30:31], v22 offset:62048\n"
"v_mfma_f32_16x16x32_bf16 v[212:215], a[4:7], a[188:191], v[212:215]\n"
"v_mfma_f32_16x16x32_bf16 v[212:215], a[0:3], a[184:187], v[212:215]\n"
"v_mfma_f32_16x16x32_bf16 v[220:223], a[0:3], a[68:71], v[220:223]\n"
"v_mfma_f32_16x16x32_bf16 v[216:219], a[0:3], a[60:63], v[216:219]\n"
"ds_read_b64_tr_b16 a[0:1], v21 offset:33216\n"
"v_mfma_f32_16x16x32_bf16 v[216:219], a[4:7], a[64:67], v[216:219]\n"
"ds_read_b64_tr_b16 a[2:3], v21 offset:37312\n"
"v_cvt_pk_bf16_f32 v208, v208, v209\n"
"v_mfma_f32_16x16x32_bf16 v[224:227], a[8:11], a[156:159], v[224:227]\n"
"ds_read_b64_tr_b16 a[4:5], v22 offset:33216\n"
"v_cvt_pk_bf16_f32 v209, v210, v211\n"
"v_mfma_f32_16x16x32_bf16 v[224:227], a[12:15], a[160:163], v[224:227]\n"
"ds_read_b64_tr_b16 a[6:7], v22 offset:37312\n"
"v_cvt_pk_bf16_f32 v210, v212, v213\n"
"v_mfma_f32_16x16x32_bf16 v[228:231], a[12:15], a[168:171], v[228:231]\n"
"v_cvt_pk_bf16_f32 v211, v214, v215\n"
"v_cvt_pk_bf16_f32 v216, v216, v217\n"
"v_mfma_f32_16x16x32_bf16 v[228:231], a[8:11], a[164:167], v[228:231]\n"
"buffer_store_dwordx4 v[208:211], v2, s[52:55], s93 offen offset:0 sc1\n"
"v_mfma_f32_16x16x32_bf16 v[236:239], a[8:11], v[108:111], v[236:239]\n"
"v_cvt_pk_bf16_f32 v217, v218, v219\n"
"v_cvt_pk_bf16_f32 v218, v220, v221\n"
"v_mfma_f32_16x16x32_bf16 v[232:235], a[8:11], a[172:175], v[232:235]\n"
"s_waitcnt lgkmcnt(3)\n"
"ds_read_b64_tr_b16 a[8:9], v21 offset:192\n"
"v_cvt_pk_bf16_f32 v219, v222, v223\n"
"v_mfma_f32_16x16x32_bf16 v[232:235], a[12:15], a[176:179], v[232:235]\n"
"buffer_store_dwordx4 v[216:219], v2, s[52:55], s93 offen offset:1024 sc1\n"
"v_mfma_f32_16x16x32_bf16 v[224:227], a[16:19], v[112:115], v[224:227]\n"
"s_addk_i32 s93, 2048\n"
"ds_read_b64_tr_b16 a[10:11], v21 offset:4288\n"
"s_add_u32 s93, s93, s25\n"
"v_mfma_f32_16x16x32_bf16 v[224:227], a[20:23], v[116:119], v[224:227]\n"
"ds_read_b64_tr_b16 a[12:13], v22 offset:192\n"
"v_mfma_f32_16x16x32_bf16 v[228:231], a[20:23], v[124:127], v[228:231]\n"
"ds_read_b64_tr_b16 a[14:15], v22 offset:4288\n"
"v_mfma_f32_16x16x32_bf16 v[228:231], a[16:19], v[120:123], v[228:231]\n"
"v_mfma_f32_16x16x32_bf16 v[236:239], a[16:19], v[136:139], v[236:239]\n"
"v_mfma_f32_16x16x32_bf16 v[232:235], a[16:19], v[128:131], v[232:235]\n"
"ds_read_b64_tr_b16 a[16:17], v21 offset:8448\n"
"v_mfma_f32_16x16x32_bf16 v[232:235], a[20:23], v[132:135], v[232:235]\n"
"ds_read_b64_tr_b16 a[18:19], v21 offset:12544\n"
"v_mfma_f32_16x16x32_bf16 v[224:227], a[24:27], v[140:143], v[224:227]\n"
"ds_read_b64_tr_b16 a[20:21], v22 offset:8448\n"
"v_mfma_f32_16x16x32_bf16 v[224:227], a[28:31], a[180:183], v[224:227]\n"
"ds_read_b64_tr_b16 a[22:23], v22 offset:12544\n"
"v_mfma_f32_16x16x32_bf16 v[228:231], a[28:31], a[188:191], v[228:231]\n"
"v_mfma_f32_16x16x32_bf16 v[228:231], a[24:27], a[184:187], v[228:231]\n"
"v_mfma_f32_16x16x32_bf16 v[236:239], a[24:27], a[68:71], v[236:239]\n"
"v_mfma_f32_16x16x32_bf16 v[232:235], a[24:27], a[60:63], v[232:235]\n"
"s_waitcnt lgkmcnt(3)\n"
"ds_read_b64_tr_b16 a[24:25], v21 offset:16704\n"
"v_mfma_f32_16x16x32_bf16 v[232:235], a[28:31], a[64:67], v[232:235]\n"
"ds_read_b64_tr_b16 a[26:27], v21 offset:20800\n"
"v_cvt_pk_bf16_f32 v96, v224, v225\n"
"v_mfma_f32_16x16x32_bf16 v[240:243], a[0:3], a[220:223], v[240:243]\n"
"ds_read_b64_tr_b16 a[28:29], v22 offset:16704\n"
"v_cvt_pk_bf16_f32 v97, v226, v227\n"
"v_mfma_f32_16x16x32_bf16 v[240:243], a[4:7], a[224:227], v[240:243]\n"
"ds_read_b64_tr_b16 a[30:31], v22 offset:20800\n"
"v_cvt_pk_bf16_f32 v98, v228, v229\n"
"v_mfma_f32_16x16x32_bf16 v[244:247], a[4:7], a[232:235], v[244:247]\n"
"v_cvt_pk_bf16_f32 v99, v230, v231\n"
"v_cvt_pk_bf16_f32 v100, v232, v233\n"
"v_mfma_f32_16x16x32_bf16 v[244:247], a[0:3], a[228:231], v[244:247]\n"
"buffer_store_dwordx4 v[96:99], v2, s[52:55], s93 offen offset:0 sc1\n"
"v_mfma_f32_16x16x32_bf16 v[252:255], a[0:3], a[244:247], v[252:255]\n"
"v_cvt_pk_bf16_f32 v101, v234, v235\n"
"v_cvt_pk_bf16_f32 v102, v236, v237\n"
"v_mfma_f32_16x16x32_bf16 v[248:251], a[0:3], a[236:239], v[248:251]\n"
"ds_read_b64_tr_b16 a[0:1], v21 offset:24960\n"
"v_cvt_pk_bf16_f32 v103, v238, v239\n"
"v_mfma_f32_16x16x32_bf16 v[248:251], a[4:7], a[240:243], v[248:251]\n"
"buffer_store_dwordx4 v[100:103], v2, s[52:55], s93 offen offset:1024 sc1\n"
"v_mfma_f32_16x16x32_bf16 v[240:243], a[8:11], a[248:251], v[240:243]\n"
"ds_read_b64_tr_b16 a[2:3], v21 offset:29056\n"
"v_mfma_f32_16x16x32_bf16 v[240:243], a[12:15], a[252:255], v[240:243]\n"
"ds_read_b64_tr_b16 a[4:5], v22 offset:24960\n"
"v_mfma_f32_16x16x32_bf16 v[244:247], a[12:15], a[36:39], v[244:247]\n"
"ds_read_b64_tr_b16 a[6:7], v22 offset:29056\n"
"v_mfma_f32_16x16x32_bf16 v[244:247], a[8:11], a[32:35], v[244:247]\n"
"s_waitcnt vmcnt(8)\n"
"buffer_store_dwordx2 v[30:31], v29, s[52:55], s73 offen sc1\n"
"v_mfma_f32_16x16x32_bf16 v[252:255], a[8:11], a[48:51], v[252:255]\n"
"v_mfma_f32_16x16x32_bf16 v[248:251], a[8:11], a[40:43], v[248:251]\n"
"s_waitcnt lgkmcnt(3)\n"
"ds_read_b64_tr_b16 a[8:9], v21 offset:41472\n"
"v_mfma_f32_16x16x32_bf16 v[248:251], a[12:15], a[44:47], v[248:251]\n"
"ds_read_b64_tr_b16 a[10:11], v21 offset:45568\n"
"v_mfma_f32_16x16x32_bf16 v[240:243], a[16:19], v[32:35], v[240:243]\n"
"ds_read_b64_tr_b16 a[12:13], v22 offset:41472\n"
"v_mfma_f32_16x16x32_bf16 v[240:243], a[20:23], v[36:39], v[240:243]\n"
"ds_read_b64_tr_b16 a[14:15], v22 offset:45568\n"
"v_mfma_f32_16x16x32_bf16 v[244:247], a[20:23], v[44:47], v[244:247]\n"
"v_mfma_f32_16x16x32_bf16 v[244:247], a[16:19], v[40:43], v[244:247]\n"
"v_mfma_f32_16x16x32_bf16 v[252:255], a[16:19], v[56:59], v[252:255]\n"
"v_mfma_f32_16x16x32_bf16 v[248:251], a[16:19], v[48:51], v[248:251]\n"
"ds_read_b64_tr_b16 a[16:17], v21 offset:49728\n"
"v_mfma_f32_16x16x32_bf16 v[248:251], a[20:23], v[52:55], v[248:251]\n"
"ds_read_b64_tr_b16 a[18:19], v21 offset:53824\n"
"v_mfma_f32_16x16x32_bf16 v[240:243], a[24:27], v[60:63], v[240:243]\n"
"ds_read_b64_tr_b16 a[20:21], v22 offset:49728\n"
"v_mfma_f32_16x16x32_bf16 v[240:243], a[28:31], v[64:67], v[240:243]\n"
"ds_read_b64_tr_b16 a[22:23], v22 offset:53824\n"
"v_mfma_f32_16x16x32_bf16 v[244:247], a[28:31], v[72:75], v[244:247]\n"
"v_mfma_f32_16x16x32_bf16 v[244:247], a[24:27], v[68:71], v[244:247]\n"
"v_mfma_f32_16x16x32_bf16 v[252:255], a[24:27], v[84:87], v[252:255]\n"
"v_mfma_f32_16x16x32_bf16 v[248:251], a[24:27], v[76:79], v[248:251]\n"
"s_waitcnt lgkmcnt(3)\n"
"ds_read_b64_tr_b16 a[24:25], v21 offset:57984\n"
"v_mfma_f32_16x16x32_bf16 v[248:251], a[28:31], v[80:83], v[248:251]\n"
"ds_read_b64_tr_b16 a[26:27], v21 offset:62080\n"
"v_mfma_f32_16x16x32_bf16 v[240:243], a[0:3], v[88:91], v[240:243]\n"
"ds_read_b64_tr_b16 a[28:29], v22 offset:57984\n"
"v_mfma_f32_16x16x32_bf16 v[240:243], a[4:7], v[92:95], v[240:243]\n"
"ds_read_b64_tr_b16 a[30:31], v22 offset:62080\n"
"v_mfma_f32_16x16x32_bf16 v[244:247], a[4:7], a[56:59], v[244:247]\n"
"v_mfma_f32_16x16x32_bf16 v[244:247], a[0:3], a[52:55], v[244:247]\n"
"v_mfma_f32_16x16x32_bf16 v[252:255], a[0:3], a[152:155], v[252:255]\n"
"v_mfma_f32_16x16x32_bf16 v[248:251], a[0:3], a[144:147], v[248:251]\n"
"v_mfma_f32_16x16x32_bf16 v[248:251], a[4:7], a[148:151], v[248:251]\n"
"v_mfma_f32_16x16x32_bf16 v[240:243], a[8:11], a[156:159], v[240:243]\n"
"v_mfma_f32_16x16x32_bf16 v[240:243], a[12:15], a[160:163], v[240:243]\n"
"v_mfma_f32_16x16x32_bf16 v[244:247], a[12:15], a[168:171], v[244:247]\n"
"v_mfma_f32_16x16x32_bf16 v[244:247], a[8:11], a[164:167], v[244:247]\n"
"v_mfma_f32_16x16x32_bf16 v[252:255], a[8:11], v[108:111], v[252:255]\n"
"v_mfma_f32_16x16x32_bf16 v[248:251], a[8:11], a[172:175], v[248:251]\n"
"s_waitcnt lgkmcnt(0)\n"
"v_mfma_f32_16x16x32_bf16 v[248:251], a[12:15], a[176:179], v[248:251]\n"
"v_mfma_f32_16x16x32_bf16 v[240:243], a[16:19], v[112:115], v[240:243]\n"
"s_waitcnt vmcnt(5)\n"
"buffer_store_dwordx2 v[30:31], v29, s[52:55], s75 offen sc1\n"
"v_mfma_f32_16x16x32_bf16 v[240:243], a[20:23], v[116:119], v[240:243]\n"
"v_mfma_f32_16x16x32_bf16 v[244:247], a[20:23], v[124:127], v[244:247]\n"
"v_mfma_f32_16x16x32_bf16 v[244:247], a[16:19], v[120:123], v[244:247]\n"
"v_mfma_f32_16x16x32_bf16 v[252:255], a[16:19], v[136:139], v[252:255]\n"
"v_mfma_f32_16x16x32_bf16 v[248:251], a[16:19], v[128:131], v[248:251]\n"
"v_mfma_f32_16x16x32_bf16 v[248:251], a[20:23], v[132:135], v[248:251]\n"
"s_buffer_load_dwordx8 s[44:51], s[52:55], s24 glc\n"
"v_mfma_f32_16x16x32_bf16 v[240:243], a[24:27], v[140:143], v[240:243]\n"
"s_mov_b32 s10, 0\n"
"s_mov_b32 s12, -64\n"
"v_mfma_f32_16x16x32_bf16 v[240:243], a[28:31], a[180:183], v[240:243]\n"
"v_mfma_f32_16x16x32_bf16 v[244:247], a[28:31], a[188:191], v[244:247]\n"
"v_mfma_f32_16x16x32_bf16 v[244:247], a[24:27], a[184:187], v[244:247]\n"
"v_mfma_f32_16x16x32_bf16 v[252:255], a[24:27], a[68:71], v[252:255]\n"
"s_waitcnt vmcnt(2)\n"
"buffer_store_dwordx2 v[30:31], v29, s[52:55], s77 offen sc1\n"
"v_mfma_f32_16x16x32_bf16 v[248:251], a[24:27], a[60:63], v[248:251]\n"
"v_mfma_f32_16x16x32_bf16 v[248:251], a[28:31], a[64:67], v[248:251]\n"
"s_waitcnt lgkmcnt(0)\n"
"TC%=:\n"
"s_cmp_lg_u64 s[44:45], s[4:5]\n"
"s_cbranch_scc1 TS1_%=\n"
"buffer_load_dwordx4 v[144:147], v26, s[52:55], s71 offen offset:0 sc1\n"
"buffer_load_dwordx4 v[148:151], v26, s[52:55], s71 offen offset:1024 sc1\n"
"buffer_load_dwordx4 v[152:155], v2, s[52:55], s71 offen offset:2048 sc1\n"
"buffer_load_dwordx4 v[156:159], v2, s[52:55], s71 offen offset:3072 sc1\n"
"s_not_b32 s4, s4\n"
"s_add_u32 s10, s10, 1\n"
"s_mov_b32 s11, 1\n"
"TS1_%=:\n"
"s_cmp_lg_u64 s[46:47], s[6:7]\n"
"s_cbranch_scc1 TS2_%=\n"
"buffer_load_dwordx4 v[160:163], v26, s[52:55], s72 offen offset:0 sc1\n"
"buffer_load_dwordx4 v[164:167], v26, s[52:55], s72 offen offset:1024 sc1\n"
"buffer_load_dwordx4 v[168:171], v2, s[52:55], s72 offen offset:2048 sc1\n"
"buffer_load_dwordx4 v[172:175], v2, s[52:55], s72 offen offset:3072 sc1\n"
"s_not_b32 s6, s6\n"
"s_add_u32 s10, s10, 1\n"
"s_mov_b32 s11, 2\n"
"TS2_%=:\n"
"s_cmp_lg_u64 s[48:49], s[8:9]\n"
"s_cbranch_scc1 TS3_%=\n"
"buffer_load_dwordx4 v[176:179], v26, s[52:55], s94 offen offset:0 sc1\n"
"buffer_load_dwordx4 v[180:183], v26, s[52:55], s94 offen offset:1024 sc1\n"
"buffer_load_dwordx4 v[184:187], v2, s[52:55], s94 offen offset:2048 sc1\n"
"buffer_load_dwordx4 v[188:191], v2, s[52:55], s94 offen offset:3072 sc1\n"
"s_not_b32 s8, s8\n"
"s_add_u32 s10, s10, 1\n"
"s_mov_b32 s11, 3\n"
"TS3_%=:\n"
"s_cmp_lt_u32 s10, 3\n"
"s_cbranch_scc1 TP%=\n"
"TX%=:\n"
"s_cmp_eq_u32 s11, 3\n"
"s_cbranch_scc1 TW%=\n"
"s_waitcnt vmcnt(0)\n"
"TW%=:\n"
"s_waitcnt vmcnt(4)\n"
"v_mfma_f32_16x16x16_bf16 v[236:239], a[192:193], v[150:151], v[236:239]\n"
"v_mfma_f32_16x16x16_bf16 v[232:235], a[192:193], v[148:149], v[232:235]\n"
"v_mfma_f32_16x16x16_bf16 v[228:231], a[192:193], v[146:147], v[228:231]\n"
"v_mfma_f32_16x16x16_bf16 v[224:227], a[192:193], v[144:145], v[224:227]\n"
"v_mfma_f32_16x16x16_bf16 v[252:255], a[192:193], v[158:159], v[252:255]\n"
"v_mfma_f32_16x16x16_bf16 v[248:251], a[192:193], v[156:157], v[248:251]\n"
"v_mfma_f32_16x16x16_bf16 v[244:247], a[192:193], v[154:155], v[244:247]\n"
"v_mfma_f32_16x16x16_bf16 v[240:243], a[192:193], v[152:153], v[240:243]\n"
"v_mfma_f32_16x16x16_bf16 v[236:239], a[192:193], v[166:167], v[236:239]\n"
"v_mfma_f32_16x16x16_bf16 v[232:235], a[192:193], v[164:165], v[232:235]\n"
"v_mfma_f32_16x16x16_bf16 v[228:231], a[192:193], v[162:163], v[228:231]\n"
"v_mfma_f32_16x16x16_bf16 v[224:227], a[192:193], v[160:161], v[224:227]\n"
"v_mfma_f32_16x16x16_bf16 v[252:255], a[192:193], v[174:175], v[252:255]\n"
"v_mfma_f32_16x16x16_bf16 v[248:251], a[192:193], v[172:173], v[248:251]\n"
"v_mfma_f32_16x16x16_bf16 v[244:247], a[192:193], v[170:171], v[244:247]\n"
"v_mfma_f32_16x16x16_bf16 v[240:243], a[192:193], v[168:169], v[240:243]\n"
"s_waitcnt vmcnt(0)\n"
"v_mfma_f32_16x16x16_bf16 v[236:239], a[192:193], v[182:183], v[236:239]\n"
"v_mfma_f32_16x16x16_bf16 v[252:255], a[192:193], v[190:191], v[252:255]\n"
"v_mfma_f32_16x16x16_bf16 v[232:235], a[192:193], v[180:181], v[232:235]\n"
"v_mfma_f32_16x16x16_bf16 v[248:251], a[192:193], v[188:189], v[248:251]\n"
"v_mfma_f32_16x16x16_bf16 v[228:231], a[192:193], v[178:179], v[228:231]\n"
"v_mfma_f32_16x16x16_bf16 v[244:247], a[192:193], v[186:187], v[244:247]\n"
"v_mfma_f32_16x16x16_bf16 v[224:227], a[192:193], v[176:177], v[224:227]\n"
"v_mfma_f32_16x16x16_bf16 v[240:243], a[192:193], v[184:185], v[240:243]\n"
"s_nop 1\n"
"ds_write_b128 v27, v[236:239] offset:0\n"
"ds_write_b128 v27, v[252:255] offset:64\n"
"v_cvt_pk_bf16_f32 v232, v232, v233\n"
"v_cvt_pk_bf16_f32 v233, v234, v235\n"
"v_cvt_pk_bf16_f32 v248, v248, v249\n"
"v_cvt_pk_bf16_f32 v249, v250, v251\n"
"ds_write_b64 v4, v[232:233] offset:2048\n"
"ds_write_b64 v25, v[248:249] offset:2048\n"
"v_cvt_pk_bf16_f32 v228, v228, v229\n"
"v_cvt_pk_bf16_f32 v229, v230, v231\n"
"v_cvt_pk_bf16_f32 v244, v244, v245\n"
"v_cvt_pk_bf16_f32 v245, v246, v247\n"
"ds_write_b64 v4, v[228:229] offset:1024\n"
"ds_write_b64 v25, v[244:245] offset:1024\n"
"v_cvt_pk_bf16_f32 v224, v224, v225\n"
"v_cvt_pk_bf16_f32 v225, v226, v227\n"
"v_cvt_pk_bf16_f32 v240, v240, v241\n"
"v_cvt_pk_bf16_f32 v241, v242, v243\n"
"ds_write_b64 v4, v[224:225] offset:0\n"
"ds_write_b64 v25, v[240:241] offset:0\n"
"v_lshl_add_u32 v10, s61, 30, v3\n"
"s_waitcnt lgkmcnt(6)\n"
"s_barrier\n"
"ds_read_b128 v[192:195], v28 offset:0\n"
"ds_read_b128 v[196:199], v28 offset:16\n"
"ds_read_b128 v[200:203], v9 offset:0\n"
"ds_read_b128 v[204:207], v9 offset:16\n"
"ds_read_b128 v[216:219], v5 offset:2048\n"
"ds_read_b128 v[212:215], v5 offset:1024\n"
"ds_read_b128 v[208:211], v5 offset:0\n"
"s_waitcnt lgkmcnt(3)\n"
"v_add_f32 v192, v192, v200\n"
"v_add_f32 v193, v193, v201\n"
"v_add_f32 v194, v194, v202\n"
"v_add_f32 v195, v195, v203\n"
"v_add_f32 v196, v196, v204\n"
"v_add_f32 v197, v197, v205\n"
"v_add_f32 v198, v198, v206\n"
"v_add_f32 v199, v199, v207\n"
"s_waitcnt lgkmcnt(0)\n"
"buffer_store_dwordx4 v[216:219], v3, s[40:43], s78 offen nt\n"
"v_cvt_pk_bf16_f32 v220, v192, v193\n"
"v_cvt_pk_bf16_f32 v221, v194, v195\n"
"v_cvt_pk_bf16_f32 v222, v196, v197\n"
"v_cvt_pk_bf16_f32 v223, v198, v199\n"
"buffer_store_dwordx4 v[212:215], v3, s[40:43], s76 offen nt\n"
"buffer_store_dwordx4 v[208:211], v3, s[40:43], s74 offen nt\n"
"buffer_store_dwordx4 v[220:223], v10, s[40:43], s80 offen nt\n"
"s_or_b32 s85, s20, s60\n"
"s_cbranch_scc1 NOSALT%=\n"
"s_mov_b32 s85, 2893824\n"
"buffer_store_dwordx2 v[30:31], v29, s[52:55], s85 offen sc1\n"
"NOSALT%=:\n"
"s_endpgm\n"
"TP%=:\n"
"s_buffer_load_dwordx8 s[44:51], s[52:55], s24 glc\n"
"s_add_u32 s12, s12, 1\n"
"s_waitcnt lgkmcnt(0)\n"
"s_cbranch_scc0 TC%=\n"
"s_branch TV%=\n"
"POLL0%=:\n"
"s_mov_b32 vcc_lo, -24\n"
"SP0_%=:\n"
"s_buffer_load_dwordx8 s[40:47], s[52:55], s95 offset:0 glc\n"
"s_add_u32 vcc_lo, vcc_lo, 1\n"
"s_waitcnt lgkmcnt(0)\n"
"s_cbranch_scc1 VP0_%=\n"
"s_cmp_lg_u64 s[40:41], s[88:89]\n"
"s_cbranch_scc1 SP0_%=\n"
"s_cmp_lg_u64 s[42:43], s[88:89]\n"
"s_cbranch_scc1 SP0_%=\n"
"s_cmp_lg_u64 s[44:45], s[88:89]\n"
"s_cbranch_scc1 SP0_%=\n"
"s_cmp_lg_u64 s[46:47], s[88:89]\n"
"s_cbranch_scc1 SP0_%=\n"
"s_branch BACK0%=\n"
"POLL1%=:\n"
"s_mov_b32 vcc_lo, -24\n"
"SP1_%=:\n"
"s_buffer_load_dwordx8 s[40:47], s[52:55], s95 offset:128 glc\n"
"s_add_u32 vcc_lo, vcc_lo, 1\n"
"s_waitcnt lgkmcnt(0)\n"
"s_cbranch_scc1 VP1_%=\n"
"s_cmp_lg_u64 s[40:41], s[88:89]\n"
"s_cbranch_scc1 SP1_%=\n"
"s_cmp_lg_u64 s[42:43], s[88:89]\n"
"s_cbranch_scc1 SP1_%=\n"
"s_cmp_lg_u64 s[44:45], s[88:89]\n"
"s_cbranch_scc1 SP1_%=\n"
"s_cmp_lg_u64 s[46:47], s[88:89]\n"
"s_cbranch_scc1 SP1_%=\n"
"s_branch BACK1%=\n"
"POLL2%=:\n"
"s_mov_b32 vcc_lo, -24\n"
"SP2_%=:\n"
"s_buffer_load_dwordx8 s[40:47], s[52:55], s95 offset:256 glc\n"
"s_add_u32 vcc_lo, vcc_lo, 1\n"
"s_waitcnt lgkmcnt(0)\n"
"s_cbranch_scc1 VP2_%=\n"
"s_cmp_lg_u64 s[40:41], s[88:89]\n"
"s_cbranch_scc1 SP2_%=\n"
"s_cmp_lg_u64 s[42:43], s[88:89]\n"
"s_cbranch_scc1 SP2_%=\n"
"s_cmp_lg_u64 s[44:45], s[88:89]\n"
"s_cbranch_scc1 SP2_%=\n"
"s_cmp_lg_u64 s[46:47], s[88:89]\n"
"s_cbranch_scc1 SP2_%=\n"
"s_branch BACK2%=\n"
"POLL3%=:\n"
"s_mov_b32 vcc_lo, -24\n"
"SP3_%=:\n"
"s_buffer_load_dwordx8 s[40:47], s[52:55], s95 offset:384 glc\n"
"s_add_u32 vcc_lo, vcc_lo, 1\n"
"s_waitcnt lgkmcnt(0)\n"
"s_cbranch_scc1 VP3_%=\n"
"s_cmp_lg_u64 s[40:41], s[88:89]\n"
"s_cbranch_scc1 SP3_%=\n"
"s_cmp_lg_u64 s[42:43], s[88:89]\n"
"s_cbranch_scc1 SP3_%=\n"
"s_cmp_lg_u64 s[44:45], s[88:89]\n"
"s_cbranch_scc1 SP3_%=\n"
"s_cmp_lg_u64 s[46:47], s[88:89]\n"
"s_cbranch_scc1 SP3_%=\n"
"s_branch BACK3%=\n"
"PC4_%=:\n"
"s_sub_u32 s0, s0, 2\n"
"s_cbranch_scc0 PC4_%=\n"
"s_branch PB4_%=\n"
"PC5_%=:\n"
"s_sub_u32 s0, s0, 2\n"
"s_cbranch_scc0 PC5_%=\n"
"s_branch PB5_%=\n"
"PC6_%=:\n"
"s_sub_u32 s0, s0, 2\n"
"s_cbranch_scc0 PC6_%=\n"
"s_branch PB6_%=\n"
"PC7_%=:\n"
"s_sub_u32 s0, s0, 2\n"
"s_cbranch_scc0 PC7_%=\n"
"s_branch PB7_%=\n"
"PC8_%=:\n"
"s_sub_u32 s0, s0, 2\n"
"s_cbranch_scc0 PC8_%=\n"
"s_branch PB8_%=\n"
"PC9_%=:\n"
"s_sub_u32 s0, s0, 2\n"
"s_cbranch_scc0 PC9_%=\n"
"s_branch PB9_%=\n"
"PC10_%=:\n"
"s_sub_u32 s0, s0, 2\n"
"s_cbranch_scc0 PC10_%=\n"
"s_branch PB10_%=\n"
"PC11_%=:\n"
"s_sub_u32 s0, s0, 2\n"
"s_cbranch_scc0 PC11_%=\n"
"s_branch PB11_%=\n"
"PC12_%=:\n"
"s_sub_u32 s0, s0, 2\n"
"s_cbranch_scc0 PC12_%=\n"
"s_branch PB12_%=\n"
"PC13_%=:\n"
"s_sub_u32 s0, s0, 2\n"
"s_cbranch_scc0 PC13_%=\n"
"s_branch PB13_%=\n"
"PC14_%=:\n"
"s_sub_u32 s0, s0, 2\n"
"s_cbranch_scc0 PC14_%=\n"
"s_branch PB14_%=\n"
"PC15_%=:\n"
"s_sub_u32 s0, s0, 2\n"
"s_cbranch_scc0 PC15_%=\n"
"s_branch PB15_%=\n"
"PC16_%=:\n"
"s_sub_u32 s0, s0, 2\n"
"s_cbranch_scc0 PC16_%=\n"
"s_branch PB16_%=\n"
"PC17_%=:\n"
"s_sub_u32 s0, s0, 2\n"
"s_cbranch_scc0 PC17_%=\n"
"s_branch PB17_%=\n"
"PC18_%=:\n"
"s_sub_u32 s0, s0, 2\n"
"s_cbranch_scc0 PC18_%=\n"
"s_branch PB18_%=\n"
"PC19_%=:\n"
"s_sub_u32 s0, s0, 2\n"
"s_cbranch_scc0 PC19_%=\n"
"s_branch PB19_%=\n"
"PC20_%=:\n"
"s_sub_u32 s0, s0, 2\n"
"s_cbranch_scc0 PC20_%=\n"
"s_branch PB20_%=\n"
"PC21_%=:\n"
"s_sub_u32 s0, s0, 2\n"
"s_cbranch_scc0 PC21_%=\n"
"s_branch PB21_%=\n"
"PC22_%=:\n"
"s_sub_u32 s0, s0, 2\n"
"s_cbranch_scc0 PC22_%=\n"
"s_branch PB22_%=\n"
"PC23_%=:\n"
"s_sub_u32 s0, s0, 2\n"
"s_cbranch_scc0 PC23_%=\n"
"s_branch PB23_%=\n"
"PC24_%=:\n"
"s_sub_u32 s0, s0, 2\n"
"s_cbranch_scc0 PC24_%=\n"
"s_branch PB24_%=\n"
"PC25_%=:\n"
"s_sub_u32 s0, s0, 2\n"
"s_cbranch_scc0 PC25_%=\n"
"s_branch PB25_%=\n"
"PC26_%=:\n"
"s_sub_u32 s0, s0, 2\n"
"s_cbranch_scc0 PC26_%=\n"
"s_branch PB26_%=\n"
"PC27_%=:\n"
"s_sub_u32 s0, s0, 2\n"
"s_cbranch_scc0 PC27_%=\n"
"s_branch PB27_%=\n"
"PC28_%=:\n"
"s_sub_u32 s0, s0, 2\n"
"s_cbranch_scc0 PC28_%=\n"
"s_branch PB28_%=\n"
"PC29_%=:\n"
"s_sub_u32 s0, s0, 2\n"
"s_cbranch_scc0 PC29_%=\n"
"s_branch PB29_%=\n"
"PC30_%=:\n"
"s_sub_u32 s0, s0, 2\n"
"s_cbranch_scc0 PC30_%=\n"
"s_branch PB30_%=\n"
"PC31_%=:\n"
"s_sub_u32 s0, s0, 2\n"
"s_cbranch_scc0 PC31_%=\n"
"s_branch PB31_%=\n"
"PC32_%=:\n"
"s_sub_u32 s0, s0, 2\n"
"s_cbranch_scc0 PC32_%=\n"
"s_branch PB32_%=\n"
"PC33_%=:\n"
"s_sub_u32 s0, s0, 2\n"
"s_cbranch_scc0 PC33_%=\n"
"s_branch PB33_%=\n"
"PC34_%=:\n"
"s_sub_u32 s0, s0, 2\n"
"s_cbranch_scc0 PC34_%=\n"
"s_branch PB34_%=\n"
"PC35_%=:\n"
"s_sub_u32 s0, s0, 2\n"
"s_cbranch_scc0 PC35_%=\n"
"s_branch PB35_%=\n"
"PC36_%=:\n"
"s_sub_u32 s0, s0, 2\n"
"s_cbranch_scc0 PC36_%=\n"
"s_branch PB36_%=\n"
"PC37_%=:\n"
"s_sub_u32 s0, s0, 2\n"
"s_cbranch_scc0 PC37_%=\n"
"s_branch PB37_%=\n"
"PC38_%=:\n"
"s_sub_u32 s0, s0, 2\n"
"s_cbranch_scc0 PC38_%=\n"
"s_branch PB38_%=\n"
"CODEEND%=:\n"
"VP0_%=:\n"
"buffer_load_dwordx2 v[6:7], v0, s[52:55], 0 offen offset:0 sc1\n"
"s_add_u32 s92, s92, 1\n"
"s_waitcnt vmcnt(0)\n"
"v_cmp_ne_u64 vcc, s[88:89], v[6:7]\n"
"s_cmp_gt_u32 s92, 2097152\n"
"s_cbranch_scc1 BACK0%=\n"
"s_cbranch_vccnz VP0_%=\n"
"s_branch BACK0%=\n"
"VP1_%=:\n"
"buffer_load_dwordx2 v[6:7], v0, s[52:55], 0 offen offset:128 sc1\n"
"s_add_u32 s92, s92, 1\n"
"s_waitcnt vmcnt(0)\n"
"v_cmp_ne_u64 vcc, s[88:89], v[6:7]\n"
"s_cmp_gt_u32 s92, 2097152\n"
"s_cbranch_scc1 BACK1%=\n"
"s_cbranch_vccnz VP1_%=\n"
"s_branch BACK1%=\n"
"VP2_%=:\n"
"buffer_load_dwordx2 v[6:7], v0, s[52:55], 0 offen offset:256 sc1\n"
"s_add_u32 s92, s92, 1\n"
"s_waitcnt vmcnt(0)\n"
"v_cmp_ne_u64 vcc, s[88:89], v[6:7]\n"
"s_cmp_gt_u32 s92, 2097152\n"
"s_cbranch_scc1 BACK2%=\n"
"s_cbranch_vccnz VP2_%=\n"
"s_branch BACK2%=\n"
"VP3_%=:\n"
"buffer_load_dwordx2 v[6:7], v0, s[52:55], 0 offen offset:384 sc1\n"
"s_add_u32 s92, s92, 1\n"
"s_waitcnt vmcnt(0)\n"
"v_cmp_ne_u64 vcc, s[88:89], v[6:7]\n"
"s_cmp_gt_u32 s92, 2097152\n"
"s_cbranch_scc1 BACK3%=\n"
"s_cbranch_vccnz VP3_%=\n"
"s_branch BACK3%=\n"
"TV%=:\n"
"buffer_load_dword v10, v0, s[52:55], s24 offen sc1\n"
"s_mov_b32 s12, -64\n"
"s_add_u32 s92, s92, 64\n"
"s_waitcnt vmcnt(0)\n"
"v_readlane_b32 s44, v10, 0\n"
"v_readlane_b32 s45, v10, 1\n"
"v_readlane_b32 s46, v10, 2\n"
"v_readlane_b32 s47, v10, 3\n"
"v_readlane_b32 s48, v10, 4\n"
"v_readlane_b32 s49, v10, 5\n"
"s_cmp_gt_u32 s92, 2097152\n"
"s_cbranch_scc1 TX%=\n"
"s_branch TC%=\n"
"FALLBACK%=:\n"
"s_waitcnt vmcnt(0) lgkmcnt(0)\n"
"s_nop 15\n"
"s_barrier\n"
"v_mov_b32 v32, 0xff800000\n"
"v_mov_b32 v36, 0xff800000\n"
"v_mov_b32 v40, 0xff800000\n"
"v_mov_b32 v44, 0xff800000\n"
"s_mov_b32 s81, s64\n"
"s_mov_b32 s85, 16\n"
"s_mov_b32 m0, s63\n"
"FBLOOP%=:\n"
"s_nop 0\n"
"buffer_load_dwordx4 v11, s[44:47], s81 offen offset:0 lds\n"
"buffer_load_dwordx4 v12, s[44:47], s81 offen offset:1024 lds\n"
"buffer_load_dwordx4 v13, s[44:47], s81 offen offset:2048 lds\n"
"buffer_load_dwordx4 v14, s[44:47], s81 offen offset:3072 lds\n"
"s_waitcnt vmcnt(0)\n"
"s_barrier\n"
"ds_read_b128 a[0:3], v15 offset:0\n"
"ds_read_b128 a[4:7], v16 offset:0\n"
"ds_read_b128 a[8:11], v17 offset:0\n"
"ds_read_b128 a[12:15], v18 offset:0\n"
"ds_read_b128 a[16:19], v15 offset:256\n"
"ds_read_b128 a[20:23], v16 offset:256\n"
"ds_read_b128 a[24:27], v17 offset:256\n"
"ds_read_b128 a[28:31], v18 offset:256\n"
"s_waitcnt lgkmcnt(0)\n"
"v_mfma_f32_16x16x32_bf16 v[48:51], a[0:3], a[32:35], 0\n"
"v_mfma_f32_16x16x32_bf16 v[48:51], a[4:7], a[36:39], v[48:51]\n"
"v_mfma_f32_16x16x32_bf16 v[48:51], a[8:11], a[40:43], v[48:51]\n"
"v_mfma_f32_16x16x32_bf16 v[48:51], a[12:15], a[44:47], v[48:51]\n"
"v_mfma_f32_16x16x32_bf16 v[48:51], a[16:19], a[48:51], v[48:51]\n"
"v_mfma_f32_16x16x32_bf16 v[48:51], a[20:23], a[52:55], v[48:51]\n"
"v_mfma_f32_16x16x32_bf16 v[48:51], a[24:27], a[56:59], v[48:51]\n"
"v_mfma_f32_16x16x32_bf16 v[48:51], a[28:31], a[60:63], v[48:51]\n"
"v_mfma_f32_16x16x32_bf16 v[52:55], a[0:3], a[64:67], 0\n"
"v_mfma_f32_16x16x32_bf16 v[52:55], a[4:7], a[68:71], v[52:55]\n"
"v_mfma_f32_16x16x32_bf16 v[52:55], a[8:11], a[72:75], v[52:55]\n"
"v_mfma_f32_16x16x32_bf16 v[52:55], a[12:15], a[76:79], v[52:55]\n"
"v_mfma_f32_16x16x32_bf16 v[52:55], a[16:19], a[80:83], v[52:55]\n"
"v_mfma_f32_16x16x32_bf16 v[52:55], a[20:23], a[84:87], v[52:55]\n"
"v_mfma_f32_16x16x32_bf16 v[52:55], a[24:27], a[88:91], v[52:55]\n"
"v_mfma_f32_16x16x32_bf16 v[52:55], a[28:31], a[92:95], v[52:55]\n"
"v_mfma_f32_16x16x32_bf16 v[56:59], a[0:3], a[96:99], 0\n"
"v_mfma_f32_16x16x32_bf16 v[56:59], a[4:7], a[100:103], v[56:59]\n"
"v_mfma_f32_16x16x32_bf16 v[56:59], a[8:11], a[104:107], v[56:59]\n"
"v_mfma_f32_16x16x32_bf16 v[56:59], a[12:15], a[108:111], v[56:59]\n"
"v_mfma_f32_16x16x32_bf16 v[56:59], a[16:19], a[112:115], v[56:59]\n"
"v_mfma_f32_16x16x32_bf16 v[56:59], a[20:23], a[116:119], v[56:59]\n"
"v_mfma_f32_16x16x32_bf16 v[56:59], a[24:27], a[120:123], v[56:59]\n"
"v_mfma_f32_16x16x32_bf16 v[56:59], a[28:31], a[124:127], v[56:59]\n"
"v_mfma_f32_16x16x32_bf16 v[60:63], a[0:3], a[128:131], 0\n"
"v_mfma_f32_16x16x32_bf16 v[60:63], a[4:7], a[132:135], v[60:63]\n"
"v_mfma_f32_16x16x32_bf16 v[60:63], a[8:11], a[136:139], v[60:63]\n"
"v_mfma_f32_16x16x32_bf16 v[60:63], a[12:15], a[140:143], v[60:63]\n"
"v_mfma_f32_16x16x32_bf16 v[60:63], a[16:19], a[144:147], v[60:63]\n"
"v_mfma_f32_16x16x32_bf16 v[60:63], a[20:23], a[148:151], v[60:63]\n"
"v_mfma_f32_16x16x32_bf16 v[60:63], a[24:27], a[152:155], v[60:63]\n"
"v_mfma_f32_16x16x32_bf16 v[60:63], a[28:31], a[156:159], v[60:63]\n"
"s_nop 15\n"
"v_max3_f32 v32, v32, v48, v49\n"
"v_max3_f32 v32, v32, v50, v51\n"
"v_max3_f32 v36, v36, v52, v53\n"
"v_max3_f32 v36, v36, v54, v55\n"
"v_max3_f32 v40, v40, v56, v57\n"
"v_max3_f32 v40, v40, v58, v59\n"
"v_max3_f32 v44, v44, v60, v61\n"
"v_max3_f32 v44, v44, v62, v63\n"
"ds_read_b128 a[0:3], v19 offset:0\n"
"ds_read_b128 a[4:7], v20 offset:0\n"
"ds_read_b128 a[8:11], v21 offset:0\n"
"ds_read_b128 a[12:15], v22 offset:0\n"
"ds_read_b128 a[16:19], v19 offset:256\n"
"ds_read_b128 a[20:23], v20 offset:256\n"
"ds_read_b128 a[24:27], v21 offset:256\n"
"ds_read_b128 a[28:31], v22 offset:256\n"
"s_waitcnt lgkmcnt(0)\n"
"v_mfma_f32_16x16x32_bf16 v[48:51], a[0:3], a[32:35], 0\n"
"v_mfma_f32_16x16x32_bf16 v[48:51], a[4:7], a[36:39], v[48:51]\n"
"v_mfma_f32_16x16x32_bf16 v[48:51], a[8:11], a[40:43], v[48:51]\n"
"v_mfma_f32_16x16x32_bf16 v[48:51], a[12:15], a[44:47], v[48:51]\n"
"v_mfma_f32_16x16x32_bf16 v[48:51], a[16:19], a[48:51], v[48:51]\n"
"v_mfma_f32_16x16x32_bf16 v[48:51], a[20:23], a[52:55], v[48:51]\n"
"v_mfma_f32_16x16x32_bf16 v[48:51], a[24:27], a[56:59], v[48:51]\n"
"v_mfma_f32_16x16x32_bf16 v[48:51], a[28:31], a[60:63], v[48:51]\n"
"v_mfma_f32_16x16x32_bf16 v[52:55], a[0:3], a[64:67], 0\n"
"v_mfma_f32_16x16x32_bf16 v[52:55], a[4:7], a[68:71], v[52:55]\n"
"v_mfma_f32_16x16x32_bf16 v[52:55], a[8:11], a[72:75], v[52:55]\n"
"v_mfma_f32_16x16x32_bf16 v[52:55], a[12:15], a[76:79], v[52:55]\n"
"v_mfma_f32_16x16x32_bf16 v[52:55], a[16:19], a[80:83], v[52:55]\n"
"v_mfma_f32_16x16x32_bf16 v[52:55], a[20:23], a[84:87], v[52:55]\n"
"v_mfma_f32_16x16x32_bf16 v[52:55], a[24:27], a[88:91], v[52:55]\n"
"v_mfma_f32_16x16x32_bf16 v[52:55], a[28:31], a[92:95], v[52:55]\n"
"v_mfma_f32_16x16x32_bf16 v[56:59], a[0:3], a[96:99], 0\n"
"v_mfma_f32_16x16x32_bf16 v[56:59], a[4:7], a[100:103], v[56:59]\n"
"v_mfma_f32_16x16x32_bf16 v[56:59], a[8:11], a[104:107], v[56:59]\n"
"v_mfma_f32_16x16x32_bf16 v[56:59], a[12:15], a[108:111], v[56:59]\n"
"v_mfma_f32_16x16x32_bf16 v[56:59], a[16:19], a[112:115], v[56:59]\n"
"v_mfma_f32_16x16x32_bf16 v[56:59], a[20:23], a[116:119], v[56:59]\n"
"v_mfma_f32_16x16x32_bf16 v[56:59], a[24:27], a[120:123], v[56:59]\n"
"v_mfma_f32_16x16x32_bf16 v[56:59], a[28:31], a[124:127], v[56:59]\n"
"v_mfma_f32_16x16x32_bf16 v[60:63], a[0:3], a[128:131], 0\n"
"v_mfma_f32_16x16x32_bf16 v[60:63], a[4:7], a[132:135], v[60:63]\n"
"v_mfma_f32_16x16x32_bf16 v[60:63], a[8:11], a[136:139], v[60:63]\n"
"v_mfma_f32_16x16x32_bf16 v[60:63], a[12:15], a[140:143], v[60:63]\n"
"v_mfma_f32_16x16x32_bf16 v[60:63], a[16:19], a[144:147], v[60:63]\n"
"v_mfma_f32_16x16x32_bf16 v[60:63], a[20:23], a[148:151], v[60:63]\n"
"v_mfma_f32_16x16x32_bf16 v[60:63], a[24:27], a[152:155], v[60:63]\n"
"v_mfma_f32_16x16x32_bf16 v[60:63], a[28:31], a[156:159], v[60:63]\n"
"s_nop 15\n"
"v_max3_f32 v32, v32, v48, v49\n"
"v_max3_f32 v32, v32, v50, v51\n"
"v_max3_f32 v36, v36, v52, v53\n"
"v_max3_f32 v36, v36, v54, v55\n"
"v_max3_f32 v40, v40, v56, v57\n"
"v_max3_f32 v40, v40, v58, v59\n"
"v_max3_f32 v44, v44, v60, v61\n"
"v_max3_f32 v44, v44, v62, v63\n"
"s_barrier\n"
"s_add_u32 s81, s81, 16384\n"
"s_sub_u32 s85, s85, 1\n"
"s_cmp_lg_u32 s85, 0\n"
"s_cbranch_scc1 FBLOOP%=\n"
"v_mov_b32 v33, v32\n"
"s_nop 1\n"
"v_permlane32_swap_b32 v32, v33\n"
"s_nop 1\n"
"v_max_f32 v32, v32, v33\n"
"v_mov_b32 v33, v32\n"
"s_nop 1\n"
"v_permlane16_swap_b32 v32, v33\n"
"s_nop 1\n"
"v_max_f32 v32, v32, v33\n"
"v_sub_f32 v32, 0, v32\n"
"v_mov_b32 v33, v32\n"
"v_mov_b32 v34, v32\n"
"v_mov_b32 v35, v32\n"
"ds_write_b128 v10, v[32:35] offset:0\n"
"v_mov_b32 v37, v36\n"
"s_nop 1\n"
"v_permlane32_swap_b32 v36, v37\n"
"s_nop 1\n"
"v_max_f32 v36, v36, v37\n"
"v_mov_b32 v37, v36\n"
"s_nop 1\n"
"v_permlane16_swap_b32 v36, v37\n"
"s_nop 1\n"
"v_max_f32 v36, v36, v37\n"
"v_sub_f32 v36, 0, v36\n"
"v_mov_b32 v37, v36\n"
"v_mov_b32 v38, v36\n"
"v_mov_b32 v39, v36\n"
"ds_write_b128 v10, v[36:39] offset:256\n"
"v_mov_b32 v41, v40\n"
"s_nop 1\n"
"v_permlane32_swap_b32 v40, v41\n"
"s_nop 1\n"
"v_max_f32 v40, v40, v41\n"
"v_mov_b32 v41, v40\n"
"s_nop 1\n"
"v_permlane16_swap_b32 v40, v41\n"
"s_nop 1\n"
"v_max_f32 v40, v40, v41\n"
"v_sub_f32 v40, 0, v40\n"
"v_mov_b32 v41, v40\n"
"v_mov_b32 v42, v40\n"
"v_mov_b32 v43, v40\n"
"ds_write_b128 v10, v[40:43] offset:512\n"
"v_mov_b32 v45, v44\n"
"s_nop 1\n"
"v_permlane32_swap_b32 v44, v45\n"
"s_nop 1\n"
"v_max_f32 v44, v44, v45\n"
"v_mov_b32 v45, v44\n"
"s_nop 1\n"
"v_permlane16_swap_b32 v44, v45\n"
"s_nop 1\n"
"v_max_f32 v44, v44, v45\n"
"v_sub_f32 v44, 0, v44\n"
"v_mov_b32 v45, v44\n"
"v_mov_b32 v46, v44\n"
"v_mov_b32 v47, v44\n"
"ds_write_b128 v10, v[44:47] offset:768\n"
"s_waitcnt lgkmcnt(0)\n"
"s_barrier\n"
"s_lshl_b32 s85, s61, 10\n"
"v_subrev_u32 v30, s85, v10\n"
"ds_read_b128 v[44:47], v30 offset:768\n"
"s_waitcnt lgkmcnt(0)\n"
"ds_write_b128 v10, v[44:47] offset:768\n"
"s_waitcnt lgkmcnt(0)\n"
"s_mov_b32 s86, 0\n"
"s_mov_b32 s87, 0x7f800000\n"
"s_barrier\n"
"s_branch RESTART%=\n"
  : : "s"((unsigned long long)__builtin_amdgcn_kernarg_segment_ptr()), "s"(blockIdx.x), "v"(threadIdx.x), "s"((unsigned)(size_t)lds) : "v1","v2","v3","v4","v5","v6","v7","v8","v9","v10","v11","v12","v13","v14","v15","v16","v17","v18","v19","v20","v21","v22","v23","v24","v25","v26","v27","v28","v29","v30","v31","v32","v33","v34","v35","v36","v37","v38","v39","v40","v41","v42","v43","v44","v45","v46","v47","v48","v49","v50","v51","v52","v53","v54","v55","v56","v57","v58","v59","v60","v61","v62","v63","v64","v65","v66","v67","v68","v69","v70","v71","v72","v73","v74","v75","v76","v77","v78","v79","v80","v81","v82","v83","v84","v85","v86","v87","v88","v89","v90","v91","v92","v93","v94","v95","v96","v97","v98","v99","v100","v101","v102","v103","v104","v105","v106","v107","v108","v109","v110","v111","v112","v113","v114","v115","v116","v117","v118","v119","v120","v121","v122","v123","v124","v125","v126","v127","v128","v129","v130","v131","v132","v133","v134","v135","v136","v137","v138","v139","v140","v141","v142","v143","v144","v145","v146","v147","v148","v149","v150","v151","v152","v153","v154","v155","v156","v157","v158","v159","v160","v161","v162","v163","v164","v165","v166","v167","v168","v169","v170","v171","v172","v173","v174","v175","v176","v177","v178","v179","v180","v181","v182","v183","v184","v185","v186","v187","v188","v189","v190","v191","v192","v193","v194","v195","v196","v197","v198","v199","v200","v201","v202","v203","v204","v205","v206","v207","v208","v209","v210","v211","v212","v213","v214","v215","v216","v217","v218","v219","v220","v221","v222","v223","v224","v225","v226","v227","v228","v229","v230","v231","v232","v233","v234","v235","v236","v237","v238","v239","v240","v241","v242","v243","v244","v245","v246","v247","v248","v249","v250","v251","v252","v253","v254","v255","a0","a1","a2","a3","a4","a5","a6","a7","a8","a9","a10","a11","a12","a13","a14","a15","a16","a17","a18","a19","a20","a21","a22","a23","a24","a25","a26","a27","a28","a29","a30","a31","a32","a33","a34","a35","a36","a37","a38","a39","a40","a41","a42","a43","a44","a45","a46","a47","a48","a49","a50","a51","a52","a53","a54","a55","a56","a57","a58","a59","a60","a61","a62","a63","a64","a65","a66","a67","a68","a69","a70","a71","a72","a73","a74","a75","a76","a77","a78","a79","a80","a81","a82","a83","a84","a85","a86","a87","a88","a89","a90","a91","a92","a93","a94","a95","a96","a97","a98","a99","a100","a101","a102","a103","a104","a105","a106","a107","a108","a109","a110","a111","a112","a113","a114","a115","a116","a117","a118","a119","a120","a121","a122","a123","a124","a125","a126","a127","a128","a129","a130","a131","a132","a133","a134","a135","a136","a137","a138","a139","a140","a141","a142","a143","a144","a145","a146","a147","a148","a149","a150","a151","a152","a153","a154","a155","a156","a157","a158","a159","a160","a161","a162","a163","a164","a165","a166","a167","a168","a169","a170","a171","a172","a173","a174","a175","a176","a177","a178","a179","a180","a181","a182","a183","a184","a185","a186","a187","a188","a189","a190","a191","a192","a193","a194","a195","a196","a197","a198","a199","a200","a201","a202","a203","a204","a205","a206","a207","a208","a209","a210","a211","a212","a213","a214","a215","a216","a217","a218","a219","a220","a221","a222","a223","a224","a225","a226","a227","a228","a229","a230","a231","a232","a233","a234","a235","a236","a237","a238","a239","a240","a241","a242","a243","a244","a245","a246","a247","a248","a249","a250","a251","a252","a253","a254","a255","s4","s5","s6","s7","s8","s9","s10","s11","s12","s13","s14","s15","s16","s17","s18","s19","s20","s21","s22","s23","s24","s25","s26","s27","s28","s29","s30","s31","s34","s35","s36","s37","s38","s39","s40","s41","s42","s43","s44","s45","s46","s47","s48","s49","s50","s51","s52","s53","s54","s55","s56","s57","s58","s59","s60","s61","s62","s63","s64","s65","s66","s67","s68","s69","s70","s71","s72","s73","s74","s75","s76","s77","s78","s79","s80","s81","s82","s83","s84","s85","s86","s87","s88","s89","s90","s91","s92","s93","s94","s95","s96","s97","s98","s99","vcc","scc","memory");
}
extern "C" void kernel_launch(void* const* d_in, const int* in_sizes, int n_in, void* d_out, int out_size, void* d_ws, size_t ws_size, hipStream_t stream) {
  Ptrs p;
  for (int i = 0; i < 2; i++) {
    for (int j = 0; j < 5; j++) p.p[i][j] = d_in[5 * i + j];
    p.p[i][5] = d_out; p.p[i][6] = d_ws; p.p[i][7] = d_ws;
  }
  fused_attn<<<256, 256, 0, stream>>>(p);
}
```
